```python
import torch

def _softmax(x, axis=-1):
    return torch.softmax(x, dim=axis)
def _gelu(x):
    return torch.nn.functional.gelu(x, approximate="tanh")
def _silu(x):
    return torch.nn.functional.silu(x)
def _sigmoid(x):
    return torch.sigmoid(x)
def _layernorm(x, axis=-1, eps=1e-5):
    m = x.mean(dim=axis, keepdim=True)
    v = x.var(dim=axis, keepdim=True, unbiased=False)
    return (x - m) / torch.sqrt(v + eps)
def _scatter_add(tgt, idx, src, axis=0):
    return tgt.clone().index_add_(axis, idx, src)
def _segment_sum(src, seg, n):
    out = torch.zeros((n,) + src.shape[1:], dtype=src.dtype)
    return out.index_add_(0, seg, src)
def _pad(x, pw):
    flat = tuple(v for pair in reversed(pw) for v in pair)
    return torch.nn.functional.pad(x, flat)

kernel_name = 'gated_mix__ssm_conv_s4d__dense_activation_mish__prompt'

def setup_inputs():

    _w = (lambda a: (lambda t: t.to(torch.bfloat16) if t.is_floating_point() else t)(torch.as_tensor(a)))
    def _A_setup():
        Bsz, L, D, N = 1, 2048, 576, 128
        return {
            "u": _w((lambda *s: torch.randn(*s, dtype=torch.bfloat16) * 0.05)(Bsz, L, D)),
            "log_A_real": _w((lambda *s: torch.randn(*s, dtype=torch.bfloat16) * 6.4)(D, N) * 0.5),
            "A_imag": _w((lambda *s: torch.randn(*s, dtype=torch.bfloat16) * 0.05)(D, N) * 3.14),
            "C_real": _w((lambda *s: torch.randn(*s, dtype=torch.bfloat16) * 0.1)(D, N) / N ** 0.5),
            "C_imag": _w((lambda *s: torch.randn(*s, dtype=torch.bfloat16) * 0.1)(D, N) / N ** 0.5),
            "log_dt": _w((lambda *s: torch.randn(*s, dtype=torch.bfloat16) * 0.05)(D) * 0.5 - 4.0),
            "D_skip": _w((lambda *s: torch.randn(*s, dtype=torch.bfloat16) * 0.05)(D)),
        }
    _A_in = _A_setup()

    def _B_setup():
        B, S, D, F = 1, 2048, 576, 4608
        return {
            "x": _w((lambda *s: torch.randn(*s, dtype=torch.bfloat16) * 0.05)(B, S, D)),
            "w1": _w((lambda *s: torch.randn(*s, dtype=torch.bfloat16) * 6.4)(D, F) * (D ** -0.5)),
            "b1": _w((lambda *s: torch.randn(*s, dtype=torch.bfloat16) * 0.05)(F)),
            "w2": _w((lambda *s: torch.randn(*s, dtype=torch.bfloat16) * 0.05)(F, D) * (F ** -0.5)),
        }
    _B_in = _B_setup()

    return {
        "A_u": _A_in["u"],
        "A_log_A_real": _A_in["log_A_real"],
        "A_A_imag": _A_in["A_imag"],
        "A_C_real": _A_in["C_real"],
        "A_C_imag": _A_in["C_imag"],
        "A_log_dt": _A_in["log_dt"],
        "A_D_skip": _A_in["D_skip"],
        "B_x": _B_in["x"],
        "B_w1": _B_in["w1"],
        "B_b1": _B_in["b1"],
        "B_w2": _B_in["w2"],
        "gate_w": _w((lambda *s: torch.randn(*s, dtype=torch.bfloat16) * 0.05)(576) * 16.0),
    }

def reference(A_u, A_log_A_real, A_A_imag, A_C_real, A_C_imag, A_log_dt, A_D_skip, B_x, B_w1, B_b1, B_w2, gate_w):
    def _A_ref(u, log_A_real, A_imag, C_real, C_imag, log_dt, D_skip):
        L = u.shape[1]
        dt = torch.exp(log_dt)
        A = -torch.exp(log_A_real) + 1j * A_imag
        C = (C_real + 1j * C_imag) * dt[:, None]
        pos = (lambda x, dt: x.to(dt))(torch.arange(L), log_dt.dtype)
        P = torch.exp((A * dt[:, None])[:, :, None] * pos[None, None, :])
        K = 2.0 * torch.einsum("dn,dnl->dl", C, P).real
        L2 = 2 * L
        u_t = (lambda x, *d: x.permute(*d))(u, 0, 2, 1)
        Kf = (lambda x, *a, **k: torch.fft.rfft(x.float(), *a, **k))(K, L2)
        Uf = (lambda x, *a, **k: torch.fft.rfft(x.float(), *a, **k))(u_t, L2)
        y = (lambda x, *a, **k: torch.fft.irfft(x, *a, **k).to(torch.bfloat16))(Uf * Kf[None, :, :], L2)[:, :, :L]
        y = (lambda x, *d: x.permute(*d))(y, 0, 2, 1)
        return y + u * D_skip[None, None, :]

    def _B_ref(x, w1, b1, w2):
        h = torch.einsum("bsd,df->bsf", x, w1) + b1
        sp = torch.log1p(torch.exp(-torch.abs(h))) + torch.where(h > 0, h, torch.zeros_like(h))
        act = h * torch.tanh(sp)
        return torch.einsum("bsf,fd->bsd", act, w2)

    x = A_u
    g = _sigmoid((x * gate_w).sum(-1, keepdims=True))
    a = _A_ref(x, A_log_A_real, A_A_imag, A_C_real, A_C_imag, A_log_dt, A_D_skip)
    b = _B_ref(x, B_w1, B_b1, B_w2)
    return g * a + (1 - g) * b
```

```cpp
#include <hip/hip_runtime.h>
#include <hip/hip_bf16.h>
#include <stdint.h>

typedef unsigned short u16;
__device__ __forceinline__ float bf2f(u16 v){ return __uint_as_float(((unsigned)v)<<16); }
__device__ __forceinline__ u16 f2bf(float f){ __bf16 b=(__bf16)f; return __builtin_bit_cast(u16,b); }
__device__ __forceinline__ float rbf(float f){ return bf2f(f2bf(f)); }

constexpr int L=2048, D=576, N=128, F=4608;

__global__ void kgen(const u16* logA,const u16* Aim,const u16* Cre,const u16* Cim,const u16* logdt,float* K){
  int d=blockIdx.x;
  __shared__ float ar[N],ai[N],cr[N],ci[N];
  float dt=rbf(expf(bf2f(logdt[d])));
  for(int n=threadIdx.x;n<N;n+=blockDim.x){
    float a=-rbf(expf(bf2f(logA[d*N+n])));
    ar[n]=a*dt; ai[n]=bf2f(Aim[d*N+n])*dt; cr[n]=bf2f(Cre[d*N+n])*dt; ci[n]=bf2f(Cim[d*N+n])*dt;
  }
  __syncthreads();
  for(int l=threadIdx.x;l<L;l+=blockDim.x){
    float p=rbf((float)l); float s=0;
    for(int n=0;n<N;n++){ float e=expf(ar[n]*p); float sn,cs; sincosf(ai[n]*p,&sn,&cs); s+=e*(cr[n]*cs-ci[n]*sn);}
    K[d*L+l]=2.f*s;
  }
}
__global__ void conv(const u16* u,const float* K,const u16* Dsk,float* a){
  int d=blockIdx.x; __shared__ float us[L],ks[L];
  for(int t=threadIdx.x;t<L;t+=blockDim.x){us[t]=bf2f(u[t*D+d]); ks[t]=K[d*L+t];}
  __syncthreads();
  for(int t=threadIdx.x;t<L;t+=blockDim.x){ float s=0; for(int l=0;l<=t;l++) s+=ks[l]*us[t-l];
    a[t*D+d]=rbf(rbf(s)+rbf(us[t]*bf2f(Dsk[d]))); }
}
__global__ void g1(const u16* x,const u16* w1,const u16* b1,u16* act){
  int t=blockIdx.y; int f=blockIdx.x*256+threadIdx.x; __shared__ float xs[D];
  for(int i=threadIdx.x;i<D;i+=256) xs[i]=bf2f(x[t*D+i]);
  __syncthreads();
  float s=0; for(int i=0;i<D;i++) s+=xs[i]*bf2f(w1[i*F+f]);
  float h=s+bf2f(b1[f]);
  float e=__expf(h); float dd=1.f+e+0.5f*e*e; float m=h-h/dd;
  act[t*F+f]=f2bf(m);
}
__global__ void g2(const u16* x,const u16* act,const u16* w2,const float* a,const u16* gw,u16* out){
  int t=blockIdx.x; int d=threadIdx.x; __shared__ float as[F]; __shared__ float red[D];
  for(int i=threadIdx.x;i<F;i+=D) as[i]=bf2f(act[t*F+i]);
  red[d]=bf2f(x[t*D+d])*bf2f(gw[d]);
  __syncthreads();
  float z=0; for(int i=0;i<D;i++) z+=red[i];
  float g=1.f/(1.f+__expf(-z));
  float s=0; for(int i=0;i<F;i++) s+=as[i]*bf2f(w2[i*D+d]);
  out[t*D+d]=f2bf(g*a[t*D+d]+(1.f-g)*s);
}
extern "C" void kernel_launch(void* const* d_in, const int* in_sizes, int n_in, void* d_out, int out_size, void* d_ws, size_t ws_size, hipStream_t stream){
  const u16* u=(const u16*)d_in[0];
  float* K=(float*)d_ws; float* a=K+D*L; u16* act=(u16*)(a+L*D);
  kgen<<<D,256,0,stream>>>((const u16*)d_in[1],(const u16*)d_in[2],(const u16*)d_in[3],(const u16*)d_in[4],(const u16*)d_in[5],K);
  conv<<<D,256,0,stream>>>(u,K,(const u16*)d_in[6],a);
  g1<<<dim3(F/256,L),256,0,stream>>>(u,(const u16*)d_in[8],(const u16*)d_in[9],act);
  g2<<<L,D,0,stream>>>(u,act,(const u16*)d_in[10],a,(const u16*)d_in[11],(u16*)d_out);
}
```

```cpp
#include <hip/hip_runtime.h>
#include <stdint.h>

typedef unsigned short u16;
typedef unsigned u32;
typedef u32 u32x2 __attribute__((ext_vector_type(2)));
typedef u32 u32x4 __attribute__((ext_vector_type(4)));
typedef u32 u32x8 __attribute__((ext_vector_type(8)));
typedef u32 u32x16 __attribute__((ext_vector_type(16)));
typedef u32 u32x32 __attribute__((ext_vector_type(32)));
typedef float f32x2 __attribute__((ext_vector_type(2)));
typedef float f32x16 __attribute__((ext_vector_type(16)));
typedef __bf16 bf16x2 __attribute__((ext_vector_type(2)));
typedef __bf16 bf16x8 __attribute__((ext_vector_type(8)));
typedef short s16x4 __attribute__((ext_vector_type(4)));
#define DI __device__ __forceinline__
#define LAS __attribute__((address_space(3)))

constexpr u32 WS_FLG = 0;
constexpr u32 WS_UT = 1u << 20, WS_AT = 11u << 19, ROW = 4224, WS_ACT = 8u << 20, WS_PART = 28u << 20, WS_END = 52u << 20;
constexpr u32 OOB = 0x7ffffff0u, LOOB = 0x30000u;
constexpr u32 XR = 61440, XSLOT = 32768, SSM = 126976, SSMCH = 12288, KC1 = 4160;

DI u32 rfl(u32 x) { return __builtin_amdgcn_readfirstlane(x); }
DI float bflo(u32 v) { return __uint_as_float(v << 16); }
DI float bfhi(u32 v) { return __uint_as_float(v & 0xffff0000u); }
DI u32 pk(float lo, float hi) { f32x2 v = {lo, hi}; return __builtin_bit_cast(u32, __builtin_convertvector(v, bf16x2)); }
DI float rbf(float x) { return bflo(pk(x, x)); }

__global__ __launch_bounds__(256, 1) void k1(const u16* x, const u16* logA, const u16* Aim, const u16* Cre, const u16* Cim, const u16* logdt,
                                             const u16* Dsk, const u16* w1, const u16* b1, const u16* w2, char* ws) {
  __shared__ __attribute__((aligned(16))) char lds[163840];
  u32 tid = threadIdx.x;
  asm volatile(
"s_load_dwordx16 s[4:19], %2, 0\n\t"
"s_load_dwordx4 s[20:23], %2, 64\n\t"
"s_load_dwordx2 s[24:25], %2, 80\n\t"
"v_readfirstlane_b32 s29, v0\n\t"
"v_and_b32 v0, 63, v0\n\t"
"s_and_b32 s26, %1, 7\n\t"
"s_bfe_u32 s27, %1, 0x20003\n\t"
"s_lshr_b32 s28, %1, 5\n\t"
"s_lshr_b32 s29, s29, 6\n\t"
"v_lshrrev_b32 v228, 3, v0\n\t"
"v_and_b32 v229, 7, v0\n\t"
"v_lshrrev_b32 v224, 4, v0\n\t"
"s_add_u32 s30, s28, s27\n\t"
"s_and_b32 s30, s30, 1\n\t"
"s_add_u32 s50, s26, s30\n\t"
"s_sub_u32 s51, s26, s30\n\t"
"v_xor_b32 v240, v229, v224\n\t"
"v_mul_u32_u24 v241, 1152, v228\n\t"
"v_lshl_or_b32 v2, v240, 4, v241\n\t"
"s_lshl_b32 s61, s28, 2\n\t"
"s_add_u32 s61, s61, s29\n\t"
"s_mul_i32 s48, s61, 73728\n\t"
"v_xor_b32 v3, 64, v2\n\t"
"v_add_u32 v3, 9216, v3\n\t"
"v_add_u32 v4, 18432, v2\n\t"
"v_add_u32 v5, 18432, v3\n\t"
"v_add_u32 v6, 36864, v2\n\t"
"v_add_u32 v7, 36864, v3\n\t"
"v_add_u32 v8, 55296, v2\n\t"
"v_add_u32 v9, 55296, v3\n\t"
"s_mul_i32 s61, s29, 147456\n\t"
"s_mul_i32 s62, s26, 1152\n\t"
"s_lshl_b32 s63, s27, 8\n\t"
"s_add_u32 s61, s61, s62\n\t"
"s_add_u32 s49, s61, s63\n\t"
"v_lshlrev_b32 v240, 4, v229\n\t"
"v_lshlrev_b32 v242, 5, v224\n\t"
"v_xor_b32 v242, v240, v242\n\t"
"s_mov_b32 s64, 9216\n\t"
"v_mad_u32_u24 v12, v228, s64, v242\n\t"
"v_xor_b32 v13, 64, v12\n\t"
"v_add_u32 v13, 73728, v13\n\t"
"s_lshr_b32 s61, s27, 1\n\t"
"s_lshl_b32 s61, s61, 6\n\t"
"s_sub_u32 s61, s61, s63\n\t"
"s_add_u32 s60, s61, 1024\n\t"
"v_and_b32 v226, 3, v0\n\t"
"v_lshrrev_b32 v230, 2, v0\n\t"
"v_and_b32 v243, 32, v0\n\t"
"v_lshlrev_b32 v244, 4, v226\n\t"
"v_xor_b32 v243, v243, v244\n\t"
"v_mad_u32_u24 v14, v230, s64, v243\n\t"
"v_add_u32 v14, s60, v14\n\t"
"s_lshl_b32 s86, s29, 13\n\t"
"s_add_u32 s86, s86, 61440\n\t"
"s_mul_i32 s87, s29, 5120\n\t"
"s_add_u32 s61, s50, 0\n\t"
"s_sub_u32 s62, s61, 9\n\t"
"s_min_u32 s61, s61, s62\n\t"
"s_lshl_b32 s62, s61, 7\n\t"
"s_add_u32 s52, s48, s62\n\t"
"s_mul_i32 s61, s61, 589824\n\t"
"s_add_u32 s54, s49, s61\n\t"
"s_mov_b32 s34, 2359296\n\t"
"s_mov_b32 s35, 0x20000\n\t"
"s_mov_b32 s38, 5308416\n\t"
"s_mov_b32 s39, 0x20000\n\t"
"s_waitcnt lgkmcnt(0)\n\t"
"s_mov_b32 s32, s4\n\t"
"s_and_b32 s33, s5, 0xffff\n\t"
"s_mov_b32 s36, s18\n\t"
"s_and_b32 s37, s19, 0xffff\n\t"
"s_add_u32 m0, s86, 0\n\t"
"s_nop 0\n\t"
"buffer_load_dwordx4 v2, s[32:35], s52 offen lds\n\t"
"s_add_u32 m0, s86, 1024\n\t"
"s_nop 0\n\t"
"buffer_load_dwordx4 v3, s[32:35], s52 offen lds\n\t"
"s_add_u32 m0, s86, 2048\n\t"
"s_nop 0\n\t"
"buffer_load_dwordx4 v4, s[32:35], s52 offen lds\n\t"
"s_add_u32 m0, s86, 3072\n\t"
"s_nop 0\n\t"
"buffer_load_dwordx4 v5, s[32:35], s52 offen lds\n\t"
"s_add_u32 m0, s86, 4096\n\t"
"s_nop 0\n\t"
"buffer_load_dwordx4 v6, s[32:35], s52 offen lds\n\t"
"s_add_u32 m0, s86, 5120\n\t"
"s_nop 0\n\t"
"buffer_load_dwordx4 v7, s[32:35], s52 offen lds\n\t"
"s_add_u32 m0, s86, 6144\n\t"
"s_nop 0\n\t"
"buffer_load_dwordx4 v8, s[32:35], s52 offen lds\n\t"
"s_add_u32 m0, s86, 7168\n\t"
"s_nop 0\n\t"
"buffer_load_dwordx4 v9, s[32:35], s52 offen lds\n\t"
"s_add_u32 m0, s87, 0\n\t"
"s_nop 0\n\t"
"buffer_load_dwordx4 v12, s[36:39], s54 offen offset:0 lds\n\t"
"s_add_u32 m0, s87, 1024\n\t"
"s_nop 0\n\t"
"buffer_load_dwordx4 v13, s[36:39], s54 offen offset:0 lds\n\t"
"s_add_u32 m0, s87, 1920\n\t"
"s_nop 0\n\t"
"buffer_load_dwordx4 v12, s[36:39], s54 offen offset:128 lds\n\t"
"s_add_u32 m0, s87, 2944\n\t"
"s_nop 0\n\t"
"buffer_load_dwordx4 v13, s[36:39], s54 offen offset:128 lds\n\t"
"s_add_u32 m0, s87, 4096\n\t"
"s_nop 0\n\t"
"buffer_load_dwordx4 v14, s[36:39], s54 offen lds\n\t"
"s_mul_i32 s61, s26, 1152\n\t"
"s_lshl_b32 s62, s27, 8\n\t"
"s_add_u32 s61, s61, s62\n\t"
"v_lshl_add_u32 v245, v224, 3, s61\n\t"
"s_mul_i32 s62, s27, 224\n\t"
"s_sub_u32 s62, 1024, s62\n\t"
"v_add_u32 v246, s62, v245\n\t"
"global_load_dwordx2 v[52:53], v245, s[20:21] offset:0\n\t"
"global_load_dwordx2 v[56:57], v245, s[20:21] offset:32\n\t"
"global_load_dwordx2 v[60:61], v245, s[20:21] offset:64\n\t"
"global_load_dwordx2 v[64:65], v245, s[20:21] offset:96\n\t"
"global_load_dwordx2 v[68:69], v245, s[20:21] offset:128\n\t"
"global_load_dwordx2 v[72:73], v245, s[20:21] offset:160\n\t"
"global_load_dwordx2 v[76:77], v245, s[20:21] offset:192\n\t"
"global_load_dwordx2 v[80:81], v245, s[20:21] offset:224\n\t"
"global_load_dwordx2 v[84:85], v246, s[20:21]\n\t"
"s_add_u32 s61, s51, 1\n\t"
"s_sub_u32 s62, s61, 9\n\t"
"s_min_u32 s61, s61, s62\n\t"
"s_lshl_b32 s62, s61, 7\n\t"
"s_add_u32 s53, s48, s62\n\t"
"s_mul_i32 s61, s61, 589824\n\t"
"s_add_u32 s55, s49, s61\n\t"
"s_add_u32 m0, s86, 32768\n\t"
"s_nop 0\n\t"
"buffer_load_dwordx4 v2, s[32:35], s53 offen lds\n\t"
"s_add_u32 m0, s86, 33792\n\t"
"s_nop 0\n\t"
"buffer_load_dwordx4 v3, s[32:35], s53 offen lds\n\t"
"s_add_u32 m0, s86, 34816\n\t"
"s_nop 0\n\t"
"buffer_load_dwordx4 v4, s[32:35], s53 offen lds\n\t"
"s_add_u32 m0, s86, 35840\n\t"
"s_nop 0\n\t"
"buffer_load_dwordx4 v5, s[32:35], s53 offen lds\n\t"
"s_add_u32 m0, s86, 36864\n\t"
"s_nop 0\n\t"
"buffer_load_dwordx4 v6, s[32:35], s53 offen lds\n\t"
"s_add_u32 m0, s86, 37888\n\t"
"s_nop 0\n\t"
"buffer_load_dwordx4 v7, s[32:35], s53 offen lds\n\t"
"s_add_u32 m0, s86, 38912\n\t"
"s_nop 0\n\t"
"buffer_load_dwordx4 v8, s[32:35], s53 offen lds\n\t"
"s_add_u32 m0, s86, 39936\n\t"
"s_nop 0\n\t"
"buffer_load_dwordx4 v9, s[32:35], s53 offen lds\n\t"
"s_add_u32 m0, s87, 20480\n\t"
"s_nop 0\n\t"
"buffer_load_dwordx4 v12, s[36:39], s55 offen offset:0 lds\n\t"
"s_add_u32 m0, s87, 21504\n\t"
"s_nop 0\n\t"
"buffer_load_dwordx4 v13, s[36:39], s55 offen offset:0 lds\n\t"
"s_add_u32 m0, s87, 22400\n\t"
"s_nop 0\n\t"
"buffer_load_dwordx4 v12, s[36:39], s55 offen offset:128 lds\n\t"
"s_add_u32 m0, s87, 23424\n\t"
"s_nop 0\n\t"
"buffer_load_dwordx4 v13, s[36:39], s55 offen offset:128 lds\n\t"
"s_add_u32 m0, s87, 24576\n\t"
"s_nop 0\n\t"
"buffer_load_dwordx4 v14, s[36:39], s55 offen lds\n\t"
"s_mov_b32 s44, s24\n\t"
"s_and_b32 s45, s25, 0xffff\n\t"
"s_mov_b32 s46, 54525952\n\t"
"s_mov_b32 s47, 0x20000\n\t"
"s_lshl2_add_u32 s61, %1, s29\n\t"
"s_lshl_b32 s61, s61, 11\n\t"
"s_add_u32 s61, s61, 50331648\n\t"
"v_min_u32 v46, 10, v0\n\t"
"v_lshl_add_u32 v46, v46, 7, s61\n\t"
"buffer_load_dword v48, v46, s[44:47], 0 offen sc1\n\t"
"s_lshr_b32 s61, s26, 1\n\t"
"s_and_b32 s62, s26, 1\n\t"
"s_add_u32 s31, s61, s27\n\t"
"s_add_u32 s31, s31, s62\n\t"
"s_add_u32 s85, s61, 4\n\t"
"s_cmp_lg_u32 s62, 0\n\t"
"s_cselect_b32 s85, s61, s85\n\t"
"s_mul_i32 s63, s28, 3\n\t"
"s_add_u32 s63, s63, s29\n\t"
"s_sub_u32 s63, s63, 1\n\t"
"s_lshl_b32 s62, s62, 3\n\t"
"s_lshl1_add_u32 s62, s27, s62\n\t"
"s_add_u32 s62, s62, s63\n\t"
"s_lshl4_add_u32 s61, s31, s63\n\t"
"s_lshl4_add_u32 s64, s85, s62\n\t"
"s_sub_u32 s64, s64, 16\n\t"
"s_cmp_lt_u32 s63, 16\n\t"
"s_cselect_b32 s56, s61, s64\n\t"
"s_cselect_b32 s59, s63, s62\n\t"
"s_lshl_b32 s61, s26, 6\n\t"
"s_add_u32 s56, s56, s61\n\t"
"s_cmp_lt_u32 s28, 6\n\t"
"s_cselect_b32 s61, s29, 0\n\t"
"s_cmp_lg_u32 s61, 0\n\t"
"s_cselect_b64 s[98:99], -1, 0\n\t"
"s_cselect_b32 s56, s56, 0\n\t"
"v_and_b32 v247, 31, v0\n\t"
"s_lshl_b32 s61, s56, 8\n\t"
"v_lshl_add_u32 v248, v247, 3, s61\n\t"
"s_lshl_b32 s61, s56, 1\n\t"
"v_mov_b32 v249, s61\n\t"
"global_load_dwordx2 v[86:87], v248, s[6:7]\n\t"
"global_load_dwordx2 v[88:89], v248, s[8:9]\n\t"
"global_load_dwordx2 v[90:91], v248, s[10:11]\n\t"
"global_load_dwordx2 v[92:93], v248, s[12:13]\n\t"
"global_load_ushort v94, v249, s[14:15]\n\t"
"global_load_ushort v101, v249, s[16:17]\n\t"
"s_lshr_b32 s61, %1, 3\n\t"
"s_lshl2_add_u32 s61, s61, s29\n\t"
"s_lshl_b32 s61, s61, 9\n\t"
"v_lshl_add_u32 v249, v0, 7, s61\n\t"
"s_getpc_b64 s[96:97]\n\t"
"LBEG%=:\n\t"
"v_min_u32 v249, LEND%=-LBEG%=-4, v249\n\t"
"s_mov_b64 exec, 15\n\t"
"global_load_dword v49, v249, s[96:97]\n\t"
"s_mov_b64 exec, -1\n\t"
"v_bfe_u32 v225, v0, 2, 2\n\t"
"v_and_b32 v227, 15, v0\n\t"
"v_lshrrev_b32 v231, 5, v0\n\t"
"v_and_b32 v250, 1, v224\n\t"
"v_bfe_u32 v240, v0, 1, 3\n\t"
"v_xor_b32 v240, v240, v224\n\t"
"v_lshlrev_b32 v240, 4, v240\n\t"
"v_lshl_or_b32 v240, v227, 7, v240\n\t"
"v_add_u32 v10, s86, v240\n\t"
"v_xor_b32 v11, 64, v10\n\t"
"v_lshrrev_b32 v251, 1, v225\n\t"
"v_mul_u32_u24 v252, 5120, v231\n\t"
"v_lshl_add_u32 v252, v226, 3, v252\n\t"
"v_lshl_add_u32 v240, v250, 3, v225\n\t"
"v_lshl_add_u32 v253, v240, 7, v252\n\t"
"v_lshl_or_b32 v254, v250, 1, v251\n\t"
"v_lshl_add_u32 v15, v254, 5, v253\n\t"
"v_xor_b32 v16, 32, v15\n\t"
"v_xor_b32 v17, 64, v15\n\t"
"v_xor_b32 v18, 96, v15\n\t"
"s_and_b32 s61, s27, 1\n\t"
"v_xor_b32 v255, s61, v250\n\t"
"v_lshl_add_u32 v19, v240, 6, v252\n\t"
"v_lshl_add_u32 v19, v255, 5, v19\n\t"
"v_add_u32 v19, 4096, v19\n\t"
"v_lshl_or_b32 v240, v250, 2, v251\n\t"
"v_lshrrev_b32 v253, 1, v226\n\t"
"v_xor_b32 v240, v240, v253\n\t"
"v_and_b32 v253, 1, v0\n\t"
"v_lshlrev_b32 v253, 3, v253\n\t"
"v_lshl_or_b32 v240, v240, 4, v253\n\t"
"v_lshl_or_b32 v240, v230, 7, v240\n\t"
"v_lshl_add_u32 v240, v224, 9, v240\n\t"
"s_lshl2_add_u32 s64, s26, s27\n\t"
"s_mul_i32 s64, s64, 135168\n\t"
"s_add_u32 s64, s64, 1048576\n\t"
"s_mul_i32 s59, s59, 4224\n\t"
"s_add_u32 s59, s59, s64\n\t"
"s_lshl2_add_u32 s63, s28, s29\n\t"
"s_lshl_b32 s63, s63, 7\n\t"
"s_add_u32 s64, s64, s63\n\t"
"s_mov_b32 s63, 4224\n\t"
"v_lshlrev_b32 v253, 4, v224\n\t"
"v_mad_u32_u24 v253, v227, s63, v253\n\t"
"s_and_b32 s61, s31, 3\n\t"
"s_lshl_b32 s61, s61, 5\n\t"
"v_xor_b32 v32, s61, v240\n\t"
"v_add_u32 v32, s86, v32\n\t"
"v_xor_b32 v110, 32, v32\n\t"
"s_lshr_b32 s61, s31, 2\n\t"
"s_xor_b32 s61, s61, s30\n\t"
"s_add_u32 s62, s64, 0\n\t"
"s_cmp_eq_u32 s61, 0\n\t"
"s_cselect_b32 s61, s62, 2146435072\n\t"
"s_cselect_b32 s62, 2146435072, s62\n\t"
"v_add_u32 v34, s61, v253\n\t"
"v_add_u32 v35, s62, v253\n\t"
"s_and_b32 s61, s85, 3\n\t"
"s_lshl_b32 s61, s61, 5\n\t"
"v_xor_b32 v33, s61, v240\n\t"
"v_add_u32 v33, s86, v33\n\t"
"v_xor_b32 v111, 32, v33\n\t"
"s_lshr_b32 s61, s85, 2\n\t"
"s_xor_b32 s61, s61, s30\n\t"
"s_add_u32 s62, s64, 67584\n\t"
"s_cmp_eq_u32 s61, 0\n\t"
"s_cselect_b32 s61, s62, 2146435072\n\t"
"s_cselect_b32 s62, 2146435072, s62\n\t"
"v_add_u32 v36, s61, v253\n\t"
"v_add_u32 v37, s62, v253\n\t"
"s_mov_b32 s88, 0x3fb8aa3b\n\t"
"s_mov_b32 s90, 0\n\t"
"s_mov_b32 s91, -1\n\t"
"s_mov_b32 s89, 2097152\n\t"
"s_add_u32 s6, s50, 2\n\t"
"s_sub_u32 s7, s6, 9\n\t"
"s_min_u32 s6, s6, s7\n\t"
"s_lshl_b32 s7, s6, 7\n\t"
"s_add_u32 s52, s48, s7\n\t"
"s_mul_i32 s6, s6, 589824\n\t"
"s_add_u32 s54, s49, s6\n\t"
"s_waitcnt vmcnt(35)\n\t"
"ds_read_b128 a[0:3], v10 offset:0\n\t"
"ds_read_b128 a[4:7], v10 offset:2048\n\t"
"ds_read_b128 a[8:11], v10 offset:4096\n\t"
"ds_read_b128 a[12:15], v10 offset:6144\n\t"
"ds_read_b128 a[16:19], v11 offset:0\n\t"
"ds_read_b128 a[20:23], v11 offset:2048\n\t"
"ds_read_b128 a[24:27], v11 offset:4096\n\t"
"ds_read_b128 a[28:31], v11 offset:6144\n\t"
"s_waitcnt vmcnt(30)\n\t"
"s_barrier\n\t"
"ds_read_b64_tr_b16 a[64:65], v15 offset:0\n\t"
"ds_read_b64_tr_b16 a[66:67], v17 offset:512\n\t"
"ds_read_b64_tr_b16 a[68:69], v16 offset:0\n\t"
"ds_read_b64_tr_b16 a[70:71], v18 offset:512\n\t"
"ds_read_b64_tr_b16 a[72:73], v17 offset:0\n\t"
"ds_read_b64_tr_b16 a[74:75], v15 offset:512\n\t"
"s_waitcnt vmcnt(21)\n\t"
"v_lshlrev_b32 v50, 16, v52\n\t"
"v_and_b32 v51, 0xffff0000, v52\n\t"
"v_lshlrev_b32 v52, 16, v53\n\t"
"v_and_b32 v53, 0xffff0000, v53\n\t"
"s_waitcnt lgkmcnt(6)\n\t"
"s_waitcnt lgkmcnt(4)\n\t"
"v_mfma_f32_16x16x32_bf16 v[112:115], a[64:67], a[0:3], v[50:53]\n\t"
"ds_read_b64_tr_b16 a[76:77], v18 offset:0\n\t"
"ds_read_b64_tr_b16 a[78:79], v16 offset:512\n\t"
"v_mfma_f32_16x16x32_bf16 v[116:119], a[64:67], a[4:7], v[50:53]\n\t"
"ds_read_b64_tr_b16 a[144:145], v32 offset:0\n\t"
"v_lshlrev_b32 v54, 16, v56\n\t"
"v_and_b32 v55, 0xffff0000, v56\n\t"
"v_mfma_f32_16x16x32_bf16 v[120:123], a[64:67], a[8:11], v[50:53]\n\t"
"ds_read_b64_tr_b16 a[146:147], v110 offset:512\n\t"
"v_lshlrev_b32 v56, 16, v57\n\t"
"v_mfma_f32_16x16x32_bf16 v[124:127], a[64:67], a[12:15], v[50:53]\n\t"
"s_waitcnt lgkmcnt(6)\n\t"
"ds_read_b64_tr_b16 a[148:149], v32 offset:4096\n\t"
"v_and_b32 v57, 0xffff0000, v57\n\t"
"s_nop 1\n\t"
"v_mfma_f32_16x16x32_bf16 v[128:131], a[68:71], a[0:3], v[54:57]\n\t"
"ds_read_b64_tr_b16 a[64:65], v15 offset:2048\n\t"
"ds_read_b64_tr_b16 a[66:67], v17 offset:2560\n\t"
"ds_read_b64_tr_b16 a[150:151], v110 offset:4608\n\t"
"v_mfma_f32_16x16x32_bf16 v[132:135], a[68:71], a[4:7], v[54:57]\n\t"
"ds_read_b64_tr_b16 a[152:153], v33 offset:0\n\t"
"v_lshlrev_b32 v58, 16, v60\n\t"
"v_and_b32 v59, 0xffff0000, v60\n\t"
"v_mfma_f32_16x16x32_bf16 v[136:139], a[68:71], a[8:11], v[54:57]\n\t"
"ds_read_b64_tr_b16 a[154:155], v111 offset:512\n\t"
"v_lshlrev_b32 v60, 16, v61\n\t"
"v_mfma_f32_16x16x32_bf16 v[140:143], a[68:71], a[12:15], v[54:57]\n\t"
"s_waitcnt lgkmcnt(10)\n\t"
"ds_read_b64_tr_b16 a[156:157], v33 offset:4096\n\t"
"v_and_b32 v61, 0xffff0000, v61\n\t"
"s_nop 1\n\t"
"v_mfma_f32_16x16x32_bf16 v[144:147], a[72:75], a[0:3], v[58:61]\n\t"
"ds_read_b64_tr_b16 a[68:69], v16 offset:2048\n\t"
"ds_read_b64_tr_b16 a[70:71], v18 offset:2560\n\t"
"ds_read_b64_tr_b16 a[158:159], v111 offset:4608\n\t"
"v_mfma_f32_16x16x32_bf16 v[148:151], a[72:75], a[4:7], v[58:61]\n\t"
"v_lshlrev_b32 v62, 16, v64\n\t"
"v_and_b32 v63, 0xffff0000, v64\n\t"
"v_mfma_f32_16x16x32_bf16 v[152:155], a[72:75], a[8:11], v[58:61]\n\t"
"v_lshlrev_b32 v64, 16, v65\n\t"
"v_mfma_f32_16x16x32_bf16 v[156:159], a[72:75], a[12:15], v[58:61]\n\t"
"s_waitcnt lgkmcnt(12)\n\t"
"v_and_b32 v65, 0xffff0000, v65\n\t"
"s_nop 1\n\t"
"v_mfma_f32_16x16x32_bf16 v[160:163], a[76:79], a[0:3], v[62:65]\n\t"
"s_waitcnt lgkmcnt(0)\n\t"
"ds_read_b64_tr_b16 a[72:73], v17 offset:2048\n\t"
"ds_read_b64_tr_b16 a[74:75], v15 offset:2560\n\t"
"v_mfma_f32_16x16x32_bf16 v[164:167], a[76:79], a[4:7], v[62:65]\n\t"
"s_add_u32 m0, s86, 0\n\t"
"buffer_store_dwordx4 a[144:147], v34, s[44:47], 0 offen offset:0 sc1\n\t"
"v_lshlrev_b32 v66, 16, v68\n\t"
"v_and_b32 v67, 0xffff0000, v68\n\t"
"v_mfma_f32_16x16x32_bf16 v[168:171], a[76:79], a[8:11], v[62:65]\n\t"
"buffer_load_dwordx4 v2, s[32:35], s52 offen lds\n\t"
"v_lshlrev_b32 v68, 16, v69\n\t"
"v_mfma_f32_16x16x32_bf16 v[172:175], a[76:79], a[12:15], v[62:65]\n\t"
"v_and_b32 v69, 0xffff0000, v69\n\t"
"s_nop 1\n\t"
"v_mfma_f32_16x16x32_bf16 v[176:179], a[64:67], a[0:3], v[66:69]\n\t"
"ds_read_b64_tr_b16 a[76:77], v18 offset:2048\n\t"
"ds_read_b64_tr_b16 a[78:79], v16 offset:2560\n\t"
"v_mfma_f32_16x16x32_bf16 v[180:183], a[64:67], a[4:7], v[66:69]\n\t"
"s_add_u32 m0, s86, 1024\n\t"
"buffer_store_dwordx4 a[148:151], v34, s[44:47], 0 offen offset:64 sc1\n\t"
"v_lshlrev_b32 v70, 16, v72\n\t"
"v_and_b32 v71, 0xffff0000, v72\n\t"
"v_mfma_f32_16x16x32_bf16 v[184:187], a[64:67], a[8:11], v[66:69]\n\t"
"buffer_load_dwordx4 v3, s[32:35], s52 offen lds\n\t"
"v_lshlrev_b32 v72, 16, v73\n\t"
"v_mfma_f32_16x16x32_bf16 v[188:191], a[64:67], a[12:15], v[66:69]\n\t"
"v_and_b32 v73, 0xffff0000, v73\n\t"
"s_nop 1\n\t"
"v_mfma_f32_16x16x32_bf16 v[192:195], a[68:71], a[0:3], v[70:73]\n\t"
"ds_read_b64_tr_b16 a[64:65], v19 offset:0\n\t"
"ds_read_b64_tr_b16 a[66:67], v19 offset:256\n\t"
"v_mfma_f32_16x16x32_bf16 v[196:199], a[68:71], a[4:7], v[70:73]\n\t"
"s_add_u32 m0, s86, 2048\n\t"
"buffer_store_dwordx4 a[152:155], v36, s[44:47], 0 offen offset:0 sc1\n\t"
"v_lshlrev_b32 v74, 16, v76\n\t"
"v_and_b32 v75, 0xffff0000, v76\n\t"
"v_mfma_f32_16x16x32_bf16 v[200:203], a[68:71], a[8:11], v[70:73]\n\t"
"buffer_load_dwordx4 v4, s[32:35], s52 offen lds\n\t"
"v_lshlrev_b32 v76, 16, v77\n\t"
"v_mfma_f32_16x16x32_bf16 v[204:207], a[68:71], a[12:15], v[70:73]\n\t"
"s_waitcnt lgkmcnt(4)\n\t"
"v_and_b32 v77, 0xffff0000, v77\n\t"
"s_nop 1\n\t"
"v_mfma_f32_16x16x32_bf16 v[208:211], a[72:75], a[0:3], v[74:77]\n\t"
"ds_read_b64_tr_b16 a[68:69], v15 offset:10240\n\t"
"ds_read_b64_tr_b16 a[70:71], v17 offset:10752\n\t"
"v_mfma_f32_16x16x32_bf16 v[212:215], a[72:75], a[4:7], v[74:77]\n\t"
"s_add_u32 m0, s86, 3072\n\t"
"buffer_store_dwordx4 a[156:159], v36, s[44:47], 0 offen offset:64 sc1\n\t"
"v_lshlrev_b32 v78, 16, v80\n\t"
"v_and_b32 v79, 0xffff0000, v80\n\t"
"v_mfma_f32_16x16x32_bf16 v[216:219], a[72:75], a[8:11], v[74:77]\n\t"
"buffer_load_dwordx4 v5, s[32:35], s52 offen lds\n\t"
"v_lshlrev_b32 v80, 16, v81\n\t"
"v_mfma_f32_16x16x32_bf16 v[220:223], a[72:75], a[12:15], v[74:77]\n\t"
"s_waitcnt lgkmcnt(4)\n\t"
"v_and_b32 v81, 0xffff0000, v81\n\t"
"s_nop 1\n\t"
"v_mfma_f32_16x16x32_bf16 v[224:227], a[76:79], a[0:3], v[78:81]\n\t"
"ds_read_b64_tr_b16 a[72:73], v16 offset:10240\n\t"
"ds_read_b64_tr_b16 a[74:75], v18 offset:10752\n\t"
"v_mfma_f32_16x16x32_bf16 v[228:231], a[76:79], a[4:7], v[78:81]\n\t"
"s_add_u32 m0, s86, 4096\n\t"
"v_lshlrev_b32 v82, 16, v84\n\t"
"v_and_b32 v83, 0xffff0000, v84\n\t"
"v_mfma_f32_16x16x32_bf16 v[232:235], a[76:79], a[8:11], v[78:81]\n\t"
"buffer_load_dwordx4 v6, s[32:35], s52 offen lds\n\t"
"v_lshlrev_b32 v84, 16, v85\n\t"
"v_mfma_f32_16x16x32_bf16 v[236:239], a[76:79], a[12:15], v[78:81]\n\t"
"s_waitcnt lgkmcnt(4)\n\t"
"v_and_b32 v85, 0xffff0000, v85\n\t"
"s_nop 1\n\t"
"v_mfma_f32_16x16x32_bf16 v[240:243], a[64:67], a[0:3], v[82:85]\n\t"
"ds_read_b64_tr_b16 a[76:77], v17 offset:10240\n\t"
"ds_read_b64_tr_b16 a[78:79], v15 offset:10752\n\t"
"v_mfma_f32_16x16x32_bf16 v[244:247], a[64:67], a[4:7], v[82:85]\n\t"
"s_add_u32 m0, s86, 5120\n\t"
"v_mfma_f32_16x16x32_bf16 v[248:251], a[64:67], a[8:11], v[82:85]\n\t"
"buffer_load_dwordx4 v7, s[32:35], s52 offen lds\n\t"
"v_mfma_f32_16x16x32_bf16 v[252:255], a[64:67], a[12:15], v[82:85]\n\t"
"s_waitcnt lgkmcnt(4)\n\t"
"v_mfma_f32_16x16x32_bf16 v[112:115], a[68:71], a[16:19], v[112:115]\n\t"
"ds_read_b64_tr_b16 a[64:65], v18 offset:10240\n\t"
"ds_read_b64_tr_b16 a[66:67], v16 offset:10752\n\t"
"v_mfma_f32_16x16x32_bf16 v[116:119], a[68:71], a[20:23], v[116:119]\n\t"
"s_add_u32 m0, s86, 6144\n\t"
"s_waitcnt vmcnt(17)\n\t"
"v_add_u32 v48, 1, v48\n\t"
"v_mfma_f32_16x16x32_bf16 v[120:123], a[68:71], a[24:27], v[120:123]\n\t"
"s_add_u32 s6, s51, 3\n\t"
"buffer_load_dwordx4 v8, s[32:35], s52 offen lds\n\t"
"s_lshl3_add_u32 s61, s27, s26\n\t"
"s_lshl_b32 s61, s61, 13\n\t"
"s_lshl_b32 s62, s28, 7\n\t"
"s_add_u32 s61, s61, s62\n\t"
"s_add_u32 s61, s61, 50331648\n\t"
"s_cmp_eq_u32 s29, 0\n\t"
"s_cselect_b32 s61, s61, 2146435072\n\t"
"s_cselect_b32 s12, 0xff, 0\n\t"
"s_mov_b32 s10, 0xffffff00\n\t"
"s_mov_b32 s11, -1\n\t"
"v_lshl_add_u32 v68, v0, 18, s61\n\t"
"v_mfma_f32_16x16x32_bf16 v[124:127], a[68:71], a[28:31], v[124:127]\n\t"
"s_waitcnt lgkmcnt(4)\n\t"
"s_lshl_b32 s61, s28, 5\n\t"
"s_add_u32 s61, s61, s26\n\t"
"s_lshl2_add_u32 s61, s61, s29\n\t"
"s_lshl_b32 s61, s61, 11\n\t"
"s_add_u32 s61, s61, 50333952\n\t"
"s_add_u32 s62, s27, 1\n\t"
"v_add_u32 v69, s62, v0\n\t"
"v_and_b32 v69, 3, v69\n\t"
"v_lshlrev_b32 v69, 16, v69\n\t"
"v_mfma_f32_16x16x32_bf16 v[128:131], a[72:75], a[16:19], v[128:131]\n\t"
"s_sub_u32 s7, s6, 9\n\t"
"ds_read_b64_tr_b16 a[68:69], v15 offset:12288\n\t"
"ds_read_b64_tr_b16 a[70:71], v17 offset:12800\n\t"
"v_lshlrev_b32 v70, 7, v0\n\t"
"v_mfma_f32_16x16x32_bf16 v[132:135], a[72:75], a[20:23], v[132:135]\n\t"
"s_add_u32 m0, s86, 7168\n\t"
"v_sub_u32 v69, v69, v70\n\t"
"v_add_u32 v69, s61, v69\n\t"
"v_cndmask_b32 v68, v68, v69, s[10:11]\n\t"
"v_mfma_f32_16x16x32_bf16 v[136:139], a[72:75], a[24:27], v[136:139]\n\t"
"s_min_u32 s6, s6, s7\n\t"
"buffer_load_dwordx4 v9, s[32:35], s52 offen lds\n\t"
"v_mov_b32 v69, 2146435072\n\t"
"v_mfma_f32_16x16x32_bf16 v[140:143], a[72:75], a[28:31], v[140:143]\n\t"
"s_waitcnt lgkmcnt(4)\n\t"
"ds_read_b128 a[32:35], v10 offset:32768\n\t"
"v_cmp_gt_u32 vcc, 11, v0\n\t"
"s_nop 1\n\t"
"v_cndmask_b32 v47, v69, v68, vcc\n\t"
"v_mfma_f32_16x16x32_bf16 v[144:147], a[76:79], a[16:19], v[144:147]\n\t"
"s_lshl_b32 s7, s6, 7\n\t"
"ds_read_b64_tr_b16 a[72:73], v16 offset:12288\n\t"
"ds_read_b64_tr_b16 a[74:75], v18 offset:12800\n\t"
"v_mfma_f32_16x16x32_bf16 v[148:151], a[76:79], a[20:23], v[148:151]\n\t"
"s_add_u32 m0, s87, 40960\n\t"
"ds_read_b64_tr_b16 a[144:145], v32 offset:32768\n\t"
"buffer_store_dword v48, v47, s[44:47], s89 offen sc1\n\t"
"s_waitcnt vmcnt(14)\n\t"
"s_sub_u32 s61, s29, 1\n\t"
"s_mul_i32 s61, s61, 12288\n\t"
"s_add_u32 s61, s61, 126976\n\t"
"s_cmp_lg_u64 s[98:99], 0\n\t"
"s_cselect_b32 s57, s61, 196608\n\t"
"s_add_u32 s61, s86, 32768\n\t"
"s_cmp_lg_u64 s[98:99], 0\n\t"
"s_cselect_b32 s58, s61, 196608\n\t"
"s_cselect_b32 s59, s59, 2146435072\n\t"
"v_lshlrev_b32 v68, 16, v94\n\t"
"v_mfma_f32_16x16x32_bf16 v[152:155], a[76:79], a[24:27], v[152:155]\n\t"
"s_add_u32 s53, s48, s7\n\t"
"buffer_load_dwordx4 v12, s[36:39], s54 offen offset:0 lds\n\t"
"v_mul_f32 v68, s88, v68\n\t"
"v_mfma_f32_16x16x32_bf16 v[156:159], a[76:79], a[28:31], v[156:159]\n\t"
"s_waitcnt lgkmcnt(6)\n\t"
"ds_read_b128 a[36:39], v10 offset:34816\n\t"
"ds_read_b64_tr_b16 a[146:147], v110 offset:33280\n\t"
"v_mfma_f32_16x16x32_bf16 v[160:163], a[64:67], a[16:19], v[160:163]\n\t"
"s_mul_i32 s6, s6, 589824\n\t"
"ds_read_b64_tr_b16 a[76:77], v17 offset:12288\n\t"
"ds_read_b64_tr_b16 a[78:79], v15 offset:12800\n\t"
"v_mfma_f32_16x16x32_bf16 v[164:167], a[64:67], a[20:23], v[164:167]\n\t"
"s_add_u32 m0, s87, 41984\n\t"
"ds_read_b64_tr_b16 a[148:149], v32 offset:36864\n\t"
"v_exp_f32 v68, v68\n\t"
"v_mfma_f32_16x16x32_bf16 v[168:171], a[64:67], a[24:27], v[168:171]\n\t"
"s_add_u32 s55, s49, s6\n\t"
"buffer_load_dwordx4 v13, s[36:39], s54 offen offset:0 lds\n\t"
"v_lshlrev_b32 v101, 16, v101\n\t"
"v_cvt_pk_bf16_f32 v68, v68, v68\n\t"
"v_mfma_f32_16x16x32_bf16 v[172:175], a[64:67], a[28:31], v[172:175]\n\t"
"s_waitcnt lgkmcnt(9)\n\t"
"ds_read_b128 a[40:43], v10 offset:36864\n\t"
"ds_read_b64_tr_b16 a[150:151], v110 offset:37376\n\t"
"v_mfma_f32_16x16x32_bf16 v[176:179], a[68:71], a[16:19], v[176:179]\n\t"
"ds_read_b64_tr_b16 a[64:65], v18 offset:12288\n\t"
"ds_read_b64_tr_b16 a[66:67], v16 offset:12800\n\t"
"v_mfma_f32_16x16x32_bf16 v[180:183], a[68:71], a[20:23], v[180:183]\n\t"
"s_add_u32 m0, s87, 42880\n\t"
"ds_read_b64_tr_b16 a[152:153], v33 offset:32768\n\t"
"v_cmp_eq_u32 vcc, 0, v0\n\t"
"v_lshlrev_b32 v68, 16, v68\n\t"
"v_mov_b32 v69, 0x41000000\n\t"
"v_cndmask_b32 v101, 0, v101, vcc\n\t"
"v_mfma_f32_16x16x32_bf16 v[184:187], a[68:71], a[24:27], v[184:187]\n\t"
"buffer_load_dwordx4 v12, s[36:39], s54 offen offset:128 lds\n\t"
"v_mfma_f32_16x16x32_bf16 v[188:191], a[68:71], a[28:31], v[188:191]\n\t"
"s_waitcnt lgkmcnt(11)\n\t"
"ds_read_b128 a[44:47], v10 offset:38912\n\t"
"ds_read_b64_tr_b16 a[154:155], v111 offset:33280\n\t"
"v_mul_f32 v94, 0xbfb8aa3b, v68\n\t"
"v_mfma_f32_16x16x32_bf16 v[192:195], a[72:75], a[16:19], v[192:195]\n\t"
"ds_read_b64_tr_b16 a[68:69], v19 offset:10240\n\t"
"ds_read_b64_tr_b16 a[70:71], v19 offset:10496\n\t"
"v_mul_f32 v95, 0.15915494, v68\n\t"
"v_mfma_f32_16x16x32_bf16 v[196:199], a[72:75], a[20:23], v[196:199]\n\t"
"s_add_u32 m0, s87, 43904\n\t"
"ds_read_b64_tr_b16 a[156:157], v33 offset:36864\n\t"
"v_add_f32 v68, v68, v68\n\t"
"v_cndmask_b32 v95, v95, -v95, s[90:91]\n\t"
"v_mfma_f32_16x16x32_bf16 v[200:203], a[72:75], a[24:27], v[200:203]\n\t"
"buffer_load_dwordx4 v13, s[36:39], s54 offen offset:128 lds\n\t"
"v_cndmask_b32 v96, 0, v68, s[90:91]\n\t"
"v_mfma_f32_16x16x32_bf16 v[204:207], a[72:75], a[28:31], v[204:207]\n\t"
"s_waitcnt lgkmcnt(11)\n\t"
"ds_read_b128 a[48:51], v11 offset:32768\n\t"
"ds_read_b64_tr_b16 a[158:159], v111 offset:37376\n\t"
"v_cndmask_b32 v97, 1.0, 0, s[90:91]\n\t"
"v_mfma_f32_16x16x32_bf16 v[208:211], a[76:79], a[16:19], v[208:211]\n\t"
"v_cndmask_b32 v98, v69, 1.0, s[90:91]\n\t"
"v_cndmask_b32 v99, 1.0, 2.0, s[90:91]\n\t"
"v_mov_b32 v68, 0x43780000\n\t"
"v_mfma_f32_16x16x32_bf16 v[212:215], a[76:79], a[20:23], v[212:215]\n\t"
"v_cndmask_b32 v100, v69, v68, s[90:91]\n\t"
"v_and_b32 v68, 31, v0\n\t"
"v_lshrrev_b32 v69, 5, v0\n\t"
"v_mfma_f32_16x16x32_bf16 v[216:219], a[76:79], a[24:27], v[216:219]\n\t"
"v_xor_b32 v70, v69, v0\n\t"
"v_not_b32 v70, v70\n\t"
"v_and_b32 v70, 1, v70\n\t"
"v_mfma_f32_16x16x32_bf16 v[220:223], a[76:79], a[28:31], v[220:223]\n\t"
"s_waitcnt lgkmcnt(3)\n\t"
"s_barrier\n\t"
"s_waitcnt lgkmcnt(0)\n\t"
"ds_read_b128 a[52:55], v11 offset:34816\n\t"
"buffer_store_dwordx4 a[144:147], v35, s[44:47], 0 offen offset:0 sc1\n\t"
"v_lshlrev_b32 v70, 6, v70\n\t"
"v_mfma_f32_16x16x32_bf16 v[224:227], a[64:67], a[16:19], v[224:227]\n\t"
"ds_read_b64_tr_b16 a[72:73], v15 offset:20480\n\t"
"ds_read_b64_tr_b16 a[74:75], v17 offset:20992\n\t"
"v_mfma_f32_16x16x32_bf16 v[228:231], a[64:67], a[20:23], v[228:231]\n\t"
"s_add_u32 m0, s87, 45056\n\t"
"ds_read_b64_tr_b16 a[76:77], v16 offset:20480\n\t"
"ds_read_b64_tr_b16 a[78:79], v18 offset:20992\n\t"
"buffer_store_dwordx4 a[148:151], v35, s[44:47], 0 offen offset:64 sc1\n\t"
"v_mfma_f32_16x16x32_bf16 v[232:235], a[64:67], a[24:27], v[232:235]\n\t"
"buffer_load_dwordx4 v14, s[36:39], s54 offen lds\n\t"
"v_lshl_or_b32 v70, v68, 8, v70\n\t"
"v_bfe_u32 v71, v0, 1, 2\n\t"
"v_mfma_f32_16x16x32_bf16 v[236:239], a[64:67], a[28:31], v[236:239]\n\t"
"ds_read_b128 a[56:59], v11 offset:36864\n\t"
"buffer_store_dwordx4 a[152:155], v37, s[44:47], 0 offen offset:0 sc1\n\t"
"v_lshl_or_b32 v70, v71, 4, v70\n\t"
"v_mfma_f32_16x16x32_bf16 v[240:243], a[68:71], a[16:19], v[240:243]\n\t"
"ds_read_b64_tr_b16 a[64:65], v17 offset:20480\n\t"
"ds_read_b64_tr_b16 a[66:67], v15 offset:20992\n\t"
"v_add_u32 v102, s57, v70\n\t"
"v_mfma_f32_16x16x32_bf16 v[244:247], a[68:71], a[20:23], v[244:247]\n\t"
"buffer_store_dwordx4 a[156:159], v37, s[44:47], 0 offen offset:64 sc1\n\t"
"v_bfe_u32 v70, v0, 2, 2\n\t"
"v_lshl_add_u32 v70, v69, 3, v70\n\t"
"v_mfma_f32_16x16x32_bf16 v[248:251], a[68:71], a[24:27], v[248:251]\n\t"
"v_lshlrev_b32 v70, 7, v70\n\t"
"v_bfe_u32 v71, v0, 3, 1\n\t"
"v_mfma_f32_16x16x32_bf16 v[252:255], a[68:71], a[28:31], v[252:255]\n\t"
"ds_read_b128 a[60:63], v11 offset:38912\n\t"
"v_lshl_or_b32 v70, v71, 6, v70\n\t"
"v_bfe_u32 v71, v0, 4, 1\n\t"
"v_xor_b32 v71, v71, v69\n\t"
"v_lshl_or_b32 v70, v71, 5, v70\n\t"
"v_and_b32 v71, 3, v0\n\t"
"v_lshl_or_b32 v70, v71, 3, v70\n\t"
"v_add_u32 v103, s57, v70\n\t"
"v_xor_b32 v104, 16, v103\n\t"
"s_waitcnt lgkmcnt(6)\n\t"
"s_waitcnt lgkmcnt(0)\n\t"
"v_mfma_f32_16x16x32_bf16 v[112:115], a[72:75], a[32:35], v[112:115]\n\t"
"s_add_u32 s6, s50, 4\n\t"
"ds_read_b64_tr_b16 a[68:69], v18 offset:20480\n\t"
"ds_read_b64_tr_b16 a[70:71], v16 offset:20992\n\t"
"v_xor_b32 v105, 64, v103\n\t"
"v_mfma_f32_16x16x32_bf16 v[116:119], a[72:75], a[36:39], v[116:119]\n\t"
"s_add_u32 m0, s86, 32768\n\t"
"v_xor_b32 v106, 80, v103\n\t"
"v_lshlrev_b32 v68, 16, v86\n\t"
"v_lshlrev_b32 v69, 16, v88\n\t"
"v_mfma_f32_16x16x32_bf16 v[120:123], a[72:75], a[40:43], v[120:123]\n\t"
"s_sub_u32 s7, s6, 9\n\t"
"buffer_load_dwordx4 v2, s[32:35], s53 offen lds\n\t"
"v_mul_f32 v68, s88, v68\n\t"
"v_lshlrev_b32 v60, 16, v90\n\t"
"v_mfma_f32_16x16x32_bf16 v[124:127], a[72:75], a[44:47], v[124:127]\n\t"
"s_min_u32 s6, s6, s7\n\t"
"v_exp_f32 v68, v68\n\t"
"v_lshlrev_b32 v61, 16, v92\n\t"
"v_mfma_f32_16x16x32_bf16 v[128:131], a[76:79], a[32:35], v[128:131]\n\t"
"s_lshl_b32 s7, s6, 7\n\t"
"ds_read_b64_tr_b16 a[72:73], v15 offset:22528\n\t"
"ds_read_b64_tr_b16 a[74:75], v17 offset:23040\n\t"
"v_cvt_pk_bf16_f32 v68, v68, v68\n\t"
"v_mfma_f32_16x16x32_bf16 v[132:135], a[76:79], a[36:39], v[132:135]\n\t"
"s_add_u32 m0, s86, 33792\n\t"
"v_mul_f32 v69, v95, v69\n\t"
"v_lshlrev_b32 v68, 16, v68\n\t"
"v_fma_f32 v60, v60, v96, v97\n\t"
"v_mfma_f32_16x16x32_bf16 v[136:139], a[76:79], a[40:43], v[136:139]\n\t"
"s_add_u32 s52, s48, s7\n\t"
"buffer_load_dwordx4 v3, s[32:35], s53 offen lds\n\t"
"v_mul_f32 v68, v94, v68\n\t"
"v_mul_f32 v61, -v96, v61\n\t"
"v_mul_f32 v72, 0x41000000, v68\n\t"
"v_mfma_f32_16x16x32_bf16 v[140:143], a[76:79], a[44:47], v[140:143]\n\t"
"s_mul_i32 s6, s6, 589824\n\t"
"v_mul_f32 v73, 0x41000000, v69\n\t"
"v_fract_f32 v71, v73\n\t"
"v_mfma_f32_16x16x32_bf16 v[144:147], a[64:67], a[32:35], v[144:147]\n\t"
"s_add_u32 s54, s49, s6\n\t"
"ds_read_b64_tr_b16 a[76:77], v16 offset:22528\n\t"
"ds_read_b64_tr_b16 a[78:79], v18 offset:23040\n\t"
"v_exp_f32 v70, v72\n\t"
"v_mfma_f32_16x16x32_bf16 v[148:151], a[64:67], a[36:39], v[148:151]\n\t"
"v_cos_f32 v56, v71\n\t"
"v_mfma_f32_16x16x32_bf16 v[152:155], a[64:67], a[40:43], v[152:155]\n\t"
"v_sin_f32 v57, v71\n\t"
"v_mul_f32 v56, v70, v56\n\t"
"v_mul_f32 v57, v70, v57\n\t"
"v_mfma_f32_16x16x32_bf16 v[156:159], a[64:67], a[44:47], v[156:159]\n\t"
"s_waitcnt lgkmcnt(4)\n\t"
"v_mul_f32 v72, v100, v68\n\t"
"v_mul_f32 v73, v100, v69\n\t"
"v_fract_f32 v71, v73\n\t"
"v_mfma_f32_16x16x32_bf16 v[160:163], a[68:71], a[32:35], v[160:163]\n\t"
"ds_read_b64_tr_b16 a[64:65], v17 offset:22528\n\t"
"ds_read_b64_tr_b16 a[66:67], v15 offset:23040\n\t"
"v_mfma_f32_16x16x32_bf16 v[164:167], a[68:71], a[36:39], v[164:167]\n\t"
"s_add_u32 m0, s86, 34816\n\t"
"v_exp_f32 v70, v72\n\t"
"v_cos_f32 v58, v71\n\t"
"v_mfma_f32_16x16x32_bf16 v[168:171], a[68:71], a[40:43], v[168:171]\n\t"
"buffer_load_dwordx4 v4, s[32:35], s53 offen lds\n\t"
"v_sin_f32 v59, v71\n\t"
"v_mfma_f32_16x16x32_bf16 v[172:175], a[68:71], a[44:47], v[172:175]\n\t"
"s_waitcnt lgkmcnt(4)\n\t"
"v_mul_f32 v58, v70, v58\n\t"
"v_mul_f32 v59, v70, v59\n\t"
"v_mul_f32 v72, v98, v68\n\t"
"v_mfma_f32_16x16x32_bf16 v[176:179], a[72:75], a[32:35], v[176:179]\n\t"
"ds_read_b64_tr_b16 a[68:69], v18 offset:22528\n\t"
"ds_read_b64_tr_b16 a[70:71], v16 offset:23040\n\t"
"v_mul_f32 v73, v98, v69\n\t"
"v_mfma_f32_16x16x32_bf16 v[180:183], a[72:75], a[36:39], v[180:183]\n\t"
"s_add_u32 m0, s86, 35840\n\t"
"v_fract_f32 v71, v73\n\t"
"v_exp_f32 v70, v72\n\t"
"v_mfma_f32_16x16x32_bf16 v[184:187], a[72:75], a[40:43], v[184:187]\n\t"
"buffer_load_dwordx4 v5, s[32:35], s53 offen lds\n\t"
"v_cos_f32 v50, v71\n\t"
"v_mfma_f32_16x16x32_bf16 v[188:191], a[72:75], a[44:47], v[188:191]\n\t"
"s_waitcnt lgkmcnt(4)\n\t"
"v_sin_f32 v51, v71\n\t"
"v_mul_f32 v50, v70, v50\n\t"
"v_mul_f32 v51, v70, v51\n\t"
"v_mfma_f32_16x16x32_bf16 v[192:195], a[76:79], a[32:35], v[192:195]\n\t"
"ds_read_b64_tr_b16 a[72:73], v19 offset:20480\n\t"
"ds_read_b64_tr_b16 a[74:75], v19 offset:20736\n\t"
"v_mul_f32 v72, v99, v72\n\t"
"v_mfma_f32_16x16x32_bf16 v[196:199], a[76:79], a[36:39], v[196:199]\n\t"
"s_add_u32 m0, s86, 36864\n\t"
"v_mul_f32 v73, v99, v73\n\t"
"v_fract_f32 v71, v73\n\t"
"v_mfma_f32_16x16x32_bf16 v[200:203], a[76:79], a[40:43], v[200:203]\n\t"
"buffer_load_dwordx4 v6, s[32:35], s53 offen lds\n\t"
"v_exp_f32 v70, v72\n\t"
"v_mfma_f32_16x16x32_bf16 v[204:207], a[76:79], a[44:47], v[204:207]\n\t"
"s_waitcnt lgkmcnt(4)\n\t"
"v_cos_f32 v52, v71\n\t"
"v_sin_f32 v53, v71\n\t"
"v_mfma_f32_16x16x32_bf16 v[208:211], a[64:67], a[32:35], v[208:211]\n\t"
"ds_read_b64_tr_b16 a[76:77], v15 offset:30720\n\t"
"ds_read_b64_tr_b16 a[78:79], v17 offset:31232\n\t"
"v_mul_f32 v52, v70, v52\n\t"
"v_mfma_f32_16x16x32_bf16 v[212:215], a[64:67], a[36:39], v[212:215]\n\t"
"v_mul_f32 v53, v70, v53\n\t"
"v_mul_f32 v72, v99, v72\n\t"
"v_mul_f32 v73, v99, v73\n\t"
"v_mfma_f32_16x16x32_bf16 v[216:219], a[64:67], a[40:43], v[216:219]\n\t"
"v_fract_f32 v71, v73\n\t"
"v_exp_f32 v70, v72\n\t"
"v_mfma_f32_16x16x32_bf16 v[220:223], a[64:67], a[44:47], v[220:223]\n\t"
"s_waitcnt lgkmcnt(4)\n\t"
"v_cos_f32 v54, v71\n\t"
"v_mfma_f32_16x16x32_bf16 v[224:227], a[68:71], a[32:35], v[224:227]\n\t"
"ds_read_b64_tr_b16 a[64:65], v16 offset:30720\n\t"
"ds_read_b64_tr_b16 a[66:67], v18 offset:31232\n\t"
"v_sin_f32 v55, v71\n\t"
"v_mfma_f32_16x16x32_bf16 v[228:231], a[68:71], a[36:39], v[228:231]\n\t"
"s_add_u32 m0, s86, 37888\n\t"
"v_mul_f32 v54, v70, v54\n\t"
"v_mul_f32 v55, v70, v55\n\t"
"v_mul_f32 v70, v60, v50\n\t"
"v_mfma_f32_16x16x32_bf16 v[232:235], a[68:71], a[40:43], v[232:235]\n\t"
"buffer_load_dwordx4 v7, s[32:35], s53 offen lds\n\t"
"v_mul_f32 v71, v60, v51\n\t"
"v_fma_f32 v62, -v61, v51, v70\n\t"
"v_mfma_f32_16x16x32_bf16 v[236:239], a[68:71], a[44:47], v[236:239]\n\t"
"s_waitcnt lgkmcnt(4)\n\t"
"v_fma_f32 v63, v61, v50, v71\n\t"
"v_cvt_pk_bf16_f32 v72, v60, v62\n\t"
"v_cvt_pk_bf16_f32 v76, v61, v63\n\t"
"v_mfma_f32_16x16x32_bf16 v[240:243], a[72:75], a[32:35], v[240:243]\n\t"
"ds_read_b64_tr_b16 a[68:69], v17 offset:30720\n\t"
"ds_read_b64_tr_b16 a[70:71], v15 offset:31232\n\t"
"v_mul_f32 v70, v62, v50\n\t"
"v_mfma_f32_16x16x32_bf16 v[244:247], a[72:75], a[36:39], v[244:247]\n\t"
"s_add_u32 m0, s86, 38912\n\t"
"v_mul_f32 v71, v62, v51\n\t"
"v_fma_f32 v64, -v63, v51, v70\n\t"
"v_fma_f32 v65, v63, v50, v71\n\t"
"v_mfma_f32_16x16x32_bf16 v[248:251], a[72:75], a[40:43], v[248:251]\n\t"
"buffer_load_dwordx4 v8, s[32:35], s53 offen lds\n\t"
"v_mul_f32 v70, v64, v50\n\t"
"v_mul_f32 v71, v64, v51\n\t"
"v_fma_f32 v66, -v65, v51, v70\n\t"
"v_mfma_f32_16x16x32_bf16 v[252:255], a[72:75], a[44:47], v[252:255]\n\t"
"s_waitcnt lgkmcnt(4)\n\t"
"v_fma_f32 v67, v65, v50, v71\n\t"
"v_cvt_pk_bf16_f32 v73, v64, v66\n\t"
"v_cvt_pk_bf16_f32 v77, v65, v67\n\t"
"v_mfma_f32_16x16x32_bf16 v[112:115], a[76:79], a[48:51], v[112:115]\n\t"
"ds_read_b64_tr_b16 a[72:73], v18 offset:30720\n\t"
"ds_read_b64_tr_b16 a[74:75], v16 offset:31232\n\t"
"v_mul_f32 v70, v66, v50\n\t"
"v_mfma_f32_16x16x32_bf16 v[116:119], a[76:79], a[52:55], v[116:119]\n\t"
"s_add_u32 m0, s86, 39936\n\t"
"v_mul_f32 v71, v66, v51\n\t"
"v_fma_f32 v60, -v67, v51, v70\n\t"
"v_fma_f32 v61, v67, v50, v71\n\t"
"v_mfma_f32_16x16x32_bf16 v[120:123], a[76:79], a[56:59], v[120:123]\n\t"
"buffer_load_dwordx4 v9, s[32:35], s53 offen lds\n\t"
"v_mul_f32 v70, v60, v50\n\t"
"v_mul_f32 v71, v60, v51\n\t"
"v_mfma_f32_16x16x32_bf16 v[124:127], a[76:79], a[60:63], v[124:127]\n\t"
"s_waitcnt lgkmcnt(4)\n\t"
"v_fma_f32 v62, -v61, v51, v70\n\t"
"v_fma_f32 v63, v61, v50, v71\n\t"
"v_cvt_pk_bf16_f32 v80, v60, v62\n\t"
"v_mfma_f32_16x16x32_bf16 v[128:131], a[64:67], a[48:51], v[128:131]\n\t"
"ds_read_b64_tr_b16 a[76:77], v15 offset:32768\n\t"
"ds_read_b64_tr_b16 a[78:79], v17 offset:33280\n\t"
"v_cvt_pk_bf16_f32 v32, v61, v63\n\t"
"v_mfma_f32_16x16x32_bf16 v[132:135], a[64:67], a[52:55], v[132:135]\n\t"
"v_mul_f32 v70, v62, v50\n\t"
"v_mul_f32 v71, v62, v51\n\t"
"v_fma_f32 v64, -v63, v51, v70\n\t"
"v_fma_f32 v65, v63, v50, v71\n\t"
"v_mfma_f32_16x16x32_bf16 v[136:139], a[64:67], a[56:59], v[136:139]\n\t"
"v_mul_f32 v70, v64, v50\n\t"
"v_mul_f32 v71, v64, v51\n\t"
"v_fma_f32 v66, -v65, v51, v70\n\t"
"v_mfma_f32_16x16x32_bf16 v[140:143], a[64:67], a[60:63], v[140:143]\n\t"
"s_waitcnt vmcnt(18)\n\t"
"s_waitcnt lgkmcnt(4)\n\t"
"ds_read_b128 a[0:3], v10 offset:0\n\t"
"v_fma_f32 v67, v65, v50, v71\n\t"
"v_cvt_pk_bf16_f32 v81, v64, v66\n\t"
"v_mfma_f32_16x16x32_bf16 v[144:147], a[68:71], a[48:51], v[144:147]\n\t"
"ds_read_b64_tr_b16 a[64:65], v16 offset:32768\n\t"
"ds_read_b64_tr_b16 a[66:67], v18 offset:33280\n\t"
"v_cvt_pk_bf16_f32 v33, v65, v67\n\t"
"v_mfma_f32_16x16x32_bf16 v[148:151], a[68:71], a[52:55], v[148:151]\n\t"
"s_add_u32 m0, s87, 0\n\t"
"v_mul_f32 v70, v66, v50\n\t"
"v_mul_f32 v71, v66, v51\n\t"
"v_fma_f32 v60, -v67, v51, v70\n\t"
"v_mfma_f32_16x16x32_bf16 v[152:155], a[68:71], a[56:59], v[152:155]\n\t"
"buffer_load_dwordx4 v12, s[36:39], s55 offen offset:0 lds\n\t"
"v_fma_f32 v61, v67, v50, v71\n\t"
"v_mul_f32 v70, v60, v58\n\t"
"v_mfma_f32_16x16x32_bf16 v[156:159], a[68:71], a[60:63], v[156:159]\n\t"
"s_waitcnt lgkmcnt(5)\n\t"
"ds_read_b128 a[4:7], v10 offset:2048\n\t"
"v_mul_f32 v71, v60, v59\n\t"
"v_fma_f32 v62, -v61, v59, v70\n\t"
"v_fma_f32 v63, v61, v58, v71\n\t"
"v_mfma_f32_16x16x32_bf16 v[160:163], a[72:75], a[48:51], v[160:163]\n\t"
"ds_read_b64_tr_b16 a[68:69], v17 offset:32768\n\t"
"ds_read_b64_tr_b16 a[70:71], v15 offset:33280\n\t"
"v_cvt_pk_bf16_f32 v74, v60, v62\n\t"
"v_mfma_f32_16x16x32_bf16 v[164:167], a[72:75], a[52:55], v[164:167]\n\t"
"s_add_u32 m0, s87, 1024\n\t"
"v_cvt_pk_bf16_f32 v78, v61, v63\n\t"
"v_mul_f32 v70, v62, v52\n\t"
"v_mul_f32 v71, v62, v53\n\t"
"v_mfma_f32_16x16x32_bf16 v[168:171], a[72:75], a[56:59], v[168:171]\n\t"
"buffer_load_dwordx4 v13, s[36:39], s55 offen offset:0 lds\n\t"
"v_fma_f32 v64, -v63, v53, v70\n\t"
"v_fma_f32 v65, v63, v52, v71\n\t"
"v_mfma_f32_16x16x32_bf16 v[172:175], a[72:75], a[60:63], v[172:175]\n\t"
"s_waitcnt lgkmcnt(6)\n\t"
"ds_read_b128 a[8:11], v10 offset:4096\n\t"
"v_mul_f32 v70, v64, v52\n\t"
"v_mul_f32 v71, v64, v53\n\t"
"v_mfma_f32_16x16x32_bf16 v[176:179], a[76:79], a[48:51], v[176:179]\n\t"
"ds_read_b64_tr_b16 a[72:73], v18 offset:32768\n\t"
"ds_read_b64_tr_b16 a[74:75], v16 offset:33280\n\t"
"v_fma_f32 v66, -v65, v53, v70\n\t"
"v_mfma_f32_16x16x32_bf16 v[180:183], a[76:79], a[52:55], v[180:183]\n\t"
"s_add_u32 m0, s87, 1920\n\t"
"v_fma_f32 v67, v65, v52, v71\n\t"
"v_cvt_pk_bf16_f32 v75, v64, v66\n\t"
"v_cvt_pk_bf16_f32 v79, v65, v67\n\t"
"v_mfma_f32_16x16x32_bf16 v[184:187], a[76:79], a[56:59], v[184:187]\n\t"
"buffer_load_dwordx4 v12, s[36:39], s55 offen offset:128 lds\n\t"
"v_xor_b32 v68, 0, v102\n\t"
"v_mfma_f32_16x16x32_bf16 v[188:191], a[76:79], a[60:63], v[188:191]\n\t"
"s_waitcnt lgkmcnt(6)\n\t"
"ds_read_b128 a[12:15], v10 offset:6144\n\t"
"ds_write_b128 v68, v[72:75]\n\t"
"v_mfma_f32_16x16x32_bf16 v[192:195], a[64:67], a[48:51], v[192:195]\n\t"
"ds_read_b64_tr_b16 a[76:77], v19 offset:30720\n\t"
"ds_read_b64_tr_b16 a[78:79], v19 offset:30976\n\t"
"v_mfma_f32_16x16x32_bf16 v[196:199], a[64:67], a[52:55], v[196:199]\n\t"
"ds_write_b128 v68, v[76:79] offset:128\n\t"
"v_mfma_f32_16x16x32_bf16 v[200:203], a[64:67], a[56:59], v[200:203]\n\t"
"v_mul_f32 v70, v66, v52\n\t"
"v_mul_f32 v71, v66, v53\n\t"
"v_fma_f32 v60, -v67, v53, v70\n\t"
"v_mfma_f32_16x16x32_bf16 v[204:207], a[64:67], a[60:63], v[204:207]\n\t"
"s_waitcnt lgkmcnt(8)\n\t"
"ds_read_b128 a[16:19], v11 offset:0\n\t"
"v_fma_f32 v61, v67, v52, v71\n\t"
"v_mul_f32 v70, v60, v52\n\t"
"v_mfma_f32_16x16x32_bf16 v[208:211], a[68:71], a[48:51], v[208:211]\n\t"
"v_mul_f32 v71, v60, v53\n\t"
"v_fma_f32 v62, -v61, v53, v70\n\t"
"v_fma_f32 v63, v61, v52, v71\n\t"
"v_mfma_f32_16x16x32_bf16 v[212:215], a[68:71], a[52:55], v[212:215]\n\t"
"s_add_u32 m0, s87, 2944\n\t"
"v_cvt_pk_bf16_f32 v82, v60, v62\n\t"
"v_cvt_pk_bf16_f32 v34, v61, v63\n\t"
"v_mul_f32 v70, v62, v58\n\t"
"v_mul_f32 v71, v62, v59\n\t"
"v_mfma_f32_16x16x32_bf16 v[216:219], a[68:71], a[56:59], v[216:219]\n\t"
"buffer_load_dwordx4 v13, s[36:39], s55 offen offset:128 lds\n\t"
"v_fma_f32 v64, -v63, v59, v70\n\t"
"v_fma_f32 v65, v63, v58, v71\n\t"
"v_mfma_f32_16x16x32_bf16 v[220:223], a[68:71], a[60:63], v[220:223]\n\t"
"s_waitcnt vmcnt(12) lgkmcnt(2)\n\t"
"s_barrier\n\t"
"ds_read_b128 a[20:23], v11 offset:2048\n\t"
"v_mul_f32 v70, v64, v54\n\t"
"v_mul_f32 v71, v64, v55\n\t"
"v_mfma_f32_16x16x32_bf16 v[224:227], a[72:75], a[48:51], v[224:227]\n\t"
"ds_read_b64_tr_b16 a[64:65], v15 offset:40960\n\t"
"ds_read_b64_tr_b16 a[66:67], v17 offset:41472\n\t"
"v_fma_f32 v66, -v65, v55, v70\n\t"
"v_mfma_f32_16x16x32_bf16 v[228:231], a[72:75], a[52:55], v[228:231]\n\t"
"s_add_u32 m0, s87, 4096\n\t"
"ds_read_b64_tr_b16 a[68:69], v16 offset:40960\n\t"
"ds_read_b64_tr_b16 a[70:71], v18 offset:41472\n\t"
"v_fma_f32 v67, v65, v54, v71\n\t"
"v_mfma_f32_16x16x32_bf16 v[232:235], a[72:75], a[56:59], v[232:235]\n\t"
"buffer_load_dwordx4 v14, s[36:39], s55 offen lds\n\t"
"v_cvt_pk_bf16_f32 v83, v64, v66\n\t"
"v_cvt_pk_bf16_f32 v35, v65, v67\n\t"
"v_mfma_f32_16x16x32_bf16 v[236:239], a[72:75], a[60:63], v[236:239]\n\t"
"ds_read_b128 a[24:27], v11 offset:4096\n\t"
"v_xor_b32 v68, 16, v102\n\t"
"v_mfma_f32_16x16x32_bf16 v[240:243], a[76:79], a[48:51], v[240:243]\n\t"
"ds_read_b64_tr_b16 a[72:73], v17 offset:40960\n\t"
"ds_read_b64_tr_b16 a[74:75], v15 offset:41472\n\t"
"ds_write_b128 v68, v[80:83]\n\t"
"v_mfma_f32_16x16x32_bf16 v[244:247], a[76:79], a[52:55], v[244:247]\n\t"
"v_mfma_f32_16x16x32_bf16 v[248:251], a[76:79], a[56:59], v[248:251]\n\t"
"ds_write_b128 v68, v[32:35] offset:128\n\t"
"v_mul_f32 v70, v66, v54\n\t"
"v_mfma_f32_16x16x32_bf16 v[252:255], a[76:79], a[60:63], v[252:255]\n\t"
"ds_read_b128 a[28:31], v11 offset:6144\n\t"
"v_mul_f32 v71, v66, v55\n\t"
"v_fma_f32 v60, -v67, v55, v70\n\t"
"v_fma_f32 v61, v67, v54, v71\n\t"
"v_mul_f32 v70, v60, v58\n\t"
"v_mul_f32 v71, v60, v59\n\t"
"v_fma_f32 v62, -v61, v59, v70\n\t"
"v_fma_f32 v63, v61, v58, v71\n\t"
"s_waitcnt lgkmcnt(8)\n\t"
"s_waitcnt lgkmcnt(0)\n\t"
"v_mfma_f32_16x16x32_bf16 v[112:115], a[64:67], a[0:3], v[112:115]\n\t"
"s_add_u32 s6, s51, 5\n\t"
"ds_read_b64_tr_b16 a[76:77], v18 offset:40960\n\t"
"ds_read_b64_tr_b16 a[78:79], v16 offset:41472\n\t"
"v_cvt_pk_bf16_f32 v74, v60, v62\n\t"
"v_mfma_f32_16x16x32_bf16 v[116:119], a[64:67], a[4:7], v[116:119]\n\t"
"s_add_u32 m0, s86, 0\n\t"
"buffer_load_dword v37, v46, s[44:47], s89 offen sc1\n\t"
"v_cvt_pk_bf16_f32 v78, v61, v63\n\t"
"v_mul_f32 v70, v62, v54\n\t"
"v_mfma_f32_16x16x32_bf16 v[120:123], a[64:67], a[8:11], v[120:123]\n\t"
"s_sub_u32 s7, s6, 9\n\t"
"buffer_load_dwordx4 v2, s[32:35], s52 offen lds\n\t"
"v_mul_f32 v71, v62, v55\n\t"
"v_fma_f32 v64, -v63, v55, v70\n\t"
"v_mfma_f32_16x16x32_bf16 v[124:127], a[64:67], a[12:15], v[124:127]\n\t"
"s_min_u32 s6, s6, s7\n\t"
"v_fma_f32 v65, v63, v54, v71\n\t"
"v_mul_f32 v70, v64, v54\n\t"
"v_mul_f32 v71, v64, v55\n\t"
"v_mfma_f32_16x16x32_bf16 v[128:131], a[68:71], a[0:3], v[128:131]\n\t"
"s_lshl_b32 s7, s6, 7\n\t"
"ds_read_b64_tr_b16 a[64:65], v15 offset:43008\n\t"
"ds_read_b64_tr_b16 a[66:67], v17 offset:43520\n\t"
"v_fma_f32 v66, -v65, v55, v70\n\t"
"v_mfma_f32_16x16x32_bf16 v[132:135], a[68:71], a[4:7], v[132:135]\n\t"
"s_add_u32 m0, s86, 1024\n\t"
"v_fma_f32 v67, v65, v54, v71\n\t"
"v_cvt_pk_bf16_f32 v75, v64, v66\n\t"
"v_cvt_pk_bf16_f32 v79, v65, v67\n\t"
"v_mul_f32 v70, v66, v58\n\t"
"v_mfma_f32_16x16x32_bf16 v[136:139], a[68:71], a[8:11], v[136:139]\n\t"
"s_add_u32 s53, s48, s7\n\t"
"buffer_load_dwordx4 v3, s[32:35], s52 offen lds\n\t"
"v_mul_f32 v71, v66, v59\n\t"
"v_fma_f32 v60, -v67, v59, v70\n\t"
"v_mfma_f32_16x16x32_bf16 v[140:143], a[68:71], a[12:15], v[140:143]\n\t"
"s_mul_i32 s6, s6, 589824\n\t"
"v_fma_f32 v61, v67, v58, v71\n\t"
"v_mul_f32 v70, v60, v56\n\t"
"v_mul_f32 v71, v60, v57\n\t"
"v_mfma_f32_16x16x32_bf16 v[144:147], a[72:75], a[0:3], v[144:147]\n\t"
"s_add_u32 s55, s49, s6\n\t"
"ds_read_b64_tr_b16 a[68:69], v16 offset:43008\n\t"
"ds_read_b64_tr_b16 a[70:71], v18 offset:43520\n\t"
"v_fma_f32 v62, -v61, v57, v70\n\t"
"v_mfma_f32_16x16x32_bf16 v[148:151], a[72:75], a[4:7], v[148:151]\n\t"
"v_fma_f32 v63, v61, v56, v71\n\t"
"v_cvt_pk_bf16_f32 v72, v60, v62\n\t"
"v_cvt_pk_bf16_f32 v76, v61, v63\n\t"
"v_mfma_f32_16x16x32_bf16 v[152:155], a[72:75], a[8:11], v[152:155]\n\t"
"v_mul_f32 v70, v62, v58\n\t"
"v_mul_f32 v71, v62, v59\n\t"
"v_fma_f32 v64, -v63, v59, v70\n\t"
"v_mfma_f32_16x16x32_bf16 v[156:159], a[72:75], a[12:15], v[156:159]\n\t"
"s_waitcnt lgkmcnt(4)\n\t"
"v_fma_f32 v65, v63, v58, v71\n\t"
"v_mul_f32 v70, v64, v56\n\t"
"v_mul_f32 v71, v64, v57\n\t"
"v_fma_f32 v66, -v65, v57, v70\n\t"
"v_mfma_f32_16x16x32_bf16 v[160:163], a[76:79], a[0:3], v[160:163]\n\t"
"ds_read_b64_tr_b16 a[72:73], v17 offset:43008\n\t"
"ds_read_b64_tr_b16 a[74:75], v15 offset:43520\n\t"
"v_fma_f32 v67, v65, v56, v71\n\t"
"v_mfma_f32_16x16x32_bf16 v[164:167], a[76:79], a[4:7], v[164:167]\n\t"
"s_add_u32 m0, s86, 2048\n\t"
"v_cvt_pk_bf16_f32 v73, v64, v66\n\t"
"v_cvt_pk_bf16_f32 v77, v65, v67\n\t"
"v_xor_b32 v68, 32, v102\n\t"
"v_mfma_f32_16x16x32_bf16 v[168:171], a[76:79], a[8:11], v[168:171]\n\t"
"buffer_load_dwordx4 v4, s[32:35], s52 offen lds\n\t"
"ds_write_b128 v68, v[72:75]\n\t"
"v_mfma_f32_16x16x32_bf16 v[172:175], a[76:79], a[12:15], v[172:175]\n\t"
"s_waitcnt lgkmcnt(5)\n\t"
"v_mfma_f32_16x16x32_bf16 v[176:179], a[64:67], a[0:3], v[176:179]\n\t"
"ds_read_b64_tr_b16 a[76:77], v18 offset:43008\n\t"
"ds_read_b64_tr_b16 a[78:79], v16 offset:43520\n\t"
"ds_write_b128 v68, v[76:79] offset:128\n\t"
"v_mfma_f32_16x16x32_bf16 v[180:183], a[64:67], a[4:7], v[180:183]\n\t"
"s_add_u32 m0, s86, 3072\n\t"
"v_mul_f32 v70, v66, v58\n\t"
"v_mul_f32 v71, v66, v59\n\t"
"v_mfma_f32_16x16x32_bf16 v[184:187], a[64:67], a[8:11], v[184:187]\n\t"
"buffer_load_dwordx4 v5, s[32:35], s52 offen lds\n\t"
"v_fma_f32 v60, -v67, v59, v70\n\t"
"v_fma_f32 v61, v67, v58, v71\n\t"
"v_mfma_f32_16x16x32_bf16 v[188:191], a[64:67], a[12:15], v[188:191]\n\t"
"s_waitcnt lgkmcnt(6)\n\t"
"v_mul_f32 v70, v60, v56\n\t"
"v_mul_f32 v71, v60, v57\n\t"
"v_fma_f32 v62, -v61, v57, v70\n\t"
"v_mfma_f32_16x16x32_bf16 v[192:195], a[68:71], a[0:3], v[192:195]\n\t"
"ds_read_b64_tr_b16 a[64:65], v19 offset:40960\n\t"
"ds_read_b64_tr_b16 a[66:67], v19 offset:41216\n\t"
"v_fma_f32 v63, v61, v56, v71\n\t"
"v_mfma_f32_16x16x32_bf16 v[196:199], a[68:71], a[4:7], v[196:199]\n\t"
"s_add_u32 m0, s86, 4096\n\t"
"v_cvt_pk_bf16_f32 v80, v60, v62\n\t"
"v_cvt_pk_bf16_f32 v32, v61, v63\n\t"
"v_mul_f32 v70, v62, v58\n\t"
"v_mul_f32 v71, v62, v59\n\t"
"v_mfma_f32_16x16x32_bf16 v[200:203], a[68:71], a[8:11], v[200:203]\n\t"
"buffer_load_dwordx4 v6, s[32:35], s52 offen lds\n\t"
"v_fma_f32 v64, -v63, v59, v70\n\t"
"v_fma_f32 v65, v63, v58, v71\n\t"
"v_mfma_f32_16x16x32_bf16 v[204:207], a[68:71], a[12:15], v[204:207]\n\t"
"s_waitcnt lgkmcnt(6)\n\t"
"v_mul_f32 v70, v64, v56\n\t"
"v_mul_f32 v71, v64, v57\n\t"
"v_fma_f32 v66, -v65, v57, v70\n\t"
"v_mfma_f32_16x16x32_bf16 v[208:211], a[72:75], a[0:3], v[208:211]\n\t"
"ds_read_b64_tr_b16 a[68:69], v15 offset:51200\n\t"
"ds_read_b64_tr_b16 a[70:71], v17 offset:51712\n\t"
"v_fma_f32 v67, v65, v56, v71\n\t"
"v_mfma_f32_16x16x32_bf16 v[212:215], a[72:75], a[4:7], v[212:215]\n\t"
"v_cvt_pk_bf16_f32 v81, v64, v66\n\t"
"v_cvt_pk_bf16_f32 v33, v65, v67\n\t"
"v_mul_f32 v70, v66, v56\n\t"
"v_mfma_f32_16x16x32_bf16 v[216:219], a[72:75], a[8:11], v[216:219]\n\t"
"v_mul_f32 v71, v66, v57\n\t"
"v_fma_f32 v60, -v67, v57, v70\n\t"
"v_fma_f32 v61, v67, v56, v71\n\t"
"v_mfma_f32_16x16x32_bf16 v[220:223], a[72:75], a[12:15], v[220:223]\n\t"
"s_waitcnt lgkmcnt(5)\n\t"
"v_mul_f32 v70, v60, v56\n\t"
"v_mul_f32 v71, v60, v57\n\t"
"v_fma_f32 v62, -v61, v57, v70\n\t"
"v_mfma_f32_16x16x32_bf16 v[224:227], a[76:79], a[0:3], v[224:227]\n\t"
"ds_read_b64_tr_b16 a[72:73], v16 offset:51200\n\t"
"ds_read_b64_tr_b16 a[74:75], v18 offset:51712\n\t"
"v_fma_f32 v63, v61, v56, v71\n\t"
"v_cvt_pk_bf16_f32 v82, v60, v62\n\t"
"v_mfma_f32_16x16x32_bf16 v[228:231], a[76:79], a[4:7], v[228:231]\n\t"
"s_add_u32 m0, s86, 5120\n\t"
"v_cvt_pk_bf16_f32 v34, v61, v63\n\t"
"v_mul_f32 v70, v62, v56\n\t"
"v_mfma_f32_16x16x32_bf16 v[232:235], a[76:79], a[8:11], v[232:235]\n\t"
"buffer_load_dwordx4 v7, s[32:35], s52 offen lds\n\t"
"v_mul_f32 v71, v62, v57\n\t"
"v_fma_f32 v64, -v63, v57, v70\n\t"
"v_fma_f32 v65, v63, v56, v71\n\t"
"v_mfma_f32_16x16x32_bf16 v[236:239], a[76:79], a[12:15], v[236:239]\n\t"
"s_waitcnt lgkmcnt(4)\n\t"
"v_mul_f32 v70, v64, v56\n\t"
"v_mul_f32 v71, v64, v57\n\t"
"v_mfma_f32_16x16x32_bf16 v[240:243], a[64:67], a[0:3], v[240:243]\n\t"
"ds_read_b64_tr_b16 a[76:77], v17 offset:51200\n\t"
"ds_read_b64_tr_b16 a[78:79], v15 offset:51712\n\t"
"v_fma_f32 v66, -v65, v57, v70\n\t"
"v_fma_f32 v67, v65, v56, v71\n\t"
"v_mfma_f32_16x16x32_bf16 v[244:247], a[64:67], a[4:7], v[244:247]\n\t"
"s_add_u32 m0, s86, 6144\n\t"
"v_cvt_pk_bf16_f32 v83, v64, v66\n\t"
"v_cvt_pk_bf16_f32 v35, v65, v67\n\t"
"v_mfma_f32_16x16x32_bf16 v[248:251], a[64:67], a[8:11], v[248:251]\n\t"
"buffer_load_dwordx4 v8, s[32:35], s52 offen lds\n\t"
"v_xor_b32 v68, 48, v102\n\t"
"v_mfma_f32_16x16x32_bf16 v[252:255], a[64:67], a[12:15], v[252:255]\n\t"
"s_waitcnt lgkmcnt(4)\n\t"
"ds_write_b128 v68, v[80:83]\n\t"
"v_mfma_f32_16x16x32_bf16 v[112:115], a[68:71], a[16:19], v[112:115]\n\t"
"ds_read_b64_tr_b16 a[64:65], v18 offset:51200\n\t"
"ds_read_b64_tr_b16 a[66:67], v16 offset:51712\n\t"
"v_mfma_f32_16x16x32_bf16 v[116:119], a[68:71], a[20:23], v[116:119]\n\t"
"s_add_u32 m0, s86, 7168\n\t"
"ds_write_b128 v68, v[32:35] offset:128\n\t"
"ds_read_b64_tr_b16 a[112:113], v103 offset:0\n\t"
"v_mfma_f32_16x16x32_bf16 v[120:123], a[68:71], a[24:27], v[120:123]\n\t"
"buffer_load_dwordx4 v9, s[32:35], s52 offen lds\n\t"
"ds_read_b64_tr_b16 a[114:115], v104 offset:512\n\t"
"ds_read_b64_tr_b16 a[116:117], v105 offset:0\n\t"
"v_mfma_f32_16x16x32_bf16 v[124:127], a[68:71], a[28:31], v[124:127]\n\t"
"s_waitcnt lgkmcnt(9)\n\t"
"ds_read_b64_tr_b16 a[118:119], v106 offset:512\n\t"
"ds_read_b64_tr_b16 a[120:121], v103 offset:2048\n\t"
"ds_read_b64_tr_b16 a[122:123], v104 offset:2560\n\t"
"v_mfma_f32_16x16x32_bf16 v[128:131], a[72:75], a[16:19], v[128:131]\n\t"
"ds_read_b64_tr_b16 a[68:69], v15 offset:53248\n\t"
"ds_read_b64_tr_b16 a[70:71], v17 offset:53760\n\t"
"ds_read_b64_tr_b16 a[124:125], v105 offset:2048\n\t"
"v_mfma_f32_16x16x32_bf16 v[132:135], a[72:75], a[20:23], v[132:135]\n\t"
"ds_read_b64_tr_b16 a[126:127], v106 offset:2560\n\t"
"v_and_b32 v68, 0xffff0000, v86\n\t"
"v_and_b32 v69, 0xffff0000, v88\n\t"
"v_mfma_f32_16x16x32_bf16 v[136:139], a[72:75], a[24:27], v[136:139]\n\t"
"v_mul_f32 v68, s88, v68\n\t"
"v_and_b32 v60, 0xffff0000, v90\n\t"
"v_exp_f32 v68, v68\n\t"
"v_mfma_f32_16x16x32_bf16 v[140:143], a[72:75], a[28:31], v[140:143]\n\t"
"s_waitcnt vmcnt(14)\n\t"
"s_waitcnt lgkmcnt(14)\n\t"
"ds_read_b128 a[32:35], v10 offset:32768\n\t"
"v_and_b32 v61, 0xffff0000, v92\n\t"
"v_mfma_f32_16x16x32_bf16 v[144:147], a[76:79], a[16:19], v[144:147]\n\t"
"ds_read_b64_tr_b16 a[72:73], v16 offset:53248\n\t"
"ds_read_b64_tr_b16 a[74:75], v18 offset:53760\n\t"
"v_cvt_pk_bf16_f32 v68, v68, v68\n\t"
"v_mul_f32 v69, v95, v69\n\t"
"v_mfma_f32_16x16x32_bf16 v[148:151], a[76:79], a[20:23], v[148:151]\n\t"
"s_add_u32 m0, s87, 20480\n\t"
"v_lshlrev_b32 v68, 16, v68\n\t"
"v_fma_f32 v60, v60, v96, v97\n\t"
"v_mfma_f32_16x16x32_bf16 v[152:155], a[76:79], a[24:27], v[152:155]\n\t"
"buffer_load_dwordx4 v12, s[36:39], s54 offen offset:0 lds\n\t"
"s_waitcnt lgkmcnt(9)\n\t"
"v_mfma_f32_32x32x16_bf16 a[128:143], a[112:115], a[116:119], 0\n\t"
"ds_read_b64_tr_b16 a[112:113], v103 offset:4096\n\t"
"v_mfma_f32_16x16x32_bf16 v[156:159], a[76:79], a[28:31], v[156:159]\n\t"
"ds_read_b128 a[36:39], v10 offset:34816\n\t"
"ds_read_b64_tr_b16 a[114:115], v104 offset:4608\n\t"
"ds_read_b64_tr_b16 a[116:117], v105 offset:4096\n\t"
"v_mfma_f32_16x16x32_bf16 v[160:163], a[64:67], a[16:19], v[160:163]\n\t"
"ds_read_b64_tr_b16 a[76:77], v17 offset:53248\n\t"
"ds_read_b64_tr_b16 a[78:79], v15 offset:53760\n\t"
"ds_read_b64_tr_b16 a[118:119], v106 offset:4608\n\t"
"v_mfma_f32_16x16x32_bf16 v[164:167], a[64:67], a[20:23], v[164:167]\n\t"
"s_add_u32 m0, s87, 21504\n\t"
"v_mul_f32 v68, v94, v68\n\t"
"v_mul_f32 v61, -v96, v61\n\t"
"v_mul_f32 v72, 0x41000000, v68\n\t"
"v_mfma_f32_16x16x32_bf16 v[168:171], a[64:67], a[24:27], v[168:171]\n\t"
"buffer_load_dwordx4 v13, s[36:39], s54 offen offset:0 lds\n\t"
"v_mul_f32 v73, 0x41000000, v69\n\t"
"v_fract_f32 v71, v73\n\t"
"v_mfma_f32_16x16x32_bf16 v[172:175], a[64:67], a[28:31], v[172:175]\n\t"
"s_waitcnt lgkmcnt(12)\n\t"
"ds_read_b128 a[40:43], v10 offset:36864\n\t"
"v_exp_f32 v70, v72\n\t"
"v_mfma_f32_16x16x32_bf16 v[176:179], a[68:71], a[16:19], v[176:179]\n\t"
"ds_read_b64_tr_b16 a[64:65], v18 offset:53248\n\t"
"ds_read_b64_tr_b16 a[66:67], v16 offset:53760\n\t"
"v_cos_f32 v56, v71\n\t"
"v_mfma_f32_16x16x32_bf16 v[180:183], a[68:71], a[20:23], v[180:183]\n\t"
"s_add_u32 m0, s87, 22400\n\t"
"v_sin_f32 v57, v71\n\t"
"v_mul_f32 v56, v70, v56\n\t"
"v_mfma_f32_16x16x32_bf16 v[184:187], a[68:71], a[24:27], v[184:187]\n\t"
"buffer_load_dwordx4 v12, s[36:39], s54 offen offset:128 lds\n\t"
"v_mul_f32 v57, v70, v57\n\t"
"v_mfma_f32_16x16x32_bf16 v[188:191], a[68:71], a[28:31], v[188:191]\n\t"
"s_waitcnt lgkmcnt(10)\n\t"
"ds_read_b128 a[44:47], v10 offset:38912\n\t"
"v_mfma_f32_32x32x16_bf16 a[128:143], a[120:123], a[124:127], a[128:143]\n\t"
"ds_read_b64_tr_b16 a[120:121], v103 offset:6144\n\t"
"v_mfma_f32_16x16x32_bf16 v[192:195], a[72:75], a[16:19], v[192:195]\n\t"
"ds_read_b64_tr_b16 a[68:69], v19 offset:51200\n\t"
"ds_read_b64_tr_b16 a[70:71], v19 offset:51456\n\t"
"ds_read_b64_tr_b16 a[122:123], v104 offset:6656\n\t"
"v_mfma_f32_16x16x32_bf16 v[196:199], a[72:75], a[20:23], v[196:199]\n\t"
"ds_read_b64_tr_b16 a[124:125], v105 offset:6144\n\t"
"ds_read_b64_tr_b16 a[126:127], v106 offset:6656\n\t"
"v_mul_f32 v72, v100, v68\n\t"
"v_mfma_f32_16x16x32_bf16 v[200:203], a[72:75], a[24:27], v[200:203]\n\t"
"v_mul_f32 v73, v100, v69\n\t"
"v_fract_f32 v71, v73\n\t"
"v_mfma_f32_16x16x32_bf16 v[204:207], a[72:75], a[28:31], v[204:207]\n\t"
"s_waitcnt lgkmcnt(11)\n\t"
"ds_read_b128 a[48:51], v11 offset:32768\n\t"
"v_exp_f32 v70, v72\n\t"
"v_mfma_f32_16x16x32_bf16 v[208:211], a[76:79], a[16:19], v[208:211]\n\t"
"v_cos_f32 v58, v71\n\t"
"v_sin_f32 v59, v71\n\t"
"v_mfma_f32_16x16x32_bf16 v[212:215], a[76:79], a[20:23], v[212:215]\n\t"
"s_add_u32 m0, s87, 23424\n\t"
"v_mul_f32 v58, v70, v58\n\t"
"v_mul_f32 v59, v70, v59\n\t"
"v_mul_f32 v72, v98, v68\n\t"
"v_mfma_f32_16x16x32_bf16 v[216:219], a[76:79], a[24:27], v[216:219]\n\t"
"buffer_load_dwordx4 v13, s[36:39], s54 offen offset:128 lds\n\t"
"v_mul_f32 v73, v98, v69\n\t"
"v_mfma_f32_16x16x32_bf16 v[220:223], a[76:79], a[28:31], v[220:223]\n\t"
"s_waitcnt vmcnt(13) lgkmcnt(4)\n\t"
"s_barrier\n\t"
"ds_read_b128 a[52:55], v11 offset:34816\n\t"
"v_mfma_f32_32x32x16_bf16 a[128:143], a[112:115], a[116:119], a[128:143]\n\t"
"v_fract_f32 v71, v73\n\t"
"v_mfma_f32_16x16x32_bf16 v[224:227], a[64:67], a[16:19], v[224:227]\n\t"
"ds_read_b64_tr_b16 a[72:73], v15 offset:0\n\t"
"ds_read_b64_tr_b16 a[74:75], v17 offset:512\n\t"
"v_mfma_f32_16x16x32_bf16 v[228:231], a[64:67], a[20:23], v[228:231]\n\t"
"s_add_u32 m0, s87, 24576\n\t"
"ds_read_b64_tr_b16 a[76:77], v16 offset:0\n\t"
"ds_read_b64_tr_b16 a[78:79], v18 offset:512\n\t"
"v_exp_f32 v70, v72\n\t"
"v_mfma_f32_16x16x32_bf16 v[232:235], a[64:67], a[24:27], v[232:235]\n\t"
"buffer_load_dwordx4 v14, s[36:39], s54 offen lds\n\t"
"s_waitcnt vmcnt(13)\n\t"
"v_cndmask_b32 v111, 1, 0, s[10:11]\n\t"
"v_add_u32 v111, v111, v37\n\t"
"buffer_store_dword v111, v47, s[44:47], 0 offen sc1\n\t"
"v_mfma_f32_16x16x32_bf16 v[236:239], a[64:67], a[28:31], v[236:239]\n\t"
"ds_read_b128 a[56:59], v11 offset:36864\n\t"
"v_mfma_f32_16x16x32_bf16 v[240:243], a[68:71], a[16:19], v[240:243]\n\t"
"ds_read_b64_tr_b16 a[64:65], v17 offset:0\n\t"
"ds_read_b64_tr_b16 a[66:67], v15 offset:512\n\t"
"v_mfma_f32_16x16x32_bf16 v[244:247], a[68:71], a[20:23], v[244:247]\n\t"
"v_cos_f32 v50, v71\n\t"
"v_mfma_f32_16x16x32_bf16 v[248:251], a[68:71], a[24:27], v[248:251]\n\t"
"v_sin_f32 v51, v71\n\t"
"v_mul_f32 v50, v70, v50\n\t"
"v_mul_f32 v51, v70, v51\n\t"
"v_mfma_f32_16x16x32_bf16 v[252:255], a[68:71], a[28:31], v[252:255]\n\t"
"ds_read_b128 a[60:63], v11 offset:38912\n\t"
"s_waitcnt lgkmcnt(10)\n\t"
"v_mfma_f32_32x32x16_bf16 a[128:143], a[120:123], a[124:127], a[128:143]\n\t"
"v_mul_f32 v72, v99, v72\n\t"
"v_mul_f32 v73, v99, v73\n\t"
"v_fract_f32 v71, v73\n\t"
"v_exp_f32 v70, v72\n\t"
"v_cos_f32 v52, v71\n\t"
"s_waitcnt lgkmcnt(6)\n\t"
"s_waitcnt lgkmcnt(0)\n\t"
"v_mfma_f32_16x16x32_bf16 v[112:115], a[72:75], a[32:35], v[112:115]\n\t"
"s_add_u32 s6, s50, 6\n\t"
"ds_read_b64_tr_b16 a[68:69], v18 offset:0\n\t"
"ds_read_b64_tr_b16 a[70:71], v16 offset:512\n\t"
"v_mfma_f32_16x16x32_bf16 v[116:119], a[72:75], a[36:39], v[116:119]\n\t"
"s_add_u32 m0, s86, 32768\n\t"
"v_sin_f32 v53, v71\n\t"
"v_mul_f32 v52, v70, v52\n\t"
"v_mul_f32 v53, v70, v53\n\t"
"v_mfma_f32_16x16x32_bf16 v[120:123], a[72:75], a[40:43], v[120:123]\n\t"
"s_sub_u32 s7, s6, 9\n\t"
"buffer_load_dwordx4 v2, s[32:35], s53 offen lds\n\t"
"v_mul_f32 v72, v99, v72\n\t"
"v_mul_f32 v73, v99, v73\n\t"
"v_mfma_f32_16x16x32_bf16 v[124:127], a[72:75], a[44:47], v[124:127]\n\t"
"s_min_u32 s6, s6, s7\n\t"
"v_fract_f32 v71, v73\n\t"
"v_exp_f32 v70, v72\n\t"
"v_mfma_f32_16x16x32_bf16 v[128:131], a[76:79], a[32:35], v[128:131]\n\t"
"s_lshl_b32 s7, s6, 7\n\t"
"ds_read_b64_tr_b16 a[72:73], v15 offset:2048\n\t"
"ds_read_b64_tr_b16 a[74:75], v17 offset:2560\n\t"
"v_mfma_f32_16x16x32_bf16 v[132:135], a[76:79], a[36:39], v[132:135]\n\t"
"s_add_u32 m0, s86, 33792\n\t"
"v_cos_f32 v54, v71\n\t"
"v_sin_f32 v55, v71\n\t"
"v_mfma_f32_16x16x32_bf16 v[136:139], a[76:79], a[40:43], v[136:139]\n\t"
"s_add_u32 s52, s48, s7\n\t"
"buffer_load_dwordx4 v3, s[32:35], s53 offen lds\n\t"
"v_mul_f32 v54, v70, v54\n\t"
"v_mul_f32 v55, v70, v55\n\t"
"v_mfma_f32_16x16x32_bf16 v[140:143], a[76:79], a[44:47], v[140:143]\n\t"
"s_mul_i32 s6, s6, 589824\n\t"
"v_mul_f32 v70, v60, v50\n\t"
"v_mul_f32 v71, v60, v51\n\t"
"v_fma_f32 v62, -v61, v51, v70\n\t"
"v_mfma_f32_16x16x32_bf16 v[144:147], a[64:67], a[32:35], v[144:147]\n\t"
"s_add_u32 s54, s49, s6\n\t"
"ds_read_b64_tr_b16 a[76:77], v16 offset:2048\n\t"
"ds_read_b64_tr_b16 a[78:79], v18 offset:2560\n\t"
"v_fma_f32 v63, v61, v50, v71\n\t"
"v_mfma_f32_16x16x32_bf16 v[148:151], a[64:67], a[36:39], v[148:151]\n\t"
"s_mov_b32 s40, s22\n\t"
"v_cvt_pk_bf16_f32 v72, v60, v62\n\t"
"v_cvt_pk_bf16_f32 v76, v61, v63\n\t"
"v_mul_f32 v70, v62, v50\n\t"
"v_mfma_f32_16x16x32_bf16 v[152:155], a[64:67], a[40:43], v[152:155]\n\t"
"s_and_b32 s41, s23, 0xffff\n\t"
"v_mul_f32 v71, v62, v51\n\t"
"v_fma_f32 v64, -v63, v51, v70\n\t"
"v_fma_f32 v65, v63, v50, v71\n\t"
"v_mfma_f32_16x16x32_bf16 v[156:159], a[64:67], a[44:47], v[156:159]\n\t"
"s_waitcnt lgkmcnt(4)\n\t"
"v_mul_f32 v70, v64, v50\n\t"
"v_mul_f32 v71, v64, v51\n\t"
"v_fma_f32 v66, -v65, v51, v70\n\t"
"v_mfma_f32_16x16x32_bf16 v[160:163], a[68:71], a[32:35], v[160:163]\n\t"
"s_mov_b32 s42, 5308416\n\t"
"ds_read_b64_tr_b16 a[64:65], v17 offset:2048\n\t"
"ds_read_b64_tr_b16 a[66:67], v15 offset:2560\n\t"
"v_fma_f32 v67, v65, v50, v71\n\t"
"v_mfma_f32_16x16x32_bf16 v[164:167], a[68:71], a[36:39], v[164:167]\n\t"
"s_add_u32 m0, s86, 34816\n\t"
"v_cvt_pk_bf16_f32 v73, v64, v66\n\t"
"v_cvt_pk_bf16_f32 v77, v65, v67\n\t"
"v_mul_f32 v70, v66, v50\n\t"
"v_mfma_f32_16x16x32_bf16 v[168:171], a[68:71], a[40:43], v[168:171]\n\t"
"s_mov_b32 s43, 0x20000\n\t"
"buffer_load_dwordx4 v4, s[32:35], s53 offen lds\n\t"
"v_mul_f32 v71, v66, v51\n\t"
"v_fma_f32 v60, -v67, v51, v70\n\t"
"v_mfma_f32_16x16x32_bf16 v[172:175], a[68:71], a[44:47], v[172:175]\n\t"
"s_waitcnt lgkmcnt(4)\n\t"
"v_fma_f32 v61, v67, v50, v71\n\t"
"v_mul_f32 v70, v60, v50\n\t"
"v_mul_f32 v71, v60, v51\n\t"
"v_mfma_f32_16x16x32_bf16 v[176:179], a[72:75], a[32:35], v[176:179]\n\t"
"s_lshl3_add_u32 s8, s28, s26\n\t"
"ds_read_b64_tr_b16 a[68:69], v18 offset:2048\n\t"
"ds_read_b64_tr_b16 a[70:71], v16 offset:2560\n\t"
"v_fma_f32 v62, -v61, v51, v70\n\t"
"v_mfma_f32_16x16x32_bf16 v[180:183], a[72:75], a[36:39], v[180:183]\n\t"
"s_add_u32 m0, s86, 35840\n\t"
"v_fma_f32 v63, v61, v50, v71\n\t"
"v_cvt_pk_bf16_f32 v80, v60, v62\n\t"
"v_cvt_pk_bf16_f32 v32, v61, v63\n\t"
"v_mfma_f32_16x16x32_bf16 v[184:187], a[72:75], a[40:43], v[184:187]\n\t"
"s_lshl2_add_u32 s8, s8, s29\n\t"
"buffer_load_dwordx4 v5, s[32:35], s53 offen lds\n\t"
"v_mul_f32 v70, v62, v50\n\t"
"v_mul_f32 v71, v62, v51\n\t"
"v_mfma_f32_16x16x32_bf16 v[188:191], a[72:75], a[44:47], v[188:191]\n\t"
"s_waitcnt lgkmcnt(4)\n\t"
"v_fma_f32 v64, -v63, v51, v70\n\t"
"v_fma_f32 v65, v63, v50, v71\n\t"
"v_mul_f32 v70, v64, v50\n\t"
"v_mfma_f32_16x16x32_bf16 v[192:195], a[76:79], a[32:35], v[192:195]\n\t"
"s_mul_i32 s8, s8, 73728\n\t"
"ds_read_b64_tr_b16 a[72:73], v19 offset:0\n\t"
"ds_read_b64_tr_b16 a[74:75], v19 offset:256\n\t"
"v_mul_f32 v71, v64, v51\n\t"
"v_mfma_f32_16x16x32_bf16 v[196:199], a[76:79], a[36:39], v[196:199]\n\t"
"s_add_u32 m0, s86, 36864\n\t"
"v_fma_f32 v66, -v65, v51, v70\n\t"
"v_fma_f32 v67, v65, v50, v71\n\t"
"v_cvt_pk_bf16_f32 v81, v64, v66\n\t"
"v_mfma_f32_16x16x32_bf16 v[200:203], a[76:79], a[40:43], v[200:203]\n\t"
"s_add_u32 s4, s8, 8388608\n\t"
"buffer_load_dwordx4 v6, s[32:35], s53 offen lds\n\t"
"v_cvt_pk_bf16_f32 v33, v65, v67\n\t"
"v_mul_f32 v70, v66, v50\n\t"
"v_mfma_f32_16x16x32_bf16 v[204:207], a[76:79], a[44:47], v[204:207]\n\t"
"s_waitcnt lgkmcnt(4)\n\t"
"v_mul_f32 v71, v66, v51\n\t"
"v_fma_f32 v60, -v67, v51, v70\n\t"
"v_fma_f32 v61, v67, v50, v71\n\t"
"v_mfma_f32_16x16x32_bf16 v[208:211], a[64:67], a[32:35], v[208:211]\n\t"
"s_mul_i32 s8, s26, 36\n\t"
"ds_read_b64_tr_b16 a[76:77], v15 offset:10240\n\t"
"ds_read_b64_tr_b16 a[78:79], v17 offset:10752\n\t"
"v_mul_f32 v70, v60, v58\n\t"
"v_mfma_f32_16x16x32_bf16 v[212:215], a[64:67], a[36:39], v[212:215]\n\t"
"s_add_u32 s8, s8, s29\n\t"
"v_mul_f32 v71, v60, v59\n\t"
"v_fma_f32 v62, -v61, v59, v70\n\t"
"v_fma_f32 v63, v61, v58, v71\n\t"
"v_mfma_f32_16x16x32_bf16 v[216:219], a[64:67], a[40:43], v[216:219]\n\t"
"s_mul_i32 s8, s8, 18432\n\t"
"v_cvt_pk_bf16_f32 v74, v60, v62\n\t"
"v_cvt_pk_bf16_f32 v78, v61, v63\n\t"
"v_mul_f32 v70, v62, v52\n\t"
"v_mfma_f32_16x16x32_bf16 v[220:223], a[64:67], a[44:47], v[220:223]\n\t"
"s_waitcnt lgkmcnt(4)\n\t"
"v_mul_f32 v71, v62, v53\n\t"
"v_fma_f32 v64, -v63, v53, v70\n\t"
"v_fma_f32 v65, v63, v52, v71\n\t"
"v_mfma_f32_16x16x32_bf16 v[224:227], a[68:71], a[32:35], v[224:227]\n\t"
"s_lshl_b32 s9, s27, 8\n\t"
"ds_read_b64_tr_b16 a[64:65], v16 offset:10240\n\t"
"ds_read_b64_tr_b16 a[66:67], v18 offset:10752\n\t"
"v_mul_f32 v70, v64, v52\n\t"
"v_mfma_f32_16x16x32_bf16 v[228:231], a[68:71], a[36:39], v[228:231]\n\t"
"s_add_u32 m0, s86, 37888\n\t"
"v_mul_f32 v71, v64, v53\n\t"
"v_fma_f32 v66, -v65, v53, v70\n\t"
"v_fma_f32 v67, v65, v52, v71\n\t"
"v_mfma_f32_16x16x32_bf16 v[232:235], a[68:71], a[40:43], v[232:235]\n\t"
"s_add_u32 s5, s8, s9\n\t"
"buffer_load_dwordx4 v7, s[32:35], s53 offen lds\n\t"
"v_cvt_pk_bf16_f32 v75, v64, v66\n\t"
"v_cvt_pk_bf16_f32 v79, v65, v67\n\t"
"v_mfma_f32_16x16x32_bf16 v[236:239], a[68:71], a[44:47], v[236:239]\n\t"
"s_waitcnt lgkmcnt(4)\n\t"
"v_xor_b32 v68, 0, v102\n\t"
"v_mfma_f32_16x16x32_bf16 v[240:243], a[72:75], a[32:35], v[240:243]\n\t"
"s_add_u32 s8, s27, 0\n\t"
"ds_read_b64_tr_b16 a[68:69], v17 offset:10240\n\t"
"ds_read_b64_tr_b16 a[70:71], v15 offset:10752\n\t"
"ds_write_b128 v68, v[72:75]\n\t"
"v_mfma_f32_16x16x32_bf16 v[244:247], a[72:75], a[36:39], v[244:247]\n\t"
"s_add_u32 m0, s86, 38912\n\t"
"v_mfma_f32_16x16x32_bf16 v[248:251], a[72:75], a[40:43], v[248:251]\n\t"
"s_and_b32 s8, s8, 3\n\t"
"buffer_load_dwordx4 v8, s[32:35], s53 offen lds\n\t"
"ds_write_b128 v68, v[76:79] offset:128\n\t"
"v_mfma_f32_16x16x32_bf16 v[252:255], a[72:75], a[44:47], v[252:255]\n\t"
"s_waitcnt lgkmcnt(6)\n\t"
"v_mul_f32 v70, v66, v52\n\t"
"v_mul_f32 v71, v66, v53\n\t"
"v_fma_f32 v60, -v67, v53, v70\n\t"
"v_mfma_f32_16x16x32_bf16 v[112:115], a[76:79], a[48:51], v[112:115]\n\t"
"s_lshl1_add_u32 s8, s8, 0\n\t"
"ds_read_b64_tr_b16 a[72:73], v18 offset:10240\n\t"
"ds_read_b64_tr_b16 a[74:75], v16 offset:10752\n\t"
"v_fma_f32 v61, v67, v52, v71\n\t"
"v_mfma_f32_16x16x32_bf16 v[116:119], a[76:79], a[52:55], v[116:119]\n\t"
"s_add_u32 m0, s86, 39936\n\t"
"v_mul_f32 v70, v60, v52\n\t"
"v_mul_f32 v71, v60, v53\n\t"
"v_fma_f32 v62, -v61, v53, v70\n\t"
"v_mfma_f32_16x16x32_bf16 v[120:123], a[76:79], a[56:59], v[120:123]\n\t"
"s_mul_i32 s9, s8, 73728\n\t"
"buffer_load_dwordx4 v9, s[32:35], s53 offen lds\n\t"
"v_fma_f32 v63, v61, v52, v71\n\t"
"v_cvt_pk_bf16_f32 v82, v60, v62\n\t"
"v_mfma_f32_16x16x32_bf16 v[124:127], a[76:79], a[60:63], v[124:127]\n\t"
"s_waitcnt lgkmcnt(6)\n\t"
"v_cvt_pk_bf16_f32 v34, v61, v63\n\t"
"v_mul_f32 v70, v62, v58\n\t"
"v_mul_f32 v71, v62, v59\n\t"
"v_mfma_f32_16x16x32_bf16 v[128:131], a[64:67], a[48:51], v[128:131]\n\t"
"s_add_u32 s66, s5, s9\n\t"
"ds_read_b64_tr_b16 a[76:77], v15 offset:12288\n\t"
"ds_read_b64_tr_b16 a[78:79], v17 offset:12800\n\t"
"v_fma_f32 v64, -v63, v59, v70\n\t"
"v_mfma_f32_16x16x32_bf16 v[132:135], a[64:67], a[52:55], v[132:135]\n\t"
"s_lshl_b32 s8, s8, 13\n\t"
"v_fma_f32 v65, v63, v58, v71\n\t"
"v_mul_f32 v70, v64, v54\n\t"
"v_mul_f32 v71, v64, v55\n\t"
"v_mfma_f32_16x16x32_bf16 v[136:139], a[64:67], a[56:59], v[136:139]\n\t"
"s_add_u32 s82, s4, s8\n\t"
"v_fma_f32 v66, -v65, v55, v70\n\t"
"v_fma_f32 v67, v65, v54, v71\n\t"
"v_cvt_pk_bf16_f32 v83, v64, v66\n\t"
"v_mfma_f32_16x16x32_bf16 v[140:143], a[64:67], a[60:63], v[140:143]\n\t"
"s_waitcnt vmcnt(14)\n\t"
"s_waitcnt lgkmcnt(6)\n\t"
"ds_read_b128 a[0:3], v10 offset:0\n\t"
"v_cvt_pk_bf16_f32 v35, v65, v67\n\t"
"v_xor_b32 v68, 16, v102\n\t"
"v_mfma_f32_16x16x32_bf16 v[144:147], a[68:71], a[48:51], v[144:147]\n\t"
"s_add_u32 s8, s27, 0\n\t"
"ds_read_b64_tr_b16 a[64:65], v16 offset:12288\n\t"
"ds_read_b64_tr_b16 a[66:67], v18 offset:12800\n\t"
"v_mfma_f32_16x16x32_bf16 v[148:151], a[68:71], a[52:55], v[148:151]\n\t"
"s_add_u32 m0, s87, 40960\n\t"
"ds_write_b128 v68, v[80:83]\n\t"
"v_mfma_f32_16x16x32_bf16 v[152:155], a[68:71], a[56:59], v[152:155]\n\t"
"s_and_b32 s8, s8, 3\n\t"
"buffer_load_dwordx4 v12, s[36:39], s55 offen offset:0 lds\n\t"
"ds_write_b128 v68, v[32:35] offset:128\n\t"
"v_mfma_f32_16x16x32_bf16 v[156:159], a[68:71], a[60:63], v[156:159]\n\t"
"s_waitcnt lgkmcnt(7)\n\t"
"ds_read_b128 a[4:7], v10 offset:2048\n\t"
"v_mul_f32 v70, v66, v54\n\t"
"v_mfma_f32_16x16x32_bf16 v[160:163], a[72:75], a[48:51], v[160:163]\n\t"
"s_lshl1_add_u32 s8, s8, 1\n\t"
"ds_read_b64_tr_b16 a[68:69], v17 offset:12288\n\t"
"ds_read_b64_tr_b16 a[70:71], v15 offset:12800\n\t"
"v_mul_f32 v71, v66, v55\n\t"
"v_mfma_f32_16x16x32_bf16 v[164:167], a[72:75], a[52:55], v[164:167]\n\t"
"s_add_u32 m0, s87, 41984\n\t"
"v_fma_f32 v60, -v67, v55, v70\n\t"
"v_fma_f32 v61, v67, v54, v71\n\t"
"v_mul_f32 v70, v60, v58\n\t"
"v_mfma_f32_16x16x32_bf16 v[168:171], a[72:75], a[56:59], v[168:171]\n\t"
"s_mul_i32 s9, s8, 73728\n\t"
"buffer_load_dwordx4 v13, s[36:39], s55 offen offset:0 lds\n\t"
"v_mul_f32 v71, v60, v59\n\t"
"v_fma_f32 v62, -v61, v59, v70\n\t"
"v_mfma_f32_16x16x32_bf16 v[172:175], a[72:75], a[60:63], v[172:175]\n\t"
"s_waitcnt lgkmcnt(8)\n\t"
"ds_read_b128 a[8:11], v10 offset:4096\n\t"
"v_fma_f32 v63, v61, v58, v71\n\t"
"v_cvt_pk_bf16_f32 v74, v60, v62\n\t"
"v_mfma_f32_16x16x32_bf16 v[176:179], a[76:79], a[48:51], v[176:179]\n\t"
"ds_read_b64_tr_b16 a[72:73], v18 offset:12288\n\t"
"ds_read_b64_tr_b16 a[74:75], v16 offset:12800\n\t"
"v_cvt_pk_bf16_f32 v78, v61, v63\n\t"
"v_mfma_f32_16x16x32_bf16 v[180:183], a[76:79], a[52:55], v[180:183]\n\t"
"s_add_u32 m0, s87, 42880\n\t"
"v_mul_f32 v70, v62, v54\n\t"
"v_mul_f32 v71, v62, v55\n\t"
"v_fma_f32 v64, -v63, v55, v70\n\t"
"v_mfma_f32_16x16x32_bf16 v[184:187], a[76:79], a[56:59], v[184:187]\n\t"
"buffer_load_dwordx4 v12, s[36:39], s55 offen offset:128 lds\n\t"
"v_fma_f32 v65, v63, v54, v71\n\t"
"v_mul_f32 v70, v64, v54\n\t"
"v_mfma_f32_16x16x32_bf16 v[188:191], a[76:79], a[60:63], v[188:191]\n\t"
"s_waitcnt lgkmcnt(8)\n\t"
"ds_read_b128 a[12:15], v10 offset:6144\n\t"
"v_mul_f32 v71, v64, v55\n\t"
"v_fma_f32 v66, -v65, v55, v70\n\t"
"v_mfma_f32_16x16x32_bf16 v[192:195], a[64:67], a[48:51], v[192:195]\n\t"
"ds_read_b64_tr_b16 a[76:77], v19 offset:10240\n\t"
"ds_read_b64_tr_b16 a[78:79], v19 offset:10496\n\t"
"v_fma_f32 v67, v65, v54, v71\n\t"
"v_mfma_f32_16x16x32_bf16 v[196:199], a[64:67], a[52:55], v[196:199]\n\t"
"v_cvt_pk_bf16_f32 v75, v64, v66\n\t"
"v_cvt_pk_bf16_f32 v79, v65, v67\n\t"
"v_mul_f32 v70, v66, v58\n\t"
"v_mfma_f32_16x16x32_bf16 v[200:203], a[64:67], a[56:59], v[200:203]\n\t"
"v_mul_f32 v71, v66, v59\n\t"
"v_fma_f32 v60, -v67, v59, v70\n\t"
"v_fma_f32 v61, v67, v58, v71\n\t"
"v_mfma_f32_16x16x32_bf16 v[204:207], a[64:67], a[60:63], v[204:207]\n\t"
"s_waitcnt lgkmcnt(6)\n\t"
"ds_read_b128 a[16:19], v11 offset:0\n\t"
"v_mul_f32 v70, v60, v56\n\t"
"v_mul_f32 v71, v60, v57\n\t"
"v_mfma_f32_16x16x32_bf16 v[208:211], a[68:71], a[48:51], v[208:211]\n\t"
"v_fma_f32 v62, -v61, v57, v70\n\t"
"v_fma_f32 v63, v61, v56, v71\n\t"
"v_cvt_pk_bf16_f32 v72, v60, v62\n\t"
"v_mfma_f32_16x16x32_bf16 v[212:215], a[68:71], a[52:55], v[212:215]\n\t"
"s_add_u32 m0, s87, 43904\n\t"
"v_cvt_pk_bf16_f32 v76, v61, v63\n\t"
"v_mul_f32 v70, v62, v58\n\t"
"v_mul_f32 v71, v62, v59\n\t"
"v_mfma_f32_16x16x32_bf16 v[216:219], a[68:71], a[56:59], v[216:219]\n\t"
"buffer_load_dwordx4 v13, s[36:39], s55 offen offset:128 lds\n\t"
"v_fma_f32 v64, -v63, v59, v70\n\t"
"v_fma_f32 v65, v63, v58, v71\n\t"
"v_mfma_f32_16x16x32_bf16 v[220:223], a[68:71], a[60:63], v[220:223]\n\t"
"s_waitcnt vmcnt(13) lgkmcnt(1)\n\t"
"s_barrier\n\t"
"ds_read_b128 a[20:23], v11 offset:2048\n\t"
"v_mul_f32 v70, v64, v56\n\t"
"v_mul_f32 v71, v64, v57\n\t"
"v_mfma_f32_16x16x32_bf16 v[224:227], a[72:75], a[48:51], v[224:227]\n\t"
"ds_read_b64_tr_b16 a[64:65], v15 offset:20480\n\t"
"ds_read_b64_tr_b16 a[66:67], v17 offset:20992\n\t"
"v_fma_f32 v66, -v65, v57, v70\n\t"
"v_mfma_f32_16x16x32_bf16 v[228:231], a[72:75], a[52:55], v[228:231]\n\t"
"s_add_u32 m0, s87, 45056\n\t"
"ds_read_b64_tr_b16 a[68:69], v16 offset:20480\n\t"
"ds_read_b64_tr_b16 a[70:71], v18 offset:20992\n\t"
"v_fma_f32 v67, v65, v56, v71\n\t"
"v_mfma_f32_16x16x32_bf16 v[232:235], a[72:75], a[56:59], v[232:235]\n\t"
"buffer_load_dwordx4 v14, s[36:39], s55 offen lds\n\t"
"v_cvt_pk_bf16_f32 v73, v64, v66\n\t"
"v_cvt_pk_bf16_f32 v77, v65, v67\n\t"
"v_mfma_f32_16x16x32_bf16 v[236:239], a[72:75], a[60:63], v[236:239]\n\t"
"ds_read_b128 a[24:27], v11 offset:4096\n\t"
"v_xor_b32 v68, 32, v102\n\t"
"v_mfma_f32_16x16x32_bf16 v[240:243], a[76:79], a[48:51], v[240:243]\n\t"
"ds_read_b64_tr_b16 a[72:73], v17 offset:20480\n\t"
"ds_read_b64_tr_b16 a[74:75], v15 offset:20992\n\t"
"ds_write_b128 v68, v[72:75]\n\t"
"v_mfma_f32_16x16x32_bf16 v[244:247], a[76:79], a[52:55], v[244:247]\n\t"
"v_mfma_f32_16x16x32_bf16 v[248:251], a[76:79], a[56:59], v[248:251]\n\t"
"ds_write_b128 v68, v[76:79] offset:128\n\t"
"v_mul_f32 v70, v66, v58\n\t"
"v_mfma_f32_16x16x32_bf16 v[252:255], a[76:79], a[60:63], v[252:255]\n\t"
"ds_read_b128 a[28:31], v11 offset:6144\n\t"
"v_mul_f32 v71, v66, v59\n\t"
"v_fma_f32 v60, -v67, v59, v70\n\t"
"v_fma_f32 v61, v67, v58, v71\n\t"
"v_mul_f32 v70, v60, v56\n\t"
"v_mul_f32 v71, v60, v57\n\t"
"s_waitcnt lgkmcnt(8)\n\t"
"s_waitcnt lgkmcnt(0)\n\t"
"v_mfma_f32_16x16x32_bf16 v[112:115], a[64:67], a[0:3], v[112:115]\n\t"
"s_add_u32 s6, s51, 7\n\t"
"ds_read_b64_tr_b16 a[76:77], v18 offset:20480\n\t"
"ds_read_b64_tr_b16 a[78:79], v16 offset:20992\n\t"
"v_fma_f32 v62, -v61, v57, v70\n\t"
"v_mfma_f32_16x16x32_bf16 v[116:119], a[64:67], a[4:7], v[116:119]\n\t"
"s_add_u32 m0, s86, 0\n\t"
"v_lshlrev_b32 v36, 7, v0\n\t"
"v_fma_f32 v63, v61, v56, v71\n\t"
"v_cvt_pk_bf16_f32 v80, v60, v62\n\t"
"v_lshlrev_b32 v28, 4, v0\n\t"
"v_mfma_f32_16x16x32_bf16 v[120:123], a[64:67], a[8:11], v[120:123]\n\t"
"s_sub_u32 s7, s6, 9\n\t"
"buffer_load_dwordx4 v2, s[32:35], s52 offen lds\n\t"
"v_cvt_pk_bf16_f32 v32, v61, v63\n\t"
"v_mul_f32 v70, v62, v58\n\t"
"v_lshlrev_b32 v31, 3, v0\n\t"
"v_mfma_f32_16x16x32_bf16 v[124:127], a[64:67], a[12:15], v[124:127]\n\t"
"s_min_u32 s6, s6, s7\n\t"
"v_mul_f32 v71, v62, v59\n\t"
"v_fma_f32 v64, -v63, v59, v70\n\t"
"v_fma_f32 v65, v63, v58, v71\n\t"
"v_add_u32 v29, 4096, v28\n\t"
"v_mfma_f32_16x16x32_bf16 v[128:131], a[68:71], a[0:3], v[128:131]\n\t"
"s_lshl_b32 s7, s6, 7\n\t"
"ds_read_b64_tr_b16 a[64:65], v15 offset:22528\n\t"
"ds_read_b64_tr_b16 a[66:67], v17 offset:23040\n\t"
"v_mul_f32 v70, v64, v56\n\t"
"v_mul_f32 v71, v64, v57\n\t"
"v_mfma_f32_16x16x32_bf16 v[132:135], a[68:71], a[4:7], v[132:135]\n\t"
"s_add_u32 m0, s86, 1024\n\t"
"s_and_b32 s61, s27, 1\n\t"
"v_fma_f32 v66, -v65, v57, v70\n\t"
"v_fma_f32 v67, v65, v56, v71\n\t"
"v_mfma_f32_16x16x32_bf16 v[136:139], a[68:71], a[8:11], v[136:139]\n\t"
"s_add_u32 s53, s48, s7\n\t"
"buffer_load_dwordx4 v3, s[32:35], s52 offen lds\n\t"
"s_lshl_b32 s62, s61, 9\n\t"
"v_cvt_pk_bf16_f32 v81, v64, v66\n\t"
"v_cvt_pk_bf16_f32 v33, v65, v67\n\t"
"v_mul_f32 v70, v66, v56\n\t"
"v_mfma_f32_16x16x32_bf16 v[140:143], a[68:71], a[12:15], v[140:143]\n\t"
"s_mul_i32 s6, s6, 589824\n\t"
"v_add_u32 v30, s62, v31\n\t"
"v_mul_f32 v71, v66, v57\n\t"
"v_fma_f32 v60, -v67, v57, v70\n\t"
"v_and_b32 v49, 15, v0\n\t"
"v_mfma_f32_16x16x32_bf16 v[144:147], a[72:75], a[0:3], v[144:147]\n\t"
"s_add_u32 s55, s49, s6\n\t"
"ds_read_b64_tr_b16 a[68:69], v16 offset:22528\n\t"
"ds_read_b64_tr_b16 a[70:71], v18 offset:23040\n\t"
"v_fma_f32 v61, v67, v56, v71\n\t"
"v_mul_f32 v70, v60, v56\n\t"
"v_mfma_f32_16x16x32_bf16 v[148:151], a[72:75], a[4:7], v[148:151]\n\t"
"s_add_u32 s67, s5, s9\n\t"
"v_mul_f32 v71, v60, v57\n\t"
"v_lshrrev_b32 v45, 4, v0\n\t"
"v_fma_f32 v62, -v61, v57, v70\n\t"
"v_fma_f32 v63, v61, v56, v71\n\t"
"v_mfma_f32_16x16x32_bf16 v[152:155], a[72:75], a[8:11], v[152:155]\n\t"
"s_lshl_b32 s8, s8, 13\n\t"
"v_lshlrev_b32 v42, 3, v45\n\t"
"v_cvt_pk_bf16_f32 v82, v60, v62\n\t"
"v_cvt_pk_bf16_f32 v34, v61, v63\n\t"
"v_sub_u32 v42, v42, v49\n\t"
"v_mfma_f32_16x16x32_bf16 v[156:159], a[72:75], a[12:15], v[156:159]\n\t"
"s_waitcnt lgkmcnt(4)\n\t"
"v_mul_f32 v70, v62, v56\n\t"
"v_mul_f32 v71, v62, v57\n\t"
"v_fma_f32 v64, -v63, v57, v70\n\t"
"v_mfma_f32_16x16x32_bf16 v[160:163], a[76:79], a[0:3], v[160:163]\n\t"
"s_add_u32 s83, s4, s8\n\t"
"ds_read_b64_tr_b16 a[72:73], v17 offset:22528\n\t"
"ds_read_b64_tr_b16 a[74:75], v15 offset:23040\n\t"
"v_and_b32 v43, 1, v0\n\t"
"v_fma_f32 v65, v63, v56, v71\n\t"
"v_mfma_f32_16x16x32_bf16 v[164:167], a[76:79], a[4:7], v[164:167]\n\t"
"s_add_u32 m0, s86, 2048\n\t"
"v_mul_f32 v70, v64, v56\n\t"
"v_mul_u32_u24 v43, 4162, v43\n\t"
"v_mul_f32 v71, v64, v57\n\t"
"v_fma_f32 v66, -v65, v57, v70\n\t"
"v_mfma_f32_16x16x32_bf16 v[168:171], a[76:79], a[8:11], v[168:171]\n\t"
"s_add_u32 s68, s5, 589824\n\t"
"buffer_load_dwordx4 v4, s[32:35], s52 offen lds\n\t"
"v_lshlrev_b32 v42, 1, v42\n\t"
"v_fma_f32 v67, v65, v56, v71\n\t"
"v_cvt_pk_bf16_f32 v83, v64, v66\n\t"
"v_mfma_f32_16x16x32_bf16 v[172:175], a[76:79], a[12:15], v[172:175]\n\t"
"s_waitcnt lgkmcnt(4)\n\t"
"v_cvt_pk_bf16_f32 v35, v65, v67\n\t"
"v_sub_u32 v42, v42, v43\n\t"
"v_xor_b32 v68, 48, v102\n\t"
"v_mfma_f32_16x16x32_bf16 v[176:179], a[64:67], a[0:3], v[176:179]\n\t"
"s_add_u32 s75, s4, 65536\n\t"
"ds_read_b64_tr_b16 a[76:77], v18 offset:22528\n\t"
"ds_read_b64_tr_b16 a[78:79], v16 offset:23040\n\t"
"ds_write_b128 v68, v[80:83]\n\t"
"v_mfma_f32_16x16x32_bf16 v[180:183], a[64:67], a[4:7], v[180:183]\n\t"
"s_add_u32 m0, s86, 3072\n\t"
"s_add_u32 s62, s57, 4192\n\t"
"v_mfma_f32_16x16x32_bf16 v[184:187], a[64:67], a[8:11], v[184:187]\n\t"
"s_add_u32 s8, s27, 1\n\t"
"buffer_load_dwordx4 v5, s[32:35], s52 offen lds\n\t"
"ds_write_b128 v68, v[32:35] offset:128\n\t"
"v_mfma_f32_16x16x32_bf16 v[188:191], a[64:67], a[12:15], v[188:191]\n\t"
"s_waitcnt lgkmcnt(6)\n\t"
"v_add_u32 v41, s62, v42\n\t"
"v_mfma_f32_16x16x32_bf16 v[192:195], a[68:71], a[0:3], v[192:195]\n\t"
"s_and_b32 s8, s8, 3\n\t"
"ds_read_b64_tr_b16 a[64:65], v19 offset:20480\n\t"
"ds_read_b64_tr_b16 a[66:67], v19 offset:20736\n\t"
"ds_read_b64_tr_b16 a[112:113], v103 offset:0\n\t"
"ds_read_b64_tr_b16 a[114:115], v104 offset:512\n\t"
"v_mfma_f32_16x16x32_bf16 v[196:199], a[68:71], a[4:7], v[196:199]\n\t"
"s_add_u32 m0, s86, 4096\n\t"
"ds_read_b64_tr_b16 a[116:117], v105 offset:0\n\t"
"v_add_u32 v40, 1024, v41\n\t"
"v_mfma_f32_16x16x32_bf16 v[200:203], a[68:71], a[8:11], v[200:203]\n\t"
"s_lshl1_add_u32 s8, s8, 0\n\t"
"buffer_load_dwordx4 v6, s[32:35], s52 offen lds\n\t"
"ds_read_b64_tr_b16 a[118:119], v106 offset:512\n\t"
"ds_read_b64_tr_b16 a[120:121], v103 offset:2048\n\t"
"v_add_u32 v39, 2048, v41\n\t"
"v_mfma_f32_16x16x32_bf16 v[204:207], a[68:71], a[12:15], v[204:207]\n\t"
"s_waitcnt lgkmcnt(11)\n\t"
"ds_read_b64_tr_b16 a[122:123], v104 offset:2560\n\t"
"ds_read_b64_tr_b16 a[124:125], v105 offset:2048\n\t"
"v_add_u32 v38, 3072, v41\n\t"
"v_mfma_f32_16x16x32_bf16 v[208:211], a[72:75], a[0:3], v[208:211]\n\t"
"s_mul_i32 s9, s8, 73728\n\t"
"ds_read_b64_tr_b16 a[68:69], v15 offset:30720\n\t"
"ds_read_b64_tr_b16 a[70:71], v17 offset:31232\n\t"
"ds_read_b64_tr_b16 a[126:127], v106 offset:2560\n\t"
"v_lshlrev_b32 v68, 16, v87\n\t"
"v_mfma_f32_16x16x32_bf16 v[212:215], a[72:75], a[4:7], v[212:215]\n\t"
"s_add_u32 s69, s5, s9\n\t"
"v_lshlrev_b32 v69, 16, v89\n\t"
"v_lshlrev_b32 v49, 5, v49\n\t"
"v_mul_f32 v68, s88, v68\n\t"
"v_lshlrev_b32 v60, 16, v91\n\t"
"v_mfma_f32_16x16x32_bf16 v[216:219], a[72:75], a[8:11], v[216:219]\n\t"
"s_lshl_b32 s8, s8, 13\n\t"
"v_lshl_add_u32 v42, v45, 4, v49\n\t"
"v_exp_f32 v68, v68\n\t"
"v_lshlrev_b32 v61, 16, v93\n\t"
"v_mfma_f32_16x16x32_bf16 v[220:223], a[72:75], a[12:15], v[220:223]\n\t"
"s_waitcnt lgkmcnt(14)\n\t"
"v_lshl_add_u32 v45, v45, 3, v49\n\t"
"v_cvt_pk_bf16_f32 v68, v68, v68\n\t"
"v_mul_f32 v69, v95, v69\n\t"
"v_lshlrev_b32 v68, 16, v68\n\t"
"v_mfma_f32_16x16x32_bf16 v[224:227], a[76:79], a[0:3], v[224:227]\n\t"
"s_add_u32 s76, s4, s8\n\t"
"ds_read_b64_tr_b16 a[72:73], v16 offset:30720\n\t"
"ds_read_b64_tr_b16 a[74:75], v18 offset:31232\n\t"
"s_add_u32 s62, s58, 32\n\t"
"v_fma_f32 v60, v60, v96, v97\n\t"
"v_mfma_f32_16x16x32_bf16 v[228:231], a[76:79], a[4:7], v[228:231]\n\t"
"s_add_u32 m0, s86, 5120\n\t"
"s_waitcnt lgkmcnt(8)\n\t"
"v_add_u32 v42, s62, v42\n\t"
"v_mfma_f32_16x16x32_bf16 v[232:235], a[76:79], a[8:11], v[232:235]\n\t"
"s_add_u32 s8, s27, 2\n\t"
"buffer_load_dwordx4 v7, s[32:35], s52 offen lds\n\t"
"v_mfma_f32_32x32x16_bf16 a[128:143], a[112:115], a[116:119], a[128:143]\n\t"
"ds_read_b64_tr_b16 a[112:113], v103 offset:4096\n\t"
"v_mfma_f32_16x16x32_bf16 v[236:239], a[76:79], a[12:15], v[236:239]\n\t"
"s_and_b32 s8, s8, 3\n\t"
"ds_read_b64_tr_b16 a[114:115], v104 offset:4608\n\t"
"s_mul_i32 s62, s56, 4224\n\t"
"ds_read_b64_tr_b16 a[116:117], v105 offset:4096\n\t"
"ds_read_b64_tr_b16 a[118:119], v106 offset:4608\n\t"
"s_add_u32 s62, s62, 5767168\n\t"
"v_mul_f32 v68, v94, v68\n\t"
"v_mfma_f32_16x16x32_bf16 v[240:243], a[64:67], a[0:3], v[240:243]\n\t"
"s_lshl1_add_u32 s8, s8, 0\n\t"
"ds_read_b64_tr_b16 a[76:77], v17 offset:30720\n\t"
"ds_read_b64_tr_b16 a[78:79], v15 offset:31232\n\t"
"v_mul_f32 v61, -v96, v61\n\t"
"v_mfma_f32_16x16x32_bf16 v[244:247], a[64:67], a[4:7], v[244:247]\n\t"
"s_add_u32 m0, s86, 6144\n\t"
"s_cmp_lg_u64 s[98:99], 0\n\t"
"s_cselect_b32 s62, s62, 2146435072\n\t"
"v_mul_f32 v72, 0x41000000, v68\n\t"
"v_mul_f32 v73, 0x41000000, v69\n\t"
"v_fract_f32 v71, v73\n\t"
"v_add_u32 v45, s62, v45\n\t"
"v_mfma_f32_16x16x32_bf16 v[248:251], a[64:67], a[8:11], v[248:251]\n\t"
"s_mul_i32 s9, s8, 73728\n\t"
"buffer_load_dwordx4 v8, s[32:35], s52 offen lds\n\t"
"v_exp_f32 v70, v72\n\t"
"v_mfma_f32_16x16x32_bf16 v[252:255], a[64:67], a[12:15], v[252:255]\n\t"
"s_waitcnt lgkmcnt(9)\n\t"
"v_cos_f32 v56, v71\n\t"
"v_add_u32 v43, s59, v28\n\t"
"v_mfma_f32_16x16x32_bf16 v[112:115], a[68:71], a[16:19], v[112:115]\n\t"
"s_add_u32 s70, s5, s9\n\t"
"ds_read_b64_tr_b16 a[64:65], v18 offset:30720\n\t"
"ds_read_b64_tr_b16 a[66:67], v16 offset:31232\n\t"
"v_sin_f32 v57, v71\n\t"
"v_mfma_f32_16x16x32_bf16 v[116:119], a[68:71], a[20:23], v[116:119]\n\t"
"s_add_u32 m0, s86, 7168\n\t"
"v_mul_f32 v56, v70, v56\n\t"
"v_mfma_f32_16x16x32_bf16 v[120:123], a[68:71], a[24:27], v[120:123]\n\t"
"s_lshl_b32 s8, s8, 13\n\t"
"buffer_load_dwordx4 v9, s[32:35], s52 offen lds\n\t"
"s_add_u32 s62, s58, 512\n\t"
"v_mul_f32 v57, v70, v57\n\t"
"v_mfma_f32_16x16x32_bf16 v[124:127], a[68:71], a[28:31], v[124:127]\n\t"
"s_waitcnt lgkmcnt(8)\n\t"
"v_mfma_f32_32x32x16_bf16 a[128:143], a[120:123], a[124:127], a[128:143]\n\t"
"v_add_u32 v44, s62, v28\n\t"
"v_mfma_f32_16x16x32_bf16 v[128:131], a[72:75], a[16:19], v[128:131]\n\t"
"s_add_u32 s77, s4, s8\n\t"
"ds_read_b64_tr_b16 a[68:69], v15 offset:32768\n\t"
"ds_read_b64_tr_b16 a[70:71], v17 offset:33280\n\t"
"ds_read_b64_tr_b16 a[120:121], v103 offset:6144\n\t"
"ds_read_b64_tr_b16 a[122:123], v104 offset:6656\n\t"
"v_mfma_f32_16x16x32_bf16 v[132:135], a[72:75], a[20:23], v[132:135]\n\t"
"s_add_u32 s8, s27, 3\n\t"
"v_add_u32 v49, s58, v31\n\t"
"v_mfma_f32_16x16x32_bf16 v[136:139], a[72:75], a[24:27], v[136:139]\n\t"
"s_and_b32 s8, s8, 3\n\t"
"ds_read_b64_tr_b16 a[124:125], v105 offset:6144\n\t"
"ds_read_b64_tr_b16 a[126:127], v106 offset:6656\n\t"
"v_lshrrev_b32 v24, 3, v0\n\t"
"v_mul_f32 v72, v100, v68\n\t"
"v_mfma_f32_16x16x32_bf16 v[140:143], a[72:75], a[28:31], v[140:143]\n\t"
"s_waitcnt vmcnt(13)\n\t"
"s_waitcnt lgkmcnt(8)\n\t"
"ds_read_b128 a[32:35], v10 offset:32768\n\t"
"v_mul_f32 v73, v100, v69\n\t"
"v_fract_f32 v71, v73\n\t"
"v_and_b32 v25, 7, v0\n\t"
"v_mfma_f32_16x16x32_bf16 v[144:147], a[76:79], a[16:19], v[144:147]\n\t"
"s_lshl1_add_u32 s8, s8, 0\n\t"
"ds_read_b64_tr_b16 a[72:73], v16 offset:32768\n\t"
"ds_read_b64_tr_b16 a[74:75], v18 offset:33280\n\t"
"v_exp_f32 v70, v72\n\t"
"v_mfma_f32_16x16x32_bf16 v[148:151], a[76:79], a[20:23], v[148:151]\n\t"
"s_add_u32 m0, s87, 0\n\t"
"v_cos_f32 v58, v71\n\t"
"v_lshrrev_b32 v26, 4, v0\n\t"
"v_mfma_f32_16x16x32_bf16 v[152:155], a[76:79], a[24:27], v[152:155]\n\t"
"s_mul_i32 s9, s8, 73728\n\t"
"buffer_load_dwordx4 v12, s[36:39], s54 offen offset:0 lds\n\t"
"v_sin_f32 v59, v71\n\t"
"v_mul_f32 v58, v70, v58\n\t"
"v_mfma_f32_16x16x32_bf16 v[156:159], a[76:79], a[28:31], v[156:159]\n\t"
"s_waitcnt lgkmcnt(9)\n\t"
"ds_read_b128 a[36:39], v10 offset:34816\n\t"
"v_lshlrev_b32 v25, 4, v25\n\t"
"v_mul_f32 v59, v70, v59\n\t"
"v_mul_f32 v72, v98, v68\n\t"
"v_mfma_f32_16x16x32_bf16 v[160:163], a[64:67], a[16:19], v[160:163]\n\t"
"s_add_u32 s71, s5, s9\n\t"
"ds_read_b64_tr_b16 a[76:77], v17 offset:32768\n\t"
"ds_read_b64_tr_b16 a[78:79], v15 offset:33280\n\t"
"v_mul_f32 v73, v98, v69\n\t"
"v_lshlrev_b32 v27, 5, v26\n\t"
"v_mfma_f32_16x16x32_bf16 v[164:167], a[64:67], a[20:23], v[164:167]\n\t"
"s_add_u32 m0, s87, 1024\n\t"
"v_mfma_f32_32x32x16_bf16 a[128:143], a[112:115], a[116:119], a[128:143]\n\t"
"v_xor_b32 v25, v25, v27\n\t"
"v_fract_f32 v71, v73\n\t"
"v_mfma_f32_16x16x32_bf16 v[168:171], a[64:67], a[24:27], v[168:171]\n\t"
"s_lshl_b32 s8, s8, 13\n\t"
"buffer_load_dwordx4 v13, s[36:39], s54 offen offset:0 lds\n\t"
"v_exp_f32 v70, v72\n\t"
"v_mfma_f32_16x16x32_bf16 v[172:175], a[64:67], a[28:31], v[172:175]\n\t"
"s_waitcnt lgkmcnt(10)\n\t"
"ds_read_b128 a[40:43], v10 offset:36864\n\t"
"v_cos_f32 v50, v71\n\t"
"v_mfma_f32_16x16x32_bf16 v[176:179], a[68:71], a[16:19], v[176:179]\n\t"
"ds_read_b64_tr_b16 a[64:65], v18 offset:32768\n\t"
"ds_read_b64_tr_b16 a[66:67], v16 offset:33280\n\t"
"s_mov_b32 s63, 1152\n\t"
"v_sin_f32 v51, v71\n\t"
"v_mfma_f32_16x16x32_bf16 v[180:183], a[68:71], a[20:23], v[180:183]\n\t"
"s_add_u32 m0, s87, 1920\n\t"
"v_mul_f32 v50, v70, v50\n\t"
"v_mad_u32_u24 v20, v24, s63, v25\n\t"
"v_mul_f32 v51, v70, v51\n\t"
"v_mfma_f32_16x16x32_bf16 v[184:187], a[68:71], a[24:27], v[184:187]\n\t"
"buffer_load_dwordx4 v12, s[36:39], s54 offen offset:128 lds\n\t"
"s_waitcnt lgkmcnt(9)\n\t"
"v_add_u32 v21, 9216, v20\n\t"
"v_mfma_f32_16x16x32_bf16 v[188:191], a[68:71], a[28:31], v[188:191]\n\t"
"s_waitcnt lgkmcnt(6)\n\t"
"ds_read_b128 a[44:47], v10 offset:38912\n\t"
"v_mfma_f32_32x32x16_bf16 a[128:143], a[120:123], a[124:127], a[128:143]\n\t"
"v_mul_f32 v72, v99, v72\n\t"
"v_mfma_f32_16x16x32_bf16 v[192:195], a[72:75], a[16:19], v[192:195]\n\t"
"ds_read_b64_tr_b16 a[68:69], v19 offset:30720\n\t"
"ds_read_b64_tr_b16 a[70:71], v19 offset:30976\n\t"
"v_mul_f32 v73, v99, v73\n\t"
"v_lshrrev_b32 v24, 2, v0\n\t"
"v_mfma_f32_16x16x32_bf16 v[196:199], a[72:75], a[20:23], v[196:199]\n\t"
"v_fract_f32 v71, v73\n\t"
"v_exp_f32 v70, v72\n\t"
"v_and_b32 v25, 3, v0\n\t"
"v_mfma_f32_16x16x32_bf16 v[200:203], a[72:75], a[24:27], v[200:203]\n\t"
"v_cos_f32 v52, v71\n\t"
"v_sin_f32 v53, v71\n\t"
"v_mfma_f32_16x16x32_bf16 v[204:207], a[72:75], a[28:31], v[204:207]\n\t"
"s_waitcnt lgkmcnt(6)\n\t"
"ds_read_b128 a[48:51], v11 offset:32768\n\t"
"v_and_b32 v27, 32, v27\n\t"
"v_mul_f32 v52, v70, v52\n\t"
"v_mul_f32 v53, v70, v53\n\t"
"v_mfma_f32_16x16x32_bf16 v[208:211], a[76:79], a[16:19], v[208:211]\n\t"
"v_mul_f32 v72, v99, v72\n\t"
"v_lshlrev_b32 v26, 4, v25\n\t"
"v_mul_f32 v73, v99, v73\n\t"
"v_mfma_f32_16x16x32_bf16 v[212:215], a[76:79], a[20:23], v[212:215]\n\t"
"s_add_u32 m0, s87, 2944\n\t"
"v_fract_f32 v71, v73\n\t"
"v_xor_b32 v26, v26, v27\n\t"
"v_exp_f32 v70, v72\n\t"
"v_mfma_f32_16x16x32_bf16 v[216:219], a[76:79], a[24:27], v[216:219]\n\t"
"buffer_load_dwordx4 v13, s[36:39], s54 offen offset:128 lds\n\t"
"v_cos_f32 v54, v71\n\t"
"v_mad_u32_u24 v22, v24, s63, v26\n\t"
"v_mfma_f32_16x16x32_bf16 v[220:223], a[76:79], a[28:31], v[220:223]\n\t"
"s_waitcnt vmcnt(12) lgkmcnt(1)\n\t"
"s_barrier\n\t"
"ds_read_b128 a[52:55], v11 offset:34816\n\t"
"v_sin_f32 v55, v71\n\t"
"v_mul_f32 v54, v70, v54\n\t"
"v_mfma_f32_16x16x32_bf16 v[224:227], a[64:67], a[16:19], v[224:227]\n\t"
"ds_read_b64_tr_b16 a[72:73], v15 offset:40960\n\t"
"ds_read_b64_tr_b16 a[74:75], v17 offset:41472\n\t"
"v_mul_f32 v55, v70, v55\n\t"
"v_add_u32 v22, s60, v22\n\t"
"v_mfma_f32_16x16x32_bf16 v[228:231], a[64:67], a[20:23], v[228:231]\n\t"
"s_add_u32 m0, s87, 4096\n\t"
"ds_read_b64_tr_b16 a[76:77], v16 offset:40960\n\t"
"ds_read_b64_tr_b16 a[78:79], v18 offset:41472\n\t"
"v_mul_f32 v70, v60, v50\n\t"
"v_mfma_f32_16x16x32_bf16 v[232:235], a[64:67], a[24:27], v[232:235]\n\t"
"buffer_load_dwordx4 v14, s[36:39], s54 offen lds\n\t"
"v_mul_f32 v71, v60, v51\n\t"
"v_lshlrev_b32 v25, 3, v25\n\t"
"v_fma_f32 v62, -v61, v51, v70\n\t"
"v_mfma_f32_16x16x32_bf16 v[236:239], a[64:67], a[28:31], v[236:239]\n\t"
"ds_read_b128 a[56:59], v11 offset:36864\n\t"
"v_fma_f32 v63, v61, v50, v71\n\t"
"v_mfma_f32_16x16x32_bf16 v[240:243], a[68:71], a[16:19], v[240:243]\n\t"
"ds_read_b64_tr_b16 a[64:65], v17 offset:40960\n\t"
"ds_read_b64_tr_b16 a[66:67], v15 offset:41472\n\t"
"s_and_b32 s61, s27, 1\n\t"
"v_cvt_pk_bf16_f32 v72, v60, v62\n\t"
"v_cvt_pk_bf16_f32 v76, v61, v63\n\t"
"v_mfma_f32_16x16x32_bf16 v[244:247], a[68:71], a[20:23], v[244:247]\n\t"
"v_mul_f32 v70, v62, v50\n\t"
"v_mfma_f32_16x16x32_bf16 v[248:251], a[68:71], a[24:27], v[248:251]\n\t"
"s_lshl_b32 s61, s61, 5\n\t"
"v_mul_f32 v71, v62, v51\n\t"
"v_fma_f32 v64, -v63, v51, v70\n\t"
"v_xor_b32 v27, s61, v27\n\t"
"v_fma_f32 v65, v63, v50, v71\n\t"
"v_mfma_f32_16x16x32_bf16 v[252:255], a[68:71], a[28:31], v[252:255]\n\t"
"ds_read_b128 a[60:63], v11 offset:38912\n\t"
"v_mul_f32 v70, v64, v50\n\t"
"v_lshl_add_u32 v26, v24, 6, v25\n\t"
"v_mul_f32 v71, v64, v51\n\t"
"v_fma_f32 v66, -v65, v51, v70\n\t"
"v_fma_f32 v67, v65, v50, v71\n\t"
"v_add3_u32 v27, v26, v27, v29\n\t"
"v_cvt_pk_bf16_f32 v73, v64, v66\n\t"
"v_cvt_pk_bf16_f32 v77, v65, v67\n\t"
"v_sub_u32 v27, v27, v28\n\t"
"v_mul_f32 v70, v66, v50\n\t"
"v_mul_f32 v71, v66, v51\n\t"
"v_fma_f32 v60, -v67, v51, v70\n\t"
"v_lshl_add_u32 v25, v24, 7, v25\n\t"
"v_fma_f32 v61, v67, v50, v71\n\t"
"v_mul_f32 v70, v60, v50\n\t"
"v_bfe_u32 v24, v0, 3, 2\n\t"
"v_mul_f32 v71, v60, v51\n\t"
"v_fma_f32 v62, -v61, v51, v70\n\t"
"v_lshl_add_u32 v23, v24, 5, v25\n\t"
"v_fma_f32 v63, v61, v50, v71\n\t"
"v_cvt_pk_bf16_f32 v80, v60, v62\n\t"
"v_cvt_pk_bf16_f32 v32, v61, v63\n\t"
"v_xor_b32 v24, 32, v23\n\t"
"v_mul_f32 v70, v62, v50\n\t"
"v_mul_f32 v71, v62, v51\n\t"
"v_xor_b32 v25, 64, v23\n\t"
"v_fma_f32 v64, -v63, v51, v70\n\t"
"v_fma_f32 v65, v63, v50, v71\n\t"
"v_xor_b32 v26, 96, v23\n\t"
"v_mul_f32 v70, v64, v50\n\t"
"v_mul_f32 v71, v64, v51\n\t"
"s_waitcnt lgkmcnt(6)\n\t"
"s_waitcnt lgkmcnt(0)\n\t"
"v_mfma_f32_16x16x32_bf16 v[112:115], a[72:75], a[32:35], v[112:115]\n\t"
"s_add_u32 s6, s26, 8\n\t"
"ds_read_b64_tr_b16 a[68:69], v18 offset:40960\n\t"
"ds_read_b64_tr_b16 a[70:71], v16 offset:41472\n\t"
"v_fma_f32 v66, -v65, v51, v70\n\t"
"v_mfma_f32_16x16x32_bf16 v[116:119], a[72:75], a[36:39], v[116:119]\n\t"
"s_add_u32 m0, s86, 32768\n\t"
"v_fma_f32 v67, v65, v50, v71\n\t"
"v_cvt_pk_bf16_f32 v81, v64, v66\n\t"
"v_cvt_pk_bf16_f32 v33, v65, v67\n\t"
"v_mfma_f32_16x16x32_bf16 v[120:123], a[72:75], a[40:43], v[120:123]\n\t"
"s_sub_u32 s7, s6, 9\n\t"
"buffer_load_dwordx4 v2, s[32:35], s53 offen lds\n\t"
"v_mul_f32 v70, v66, v50\n\t"
"v_mul_f32 v71, v66, v51\n\t"
"v_mfma_f32_16x16x32_bf16 v[124:127], a[72:75], a[44:47], v[124:127]\n\t"
"s_min_u32 s6, s6, s7\n\t"
"v_fma_f32 v60, -v67, v51, v70\n\t"
"v_fma_f32 v61, v67, v50, v71\n\t"
"v_mul_f32 v70, v60, v58\n\t"
"v_mfma_f32_16x16x32_bf16 v[128:131], a[76:79], a[32:35], v[128:131]\n\t"
"s_lshl_b32 s7, s6, 7\n\t"
"ds_read_b64_tr_b16 a[72:73], v15 offset:43008\n\t"
"ds_read_b64_tr_b16 a[74:75], v17 offset:43520\n\t"
"v_mul_f32 v71, v60, v59\n\t"
"v_mfma_f32_16x16x32_bf16 v[132:135], a[76:79], a[36:39], v[132:135]\n\t"
"s_add_u32 m0, s86, 33792\n\t"
"v_fma_f32 v62, -v61, v59, v70\n\t"
"v_fma_f32 v63, v61, v58, v71\n\t"
"v_cvt_pk_bf16_f32 v74, v60, v62\n\t"
"v_mfma_f32_16x16x32_bf16 v[136:139], a[76:79], a[40:43], v[136:139]\n\t"
"s_add_u32 s52, s48, s7\n\t"
"buffer_load_dwordx4 v3, s[32:35], s53 offen lds\n\t"
"v_cvt_pk_bf16_f32 v78, v61, v63\n\t"
"v_mul_f32 v70, v62, v52\n\t"
"v_mfma_f32_16x16x32_bf16 v[140:143], a[76:79], a[44:47], v[140:143]\n\t"
"s_mul_i32 s6, s6, 589824\n\t"
"v_mul_f32 v71, v62, v53\n\t"
"v_fma_f32 v64, -v63, v53, v70\n\t"
"v_fma_f32 v65, v63, v52, v71\n\t"
"v_mfma_f32_16x16x32_bf16 v[144:147], a[64:67], a[32:35], v[144:147]\n\t"
"s_add_u32 s54, s49, s6\n\t"
"ds_read_b64_tr_b16 a[76:77], v16 offset:43008\n\t"
"ds_read_b64_tr_b16 a[78:79], v18 offset:43520\n\t"
"v_mul_f32 v70, v64, v52\n\t"
"v_mfma_f32_16x16x32_bf16 v[148:151], a[64:67], a[36:39], v[148:151]\n\t"
"s_add_u32 s78, s4, s8\n\t"
"v_mul_f32 v71, v64, v53\n\t"
"v_fma_f32 v66, -v65, v53, v70\n\t"
"v_fma_f32 v67, v65, v52, v71\n\t"
"v_mfma_f32_16x16x32_bf16 v[152:155], a[64:67], a[40:43], v[152:155]\n\t"
"s_add_u32 s8, s27, 1\n\t"
"v_cvt_pk_bf16_f32 v75, v64, v66\n\t"
"v_cvt_pk_bf16_f32 v79, v65, v67\n\t"
"v_xor_b32 v68, 0, v102\n\t"
"v_mfma_f32_16x16x32_bf16 v[156:159], a[64:67], a[44:47], v[156:159]\n\t"
"s_waitcnt lgkmcnt(4)\n\t"
"ds_write_b128 v68, v[72:75]\n\t"
"v_mfma_f32_16x16x32_bf16 v[160:163], a[68:71], a[32:35], v[160:163]\n\t"
"s_and_b32 s8, s8, 3\n\t"
"ds_read_b64_tr_b16 a[64:65], v17 offset:43008\n\t"
"ds_read_b64_tr_b16 a[66:67], v15 offset:43520\n\t"
"v_mfma_f32_16x16x32_bf16 v[164:167], a[68:71], a[36:39], v[164:167]\n\t"
"s_add_u32 m0, s86, 34816\n\t"
"ds_write_b128 v68, v[76:79] offset:128\n\t"
"v_mfma_f32_16x16x32_bf16 v[168:171], a[68:71], a[40:43], v[168:171]\n\t"
"s_lshl1_add_u32 s8, s8, 1\n\t"
"buffer_load_dwordx4 v4, s[32:35], s53 offen lds\n\t"
"v_mul_f32 v70, v66, v52\n\t"
"v_mul_f32 v71, v66, v53\n\t"
"v_mfma_f32_16x16x32_bf16 v[172:175], a[68:71], a[44:47], v[172:175]\n\t"
"s_waitcnt lgkmcnt(6)\n\t"
"v_fma_f32 v60, -v67, v53, v70\n\t"
"v_fma_f32 v61, v67, v52, v71\n\t"
"v_mul_f32 v70, v60, v52\n\t"
"v_mfma_f32_16x16x32_bf16 v[176:179], a[72:75], a[32:35], v[176:179]\n\t"
"s_mul_i32 s9, s8, 73728\n\t"
"ds_read_b64_tr_b16 a[68:69], v18 offset:43008\n\t"
"ds_read_b64_tr_b16 a[70:71], v16 offset:43520\n\t"
"v_mul_f32 v71, v60, v53\n\t"
"v_mfma_f32_16x16x32_bf16 v[180:183], a[72:75], a[36:39], v[180:183]\n\t"
"s_add_u32 m0, s86, 35840\n\t"
"v_fma_f32 v62, -v61, v53, v70\n\t"
"v_fma_f32 v63, v61, v52, v71\n\t"
"v_cvt_pk_bf16_f32 v82, v60, v62\n\t"
"v_mfma_f32_16x16x32_bf16 v[184:187], a[72:75], a[40:43], v[184:187]\n\t"
"s_add_u32 s72, s5, s9\n\t"
"buffer_load_dwordx4 v5, s[32:35], s53 offen lds\n\t"
"v_cvt_pk_bf16_f32 v34, v61, v63\n\t"
"v_mul_f32 v70, v62, v58\n\t"
"v_mfma_f32_16x16x32_bf16 v[188:191], a[72:75], a[44:47], v[188:191]\n\t"
"s_waitcnt lgkmcnt(6)\n\t"
"v_mul_f32 v71, v62, v59\n\t"
"v_fma_f32 v64, -v63, v59, v70\n\t"
"v_fma_f32 v65, v63, v58, v71\n\t"
"v_mfma_f32_16x16x32_bf16 v[192:195], a[76:79], a[32:35], v[192:195]\n\t"
"s_lshl_b32 s8, s8, 13\n\t"
"ds_read_b64_tr_b16 a[72:73], v19 offset:40960\n\t"
"ds_read_b64_tr_b16 a[74:75], v19 offset:41216\n\t"
"v_mul_f32 v70, v64, v54\n\t"
"v_mfma_f32_16x16x32_bf16 v[196:199], a[76:79], a[36:39], v[196:199]\n\t"
"s_add_u32 m0, s86, 36864\n\t"
"v_mul_f32 v71, v64, v55\n\t"
"v_fma_f32 v66, -v65, v55, v70\n\t"
"v_fma_f32 v67, v65, v54, v71\n\t"
"v_mfma_f32_16x16x32_bf16 v[200:203], a[76:79], a[40:43], v[200:203]\n\t"
"s_add_u32 s79, s4, s8\n\t"
"buffer_load_dwordx4 v6, s[32:35], s53 offen lds\n\t"
"v_cvt_pk_bf16_f32 v83, v64, v66\n\t"
"v_cvt_pk_bf16_f32 v35, v65, v67\n\t"
"v_mfma_f32_16x16x32_bf16 v[204:207], a[76:79], a[44:47], v[204:207]\n\t"
"s_waitcnt lgkmcnt(5)\n\t"
"v_xor_b32 v68, 16, v102\n\t"
"v_mfma_f32_16x16x32_bf16 v[208:211], a[64:67], a[32:35], v[208:211]\n\t"
"s_add_u32 s8, s27, 2\n\t"
"ds_read_b64_tr_b16 a[76:77], v15 offset:51200\n\t"
"ds_read_b64_tr_b16 a[78:79], v17 offset:51712\n\t"
"ds_write_b128 v68, v[80:83]\n\t"
"v_mfma_f32_16x16x32_bf16 v[212:215], a[64:67], a[36:39], v[212:215]\n\t"
"s_and_b32 s8, s8, 3\n\t"
"v_mfma_f32_16x16x32_bf16 v[216:219], a[64:67], a[40:43], v[216:219]\n\t"
"s_lshl1_add_u32 s8, s8, 1\n\t"
"ds_write_b128 v68, v[32:35] offset:128\n\t"
"v_mul_f32 v70, v66, v54\n\t"
"v_mfma_f32_16x16x32_bf16 v[220:223], a[64:67], a[44:47], v[220:223]\n\t"
"s_waitcnt lgkmcnt(6)\n\t"
"v_mul_f32 v71, v66, v55\n\t"
"v_fma_f32 v60, -v67, v55, v70\n\t"
"v_fma_f32 v61, v67, v54, v71\n\t"
"v_mfma_f32_16x16x32_bf16 v[224:227], a[68:71], a[32:35], v[224:227]\n\t"
"s_mul_i32 s9, s8, 73728\n\t"
"ds_read_b64_tr_b16 a[64:65], v16 offset:51200\n\t"
"ds_read_b64_tr_b16 a[66:67], v18 offset:51712\n\t"
"v_mul_f32 v70, v60, v58\n\t"
"v_mfma_f32_16x16x32_bf16 v[228:231], a[68:71], a[36:39], v[228:231]\n\t"
"s_add_u32 m0, s86, 37888\n\t"
"v_mul_f32 v71, v60, v59\n\t"
"v_fma_f32 v62, -v61, v59, v70\n\t"
"v_fma_f32 v63, v61, v58, v71\n\t"
"v_mfma_f32_16x16x32_bf16 v[232:235], a[68:71], a[40:43], v[232:235]\n\t"
"s_add_u32 s73, s5, s9\n\t"
"buffer_load_dwordx4 v7, s[32:35], s53 offen lds\n\t"
"v_cvt_pk_bf16_f32 v74, v60, v62\n\t"
"v_cvt_pk_bf16_f32 v78, v61, v63\n\t"
"v_mfma_f32_16x16x32_bf16 v[236:239], a[68:71], a[44:47], v[236:239]\n\t"
"s_waitcnt lgkmcnt(6)\n\t"
"v_mul_f32 v70, v62, v54\n\t"
"v_mul_f32 v71, v62, v55\n\t"
"v_fma_f32 v64, -v63, v55, v70\n\t"
"v_mfma_f32_16x16x32_bf16 v[240:243], a[72:75], a[32:35], v[240:243]\n\t"
"s_lshl_b32 s8, s8, 13\n\t"
"ds_read_b64_tr_b16 a[68:69], v17 offset:51200\n\t"
"ds_read_b64_tr_b16 a[70:71], v15 offset:51712\n\t"
"v_fma_f32 v65, v63, v54, v71\n\t"
"v_mfma_f32_16x16x32_bf16 v[244:247], a[72:75], a[36:39], v[244:247]\n\t"
"s_add_u32 m0, s86, 38912\n\t"
"v_mul_f32 v70, v64, v54\n\t"
"v_mul_f32 v71, v64, v55\n\t"
"v_fma_f32 v66, -v65, v55, v70\n\t"
"v_mfma_f32_16x16x32_bf16 v[248:251], a[72:75], a[40:43], v[248:251]\n\t"
"s_add_u32 s80, s4, s8\n\t"
"buffer_load_dwordx4 v8, s[32:35], s53 offen lds\n\t"
"v_fma_f32 v67, v65, v54, v71\n\t"
"v_cvt_pk_bf16_f32 v75, v64, v66\n\t"
"v_mfma_f32_16x16x32_bf16 v[252:255], a[72:75], a[44:47], v[252:255]\n\t"
"s_waitcnt lgkmcnt(6)\n\t"
"v_cvt_pk_bf16_f32 v79, v65, v67\n\t"
"v_mul_f32 v70, v66, v58\n\t"
"v_mul_f32 v71, v66, v59\n\t"
"v_mfma_f32_16x16x32_bf16 v[112:115], a[76:79], a[48:51], v[112:115]\n\t"
"s_add_u32 s8, s27, 3\n\t"
"ds_read_b64_tr_b16 a[72:73], v18 offset:51200\n\t"
"ds_read_b64_tr_b16 a[74:75], v16 offset:51712\n\t"
"v_fma_f32 v60, -v67, v59, v70\n\t"
"v_mfma_f32_16x16x32_bf16 v[116:119], a[76:79], a[52:55], v[116:119]\n\t"
"s_add_u32 m0, s86, 39936\n\t"
"v_fma_f32 v61, v67, v58, v71\n\t"
"v_mul_f32 v70, v60, v56\n\t"
"v_mul_f32 v71, v60, v57\n\t"
"v_mfma_f32_16x16x32_bf16 v[120:123], a[76:79], a[56:59], v[120:123]\n\t"
"s_and_b32 s8, s8, 3\n\t"
"buffer_load_dwordx4 v9, s[32:35], s53 offen lds\n\t"
"v_fma_f32 v62, -v61, v57, v70\n\t"
"v_fma_f32 v63, v61, v56, v71\n\t"
"v_mfma_f32_16x16x32_bf16 v[124:127], a[76:79], a[60:63], v[124:127]\n\t"
"s_waitcnt lgkmcnt(4)\n\t"
"v_cvt_pk_bf16_f32 v72, v60, v62\n\t"
"v_cvt_pk_bf16_f32 v76, v61, v63\n\t"
"v_mul_f32 v70, v62, v58\n\t"
"v_mfma_f32_16x16x32_bf16 v[128:131], a[64:67], a[48:51], v[128:131]\n\t"
"s_lshl1_add_u32 s8, s8, 1\n\t"
"ds_read_b64_tr_b16 a[76:77], v15 offset:53248\n\t"
"ds_read_b64_tr_b16 a[78:79], v17 offset:53760\n\t"
"v_mul_f32 v71, v62, v59\n\t"
"v_mfma_f32_16x16x32_bf16 v[132:135], a[64:67], a[52:55], v[132:135]\n\t"
"s_mul_i32 s9, s8, 73728\n\t"
"v_fma_f32 v64, -v63, v59, v70\n\t"
"v_fma_f32 v65, v63, v58, v71\n\t"
"v_mul_f32 v70, v64, v56\n\t"
"v_mfma_f32_16x16x32_bf16 v[136:139], a[64:67], a[56:59], v[136:139]\n\t"
"s_add_u32 s74, s5, s9\n\t"
"v_mul_f32 v71, v64, v57\n\t"
"v_fma_f32 v66, -v65, v57, v70\n\t"
"v_fma_f32 v67, v65, v56, v71\n\t"
"v_mfma_f32_16x16x32_bf16 v[140:143], a[64:67], a[60:63], v[140:143]\n\t"
"s_waitcnt vmcnt(13)\n\t"
"s_waitcnt lgkmcnt(4)\n\t"
"ds_read_b128 a[0:3], v10 offset:0\n\t"
"v_cvt_pk_bf16_f32 v73, v64, v66\n\t"
"v_cvt_pk_bf16_f32 v77, v65, v67\n\t"
"v_mfma_f32_16x16x32_bf16 v[144:147], a[68:71], a[48:51], v[144:147]\n\t"
"s_lshl_b32 s8, s8, 13\n\t"
"ds_read_b64_tr_b16 a[64:65], v16 offset:53248\n\t"
"ds_read_b64_tr_b16 a[66:67], v18 offset:53760\n\t"
"v_xor_b32 v68, 32, v102\n\t"
"v_mfma_f32_16x16x32_bf16 v[148:151], a[68:71], a[52:55], v[148:151]\n\t"
"s_add_u32 m0, s87, 20480\n\t"
"ds_write_b128 v68, v[72:75]\n\t"
"v_mfma_f32_16x16x32_bf16 v[152:155], a[68:71], a[56:59], v[152:155]\n\t"
"s_add_u32 s81, s4, s8\n\t"
"buffer_load_dwordx4 v12, s[36:39], s55 offen offset:0 lds\n\t"
"v_mfma_f32_16x16x32_bf16 v[156:159], a[68:71], a[60:63], v[156:159]\n\t"
"s_waitcnt lgkmcnt(6)\n\t"
"ds_read_b128 a[4:7], v10 offset:2048\n\t"
"ds_write_b128 v68, v[76:79] offset:128\n\t"
"v_mfma_f32_16x16x32_bf16 v[160:163], a[72:75], a[48:51], v[160:163]\n\t"
"s_lshr_b32 s8, s27, 1\n\t"
"ds_read_b64_tr_b16 a[68:69], v17 offset:53248\n\t"
"ds_read_b64_tr_b16 a[70:71], v15 offset:53760\n\t"
"v_mul_f32 v70, v66, v58\n\t"
"v_mfma_f32_16x16x32_bf16 v[164:167], a[72:75], a[52:55], v[164:167]\n\t"
"s_add_u32 m0, s87, 21504\n\t"
"v_mul_f32 v71, v66, v59\n\t"
"v_fma_f32 v60, -v67, v59, v70\n\t"
"v_fma_f32 v61, v67, v58, v71\n\t"
"v_mfma_f32_16x16x32_bf16 v[168:171], a[72:75], a[56:59], v[168:171]\n\t"
"s_lshl_b32 s8, s8, 12\n\t"
"buffer_load_dwordx4 v13, s[36:39], s55 offen offset:0 lds\n\t"
"v_mul_f32 v70, v60, v56\n\t"
"v_mul_f32 v71, v60, v57\n\t"
"v_mfma_f32_16x16x32_bf16 v[172:175], a[72:75], a[60:63], v[172:175]\n\t"
"s_waitcnt lgkmcnt(8)\n\t"
"ds_read_b128 a[8:11], v10 offset:4096\n\t"
"v_fma_f32 v62, -v61, v57, v70\n\t"
"v_fma_f32 v63, v61, v56, v71\n\t"
"v_mfma_f32_16x16x32_bf16 v[176:179], a[76:79], a[48:51], v[176:179]\n\t"
"s_add_u32 s84, s75, s8\n\t"
"ds_read_b64_tr_b16 a[72:73], v18 offset:53248\n\t"
"ds_read_b64_tr_b16 a[74:75], v16 offset:53760\n\t"
"v_cvt_pk_bf16_f32 v80, v60, v62\n\t"
"v_mfma_f32_16x16x32_bf16 v[180:183], a[76:79], a[52:55], v[180:183]\n\t"
"s_add_u32 m0, s87, 22400\n\t"
"v_cvt_pk_bf16_f32 v32, v61, v63\n\t"
"v_mul_f32 v70, v62, v58\n\t"
"v_mul_f32 v71, v62, v59\n\t"
"v_mfma_f32_16x16x32_bf16 v[184:187], a[76:79], a[56:59], v[184:187]\n\t"
"buffer_load_dwordx4 v12, s[36:39], s55 offen offset:128 lds\n\t"
"v_fma_f32 v64, -v63, v59, v70\n\t"
"v_fma_f32 v65, v63, v58, v71\n\t"
"v_mfma_f32_16x16x32_bf16 v[188:191], a[76:79], a[60:63], v[188:191]\n\t"
"s_waitcnt lgkmcnt(8)\n\t"
"ds_read_b128 a[12:15], v10 offset:6144\n\t"
"v_mul_f32 v70, v64, v56\n\t"
"v_mul_f32 v71, v64, v57\n\t"
"v_mfma_f32_16x16x32_bf16 v[192:195], a[64:67], a[48:51], v[192:195]\n\t"
"ds_read_b64_tr_b16 a[76:77], v19 offset:51200\n\t"
"ds_read_b64_tr_b16 a[78:79], v19 offset:51456\n\t"
"v_fma_f32 v66, -v65, v57, v70\n\t"
"v_mfma_f32_16x16x32_bf16 v[196:199], a[64:67], a[52:55], v[196:199]\n\t"
"v_fma_f32 v67, v65, v56, v71\n\t"
"v_cvt_pk_bf16_f32 v81, v64, v66\n\t"
"v_cvt_pk_bf16_f32 v33, v65, v67\n\t"
"v_mfma_f32_16x16x32_bf16 v[200:203], a[64:67], a[56:59], v[200:203]\n\t"
"v_mul_f32 v70, v66, v56\n\t"
"v_mul_f32 v71, v66, v57\n\t"
"v_fma_f32 v60, -v67, v57, v70\n\t"
"v_mfma_f32_16x16x32_bf16 v[204:207], a[64:67], a[60:63], v[204:207]\n\t"
"s_waitcnt lgkmcnt(6)\n\t"
"ds_read_b128 a[16:19], v11 offset:0\n\t"
"v_fma_f32 v61, v67, v56, v71\n\t"
"v_mul_f32 v70, v60, v56\n\t"
"v_mfma_f32_16x16x32_bf16 v[208:211], a[68:71], a[48:51], v[208:211]\n\t"
"v_mul_f32 v71, v60, v57\n\t"
"v_fma_f32 v62, -v61, v57, v70\n\t"
"v_fma_f32 v63, v61, v56, v71\n\t"
"v_mfma_f32_16x16x32_bf16 v[212:215], a[68:71], a[52:55], v[212:215]\n\t"
"s_add_u32 m0, s87, 23424\n\t"
"v_cvt_pk_bf16_f32 v82, v60, v62\n\t"
"v_cvt_pk_bf16_f32 v34, v61, v63\n\t"
"v_mul_f32 v70, v62, v56\n\t"
"v_mfma_f32_16x16x32_bf16 v[216:219], a[68:71], a[56:59], v[216:219]\n\t"
"buffer_load_dwordx4 v13, s[36:39], s55 offen offset:128 lds\n\t"
"v_mul_f32 v71, v62, v57\n\t"
"v_fma_f32 v64, -v63, v57, v70\n\t"
"v_mfma_f32_16x16x32_bf16 v[220:223], a[68:71], a[60:63], v[220:223]\n\t"
"s_waitcnt vmcnt(12) lgkmcnt(1)\n\t"
"s_barrier\n\t"
"ds_read_b128 a[20:23], v11 offset:2048\n\t"
"v_fma_f32 v65, v63, v56, v71\n\t"
"v_mul_f32 v70, v64, v56\n\t"
"v_mfma_f32_16x16x32_bf16 v[224:227], a[72:75], a[48:51], v[224:227]\n\t"
"ds_read_b64_tr_b16 a[64:65], v15 offset:0\n\t"
"ds_read_b64_tr_b16 a[66:67], v17 offset:512\n\t"
"v_mul_f32 v71, v64, v57\n\t"
"v_mfma_f32_16x16x32_bf16 v[228:231], a[72:75], a[52:55], v[228:231]\n\t"
"s_add_u32 m0, s87, 24576\n\t"
"ds_read_b64_tr_b16 a[68:69], v16 offset:0\n\t"
"ds_read_b64_tr_b16 a[70:71], v18 offset:512\n\t"
"v_fma_f32 v66, -v65, v57, v70\n\t"
"v_mfma_f32_16x16x32_bf16 v[232:235], a[72:75], a[56:59], v[232:235]\n\t"
"buffer_load_dwordx4 v14, s[36:39], s55 offen lds\n\t"
"v_fma_f32 v67, v65, v56, v71\n\t"
"v_cvt_pk_bf16_f32 v83, v64, v66\n\t"
"v_mfma_f32_16x16x32_bf16 v[236:239], a[72:75], a[60:63], v[236:239]\n\t"
"ds_read_b128 a[24:27], v11 offset:4096\n\t"
"v_cvt_pk_bf16_f32 v35, v65, v67\n\t"
"v_xor_b32 v68, 48, v102\n\t"
"v_mfma_f32_16x16x32_bf16 v[240:243], a[76:79], a[48:51], v[240:243]\n\t"
"ds_read_b64_tr_b16 a[72:73], v17 offset:0\n\t"
"ds_read_b64_tr_b16 a[74:75], v15 offset:512\n\t"
"v_mfma_f32_16x16x32_bf16 v[244:247], a[76:79], a[52:55], v[244:247]\n\t"
"ds_write_b128 v68, v[80:83]\n\t"
"v_mfma_f32_16x16x32_bf16 v[248:251], a[76:79], a[56:59], v[248:251]\n\t"
"ds_write_b128 v68, v[32:35] offset:128\n\t"
"v_mfma_f32_16x16x32_bf16 v[252:255], a[76:79], a[60:63], v[252:255]\n\t"
"ds_read_b128 a[28:31], v11 offset:6144\n\t"
"ds_read_b64_tr_b16 a[112:113], v103 offset:0\n\t"
"ds_read_b64_tr_b16 a[114:115], v104 offset:512\n\t"
"ds_read_b64_tr_b16 a[116:117], v105 offset:0\n\t"
"ds_read_b64_tr_b16 a[118:119], v106 offset:512\n\t"
"ds_read_b64_tr_b16 a[120:121], v103 offset:2048\n\t"
"s_waitcnt lgkmcnt(13)\n\t"
"s_waitcnt lgkmcnt(5)\n\t"
"v_mfma_f32_16x16x32_bf16 v[112:115], a[64:67], a[0:3], v[112:115]\n\t"
"ds_read_b64_tr_b16 a[76:77], v18 offset:0\n\t"
"ds_read_b64_tr_b16 a[78:79], v16 offset:512\n\t"
"ds_read_b64_tr_b16 a[122:123], v104 offset:2560\n\t"
"v_mfma_f32_16x16x32_bf16 v[116:119], a[64:67], a[4:7], v[116:119]\n\t"
"s_add_u32 m0, s86, 0\n\t"
"ds_read_b64_tr_b16 a[124:125], v105 offset:2048\n\t"
"ds_read_b64_tr_b16 a[126:127], v106 offset:2560\n\t"
"v_and_b32 v68, 0xffff0000, v87\n\t"
"v_mfma_f32_16x16x32_bf16 v[120:123], a[64:67], a[8:11], v[120:123]\n\t"
"buffer_load_dwordx4 v2, s[32:35], s52 offen lds\n\t"
"buffer_load_dword v110, v46, s[44:47], 0 offen sc1\n\t"
"v_and_b32 v69, 0xffff0000, v89\n\t"
"v_mfma_f32_16x16x32_bf16 v[124:127], a[64:67], a[12:15], v[124:127]\n\t"
"v_mul_f32 v68, s88, v68\n\t"
"v_and_b32 v60, 0xffff0000, v91\n\t"
"v_mfma_f32_16x16x32_bf16 v[128:131], a[68:71], a[0:3], v[128:131]\n\t"
"ds_read_b64_tr_b16 a[64:65], v15 offset:2048\n\t"
"ds_read_b64_tr_b16 a[66:67], v17 offset:2560\n\t"
"v_exp_f32 v68, v68\n\t"
"v_mfma_f32_16x16x32_bf16 v[132:135], a[68:71], a[4:7], v[132:135]\n\t"
"s_add_u32 m0, s86, 1024\n\t"
"s_lshr_b32 s63, %1, 3\n\t"
"s_lshl2_add_u32 s63, s63, s29\n\t"
"s_mul_i32 s63, s63, 5248\n\t"
"s_mul_i32 s64, s26, 663552\n\t"
"s_add_u32 s63, s63, s64\n\t"
"v_add_u32 v111, s63, v36\n\t"
"s_mov_b32 exec_hi, 0x1ff\n\t"
"buffer_load_dword a176, v111, s[40:43], 0 offen\n\t"
"s_mov_b32 exec_hi, -1\n\t"
"v_and_b32 v61, 0xffff0000, v93\n\t"
"v_mfma_f32_16x16x32_bf16 v[136:139], a[68:71], a[8:11], v[136:139]\n\t"
"buffer_load_dwordx4 v3, s[32:35], s52 offen lds\n\t"
"v_cvt_pk_bf16_f32 v68, v68, v68\n\t"
"v_mul_f32 v69, v95, v69\n\t"
"v_mfma_f32_16x16x32_bf16 v[140:143], a[68:71], a[12:15], v[140:143]\n\t"
"v_lshlrev_b32 v68, 16, v68\n\t"
"v_fma_f32 v60, v60, v96, v97\n\t"
"v_mfma_f32_16x16x32_bf16 v[144:147], a[72:75], a[0:3], v[144:147]\n\t"
"ds_read_b64_tr_b16 a[68:69], v16 offset:2048\n\t"
"ds_read_b64_tr_b16 a[70:71], v18 offset:2560\n\t"
"s_waitcnt lgkmcnt(10)\n\t"
"v_mfma_f32_32x32x16_bf16 a[128:143], a[112:115], a[116:119], a[128:143]\n\t"
"v_mfma_f32_16x16x32_bf16 v[148:151], a[72:75], a[4:7], v[148:151]\n\t"
"ds_read_b64_tr_b16 a[112:113], v103 offset:4096\n\t"
"ds_read_b64_tr_b16 a[114:115], v104 offset:4608\n\t"
"ds_read_b64_tr_b16 a[116:117], v105 offset:4096\n\t"
"v_mfma_f32_16x16x32_bf16 v[152:155], a[72:75], a[8:11], v[152:155]\n\t"
"ds_read_b64_tr_b16 a[118:119], v106 offset:4608\n\t"
"v_mul_f32 v68, v94, v68\n\t"
"v_mul_f32 v61, -v96, v61\n\t"
"v_mfma_f32_16x16x32_bf16 v[156:159], a[72:75], a[12:15], v[156:159]\n\t"
"s_waitcnt lgkmcnt(11)\n\t"
"v_mul_f32 v72, 0x41000000, v68\n\t"
"v_mul_f32 v73, 0x41000000, v69\n\t"
"v_fract_f32 v71, v73\n\t"
"v_mfma_f32_16x16x32_bf16 v[160:163], a[76:79], a[0:3], v[160:163]\n\t"
"ds_read_b64_tr_b16 a[72:73], v17 offset:2048\n\t"
"ds_read_b64_tr_b16 a[74:75], v15 offset:2560\n\t"
"v_exp_f32 v70, v72\n\t"
"v_mfma_f32_16x16x32_bf16 v[164:167], a[76:79], a[4:7], v[164:167]\n\t"
"s_add_u32 m0, s86, 2048\n\t"
"v_cos_f32 v56, v71\n\t"
"v_mfma_f32_16x16x32_bf16 v[168:171], a[76:79], a[8:11], v[168:171]\n\t"
"buffer_load_dwordx4 v4, s[32:35], s52 offen lds\n\t"
"v_sin_f32 v57, v71\n\t"
"v_mul_f32 v56, v70, v56\n\t"
"v_mfma_f32_16x16x32_bf16 v[172:175], a[76:79], a[12:15], v[172:175]\n\t"
"s_waitcnt lgkmcnt(8)\n\t"
"v_mul_f32 v57, v70, v57\n\t"
"v_mfma_f32_16x16x32_bf16 v[176:179], a[64:67], a[0:3], v[176:179]\n\t"
"ds_read_b64_tr_b16 a[76:77], v18 offset:2048\n\t"
"ds_read_b64_tr_b16 a[78:79], v16 offset:2560\n\t"
"v_mfma_f32_32x32x16_bf16 a[128:143], a[120:123], a[124:127], a[128:143]\n\t"
"v_mfma_f32_16x16x32_bf16 v[180:183], a[64:67], a[4:7], v[180:183]\n\t"
"s_add_u32 m0, s86, 3072\n\t"
"ds_read_b64_tr_b16 a[120:121], v103 offset:6144\n\t"
"ds_read_b64_tr_b16 a[122:123], v104 offset:6656\n\t"
"ds_read_b64_tr_b16 a[124:125], v105 offset:6144\n\t"
"v_mfma_f32_16x16x32_bf16 v[184:187], a[64:67], a[8:11], v[184:187]\n\t"
"buffer_load_dwordx4 v5, s[32:35], s52 offen lds\n\t"
"ds_read_b64_tr_b16 a[126:127], v106 offset:6656\n\t"
"v_mul_f32 v72, v100, v68\n\t"
"v_mfma_f32_16x16x32_bf16 v[188:191], a[64:67], a[12:15], v[188:191]\n\t"
"s_waitcnt lgkmcnt(12)\n\t"
"v_mul_f32 v73, v100, v69\n\t"
"v_fract_f32 v71, v73\n\t"
"v_mfma_f32_16x16x32_bf16 v[192:195], a[68:71], a[0:3], v[192:195]\n\t"
"ds_read_b64_tr_b16 a[64:65], v19 offset:0\n\t"
"ds_read_b64_tr_b16 a[66:67], v19 offset:256\n\t"
"v_exp_f32 v70, v72\n\t"
"v_mfma_f32_16x16x32_bf16 v[196:199], a[68:71], a[4:7], v[196:199]\n\t"
"s_add_u32 m0, s86, 4096\n\t"
"v_cos_f32 v58, v71\n\t"
"v_mfma_f32_16x16x32_bf16 v[200:203], a[68:71], a[8:11], v[200:203]\n\t"
"buffer_load_dwordx4 v6, s[32:35], s52 offen lds\n\t"
"v_sin_f32 v59, v71\n\t"
"v_mul_f32 v58, v70, v58\n\t"
"v_mfma_f32_16x16x32_bf16 v[204:207], a[68:71], a[12:15], v[204:207]\n\t"
"s_waitcnt lgkmcnt(8)\n\t"
"v_mul_f32 v59, v70, v59\n\t"
"v_mul_f32 v72, v98, v68\n\t"
"v_mul_f32 v73, v98, v69\n\t"
"v_mfma_f32_16x16x32_bf16 v[208:211], a[72:75], a[0:3], v[208:211]\n\t"
"ds_read_b64_tr_b16 a[68:69], v15 offset:10240\n\t"
"ds_read_b64_tr_b16 a[70:71], v17 offset:10752\n\t"
"v_mfma_f32_16x16x32_bf16 v[212:215], a[72:75], a[4:7], v[212:215]\n\t"
"v_mfma_f32_32x32x16_bf16 a[128:143], a[112:115], a[116:119], a[128:143]\n\t"
"v_fract_f32 v71, v73\n\t"
"v_mfma_f32_16x16x32_bf16 v[216:219], a[72:75], a[8:11], v[216:219]\n\t"
"v_exp_f32 v70, v72\n\t"
"v_cos_f32 v50, v71\n\t"
"v_mfma_f32_16x16x32_bf16 v[220:223], a[72:75], a[12:15], v[220:223]\n\t"
"s_waitcnt lgkmcnt(8)\n\t"
"v_sin_f32 v51, v71\n\t"
"v_mul_f32 v50, v70, v50\n\t"
"v_mfma_f32_16x16x32_bf16 v[224:227], a[76:79], a[0:3], v[224:227]\n\t"
"ds_read_b64_tr_b16 a[72:73], v16 offset:10240\n\t"
"ds_read_b64_tr_b16 a[74:75], v18 offset:10752\n\t"
"v_mul_f32 v51, v70, v51\n\t"
"v_mfma_f32_16x16x32_bf16 v[228:231], a[76:79], a[4:7], v[228:231]\n\t"
"s_add_u32 m0, s86, 5120\n\t"
"s_waitcnt lgkmcnt(6)\n\t"
"v_mfma_f32_32x32x16_bf16 a[128:143], a[120:123], a[124:127], a[128:143]\n\t"
"v_mul_f32 v72, v99, v72\n\t"
"v_mfma_f32_16x16x32_bf16 v[232:235], a[76:79], a[8:11], v[232:235]\n\t"
"buffer_load_dwordx4 v7, s[32:35], s52 offen lds\n\t"
"v_mul_f32 v73, v99, v73\n\t"
"v_fract_f32 v71, v73\n\t"
"v_mfma_f32_16x16x32_bf16 v[236:239], a[76:79], a[12:15], v[236:239]\n\t"
"s_waitcnt lgkmcnt(4)\n\t"
"v_exp_f32 v70, v72\n\t"
"v_cos_f32 v52, v71\n\t"
"v_mfma_f32_16x16x32_bf16 v[240:243], a[64:67], a[0:3], v[240:243]\n\t"
"ds_read_b64_tr_b16 a[76:77], v17 offset:10240\n\t"
"ds_read_b64_tr_b16 a[78:79], v15 offset:10752\n\t"
"v_mfma_f32_16x16x32_bf16 v[244:247], a[64:67], a[4:7], v[244:247]\n\t"
"s_add_u32 m0, s86, 6144\n\t"
"v_sin_f32 v53, v71\n\t"
"v_mul_f32 v52, v70, v52\n\t"
"v_mul_f32 v53, v70, v53\n\t"
"v_mfma_f32_16x16x32_bf16 v[248:251], a[64:67], a[8:11], v[248:251]\n\t"
"buffer_load_dwordx4 v8, s[32:35], s52 offen lds\n\t"
"v_mul_f32 v72, v99, v72\n\t"
"v_mul_f32 v73, v99, v73\n\t"
"v_mfma_f32_16x16x32_bf16 v[252:255], a[64:67], a[12:15], v[252:255]\n\t"
"s_waitcnt lgkmcnt(4)\n\t"
"v_fract_f32 v71, v73\n\t"
"v_exp_f32 v70, v72\n\t"
"v_mfma_f32_16x16x32_bf16 v[112:115], a[68:71], a[16:19], v[112:115]\n\t"
"ds_read_b64_tr_b16 a[64:65], v18 offset:10240\n\t"
"ds_read_b64_tr_b16 a[66:67], v16 offset:10752\n\t"
"v_mfma_f32_16x16x32_bf16 v[116:119], a[68:71], a[20:23], v[116:119]\n\t"
"s_add_u32 m0, s86, 7168\n\t"
"v_cos_f32 v54, v71\n\t"
"v_sin_f32 v55, v71\n\t"
"v_mfma_f32_16x16x32_bf16 v[120:123], a[68:71], a[24:27], v[120:123]\n\t"
"buffer_load_dwordx4 v9, s[32:35], s52 offen lds\n\t"
"v_mul_f32 v54, v70, v54\n\t"
"v_mul_f32 v55, v70, v55\n\t"
"v_mfma_f32_16x16x32_bf16 v[124:127], a[68:71], a[28:31], v[124:127]\n\t"
"s_waitcnt lgkmcnt(4)\n\t"
"v_mul_f32 v70, v60, v50\n\t"
"v_mul_f32 v71, v60, v51\n\t"
"v_fma_f32 v62, -v61, v51, v70\n\t"
"v_mfma_f32_16x16x32_bf16 v[128:131], a[72:75], a[16:19], v[128:131]\n\t"
"ds_read_b64_tr_b16 a[68:69], v15 offset:12288\n\t"
"ds_read_b64_tr_b16 a[70:71], v17 offset:12800\n\t"
"v_fma_f32 v63, v61, v50, v71\n\t"
"v_mfma_f32_16x16x32_bf16 v[132:135], a[72:75], a[20:23], v[132:135]\n\t"
"v_cvt_pk_bf16_f32 v72, v60, v62\n\t"
"v_cvt_pk_bf16_f32 v76, v61, v63\n\t"
"v_mul_f32 v70, v62, v50\n\t"
"v_mul_f32 v71, v62, v51\n\t"
"v_mfma_f32_16x16x32_bf16 v[136:139], a[72:75], a[24:27], v[136:139]\n\t"
"v_fma_f32 v64, -v63, v51, v70\n\t"
"v_fma_f32 v65, v63, v50, v71\n\t"
"v_mul_f32 v70, v64, v50\n\t"
"v_mfma_f32_16x16x32_bf16 v[140:143], a[72:75], a[28:31], v[140:143]\n\t"
"s_waitcnt vmcnt(15)\n\t"
"s_waitcnt lgkmcnt(4)\n\t"
"ds_read_b128 a[32:35], v10 offset:32768\n\t"
"v_mul_f32 v71, v64, v51\n\t"
"v_fma_f32 v66, -v65, v51, v70\n\t"
"v_mfma_f32_16x16x32_bf16 v[144:147], a[76:79], a[16:19], v[144:147]\n\t"
"ds_read_b64_tr_b16 a[72:73], v16 offset:12288\n\t"
"ds_read_b64_tr_b16 a[74:75], v18 offset:12800\n\t"
"v_fma_f32 v67, v65, v50, v71\n\t"
"v_mfma_f32_16x16x32_bf16 v[148:151], a[76:79], a[20:23], v[148:151]\n\t"
"s_add_u32 m0, s87, 40960\n\t"
"v_cvt_pk_bf16_f32 v73, v64, v66\n\t"
"v_cvt_pk_bf16_f32 v77, v65, v67\n\t"
"v_mul_f32 v70, v66, v50\n\t"
"v_mul_f32 v71, v66, v51\n\t"
"v_mfma_f32_16x16x32_bf16 v[152:155], a[76:79], a[24:27], v[152:155]\n\t"
"buffer_load_dwordx4 v12, s[36:39], s54 offen offset:0 lds\n\t"
"v_fma_f32 v60, -v67, v51, v70\n\t"
"v_fma_f32 v61, v67, v50, v71\n\t"
"v_mfma_f32_16x16x32_bf16 v[156:159], a[76:79], a[28:31], v[156:159]\n\t"
"s_waitcnt lgkmcnt(5)\n\t"
"ds_read_b128 a[36:39], v10 offset:34816\n\t"
"v_mul_f32 v70, v60, v50\n\t"
"v_mul_f32 v71, v60, v51\n\t"
"v_mfma_f32_16x16x32_bf16 v[160:163], a[64:67], a[16:19], v[160:163]\n\t"
"ds_read_b64_tr_b16 a[76:77], v17 offset:12288\n\t"
"ds_read_b64_tr_b16 a[78:79], v15 offset:12800\n\t"
"v_fma_f32 v62, -v61, v51, v70\n\t"
"v_mfma_f32_16x16x32_bf16 v[164:167], a[64:67], a[20:23], v[164:167]\n\t"
"s_add_u32 m0, s87, 41984\n\t"
"v_fma_f32 v63, v61, v50, v71\n\t"
"v_cvt_pk_bf16_f32 v80, v60, v62\n\t"
"v_cvt_pk_bf16_f32 v32, v61, v63\n\t"
"v_mfma_f32_16x16x32_bf16 v[168:171], a[64:67], a[24:27], v[168:171]\n\t"
"buffer_load_dwordx4 v13, s[36:39], s54 offen offset:0 lds\n\t"
"v_mul_f32 v70, v62, v50\n\t"
"v_mul_f32 v71, v62, v51\n\t"
"v_fma_f32 v64, -v63, v51, v70\n\t"
"v_mfma_f32_16x16x32_bf16 v[172:175], a[64:67], a[28:31], v[172:175]\n\t"
"s_waitcnt lgkmcnt(6)\n\t"
"ds_read_b128 a[40:43], v10 offset:36864\n\t"
"v_fma_f32 v65, v63, v50, v71\n\t"
"v_mul_f32 v70, v64, v50\n\t"
"v_mfma_f32_16x16x32_bf16 v[176:179], a[68:71], a[16:19], v[176:179]\n\t"
"ds_read_b64_tr_b16 a[64:65], v18 offset:12288\n\t"
"ds_read_b64_tr_b16 a[66:67], v16 offset:12800\n\t"
"v_mul_f32 v71, v64, v51\n\t"
"v_mfma_f32_16x16x32_bf16 v[180:183], a[68:71], a[20:23], v[180:183]\n\t"
"s_add_u32 m0, s87, 42880\n\t"
"v_fma_f32 v66, -v65, v51, v70\n\t"
"v_fma_f32 v67, v65, v50, v71\n\t"
"v_cvt_pk_bf16_f32 v81, v64, v66\n\t"
"v_mfma_f32_16x16x32_bf16 v[184:187], a[68:71], a[24:27], v[184:187]\n\t"
"buffer_load_dwordx4 v12, s[36:39], s54 offen offset:128 lds\n\t"
"v_cvt_pk_bf16_f32 v33, v65, v67\n\t"
"v_mul_f32 v70, v66, v50\n\t"
"v_mul_f32 v71, v66, v51\n\t"
"v_mfma_f32_16x16x32_bf16 v[188:191], a[68:71], a[28:31], v[188:191]\n\t"
"s_waitcnt lgkmcnt(6)\n\t"
"ds_read_b128 a[44:47], v10 offset:38912\n\t"
"v_fma_f32 v60, -v67, v51, v70\n\t"
"v_fma_f32 v61, v67, v50, v71\n\t"
"v_mfma_f32_16x16x32_bf16 v[192:195], a[72:75], a[16:19], v[192:195]\n\t"
"ds_read_b64_tr_b16 a[68:69], v19 offset:10240\n\t"
"ds_read_b64_tr_b16 a[70:71], v19 offset:10496\n\t"
"v_mul_f32 v70, v60, v58\n\t"
"v_mfma_f32_16x16x32_bf16 v[196:199], a[72:75], a[20:23], v[196:199]\n\t"
"v_mul_f32 v71, v60, v59\n\t"
"v_fma_f32 v62, -v61, v59, v70\n\t"
"v_fma_f32 v63, v61, v58, v71\n\t"
"v_mfma_f32_16x16x32_bf16 v[200:203], a[72:75], a[24:27], v[200:203]\n\t"
"s_mov_b32 s92, s12\n\t"
"s_mov_b32 s93, 0\n\t"
"s_waitcnt vmcnt(11)\n\t"
"v_sub_u32 v110, v110, v48\n\t"
"v_subrev_u32 v110, 1, v110\n\t"
"v_cmp_lt_u32 vcc, 3, v110\n\t"
"s_nop 2\n\t"
"s_and_b64 vcc, vcc, s[92:93]\n\t"
"s_cbranch_scc0 LP1_ok%=\n\t"
"s_mov_b32 s94, 0\n\t"
"LP1_spin%=:\n\t"
"buffer_load_dword v37, v46, s[44:47], s89 offen sc1\n\t"
"s_waitcnt vmcnt(0)\n\t"
"v_cndmask_b32 v111, 1, 0, s[10:11]\n\t"
"v_add_u32 v111, v111, v37\n\t"
"buffer_store_dword v111, v47, s[44:47], 0 offen sc1\n\t"
"buffer_load_dword v110, v46, s[44:47], 0 offen sc1\n\t"
"s_waitcnt vmcnt(0)\n\t"
"s_add_u32 s94, s94, 1\n\t"
"s_cmp_gt_u32 s94, 0x4000\n\t"
"s_cbranch_scc1 LP1_out%=\n\t"
"v_sub_u32 v110, v110, v48\n\t"
"v_subrev_u32 v110, 1, v110\n\t"
"v_cmp_lt_u32 vcc, 3, v110\n\t"
"s_nop 2\n\t"
"s_and_b64 vcc, vcc, s[92:93]\n\t"
"s_cbranch_scc1 LP1_spin%=\n\t"
"LP1_out%=:\n\t"
"LP1_ok%=:\n\t"
"buffer_load_dword v37, v46, s[44:47], s89 offen sc1\n\t"
"v_mfma_f32_16x16x32_bf16 v[204:207], a[72:75], a[28:31], v[204:207]\n\t"
"s_waitcnt lgkmcnt(6)\n\t"
"ds_read_b128 a[48:51], v11 offset:32768\n\t"
"v_mfma_f32_16x16x32_bf16 v[208:211], a[76:79], a[16:19], v[208:211]\n\t"
"v_cvt_pk_bf16_f32 v74, v60, v62\n\t"
"v_cvt_pk_bf16_f32 v78, v61, v63\n\t"
"v_mul_f32 v70, v62, v52\n\t"
"v_mfma_f32_16x16x32_bf16 v[212:215], a[76:79], a[20:23], v[212:215]\n\t"
"s_add_u32 m0, s87, 43904\n\t"
"v_mul_f32 v71, v62, v53\n\t"
"v_fma_f32 v64, -v63, v53, v70\n\t"
"v_fma_f32 v65, v63, v52, v71\n\t"
"v_mfma_f32_16x16x32_bf16 v[216:219], a[76:79], a[24:27], v[216:219]\n\t"
"buffer_load_dwordx4 v13, s[36:39], s54 offen offset:128 lds\n\t"
"v_mul_f32 v70, v64, v52\n\t"
"v_mul_f32 v71, v64, v53\n\t"
"v_mfma_f32_16x16x32_bf16 v[220:223], a[76:79], a[28:31], v[220:223]\n\t"
"s_waitcnt lgkmcnt(1)\n\t"
"s_barrier\n\t"
"ds_read_b128 a[52:55], v11 offset:34816\n\t"
"v_fma_f32 v66, -v65, v53, v70\n\t"
"v_fma_f32 v67, v65, v52, v71\n\t"
"v_cvt_pk_bf16_f32 v75, v64, v66\n\t"
"v_mfma_f32_16x16x32_bf16 v[224:227], a[64:67], a[16:19], v[224:227]\n\t"
"ds_read_b64_tr_b16 a[72:73], v15 offset:20480\n\t"
"ds_read_b64_tr_b16 a[74:75], v17 offset:20992\n\t"
"v_cvt_pk_bf16_f32 v79, v65, v67\n\t"
"v_mfma_f32_16x16x32_bf16 v[228:231], a[64:67], a[20:23], v[228:231]\n\t"
"s_add_u32 m0, s87, 45056\n\t"
"ds_read_b64_tr_b16 a[76:77], v16 offset:20480\n\t"
"ds_read_b64_tr_b16 a[78:79], v18 offset:20992\n\t"
"v_xor_b32 v68, 0, v102\n\t"
"v_mfma_f32_16x16x32_bf16 v[232:235], a[64:67], a[24:27], v[232:235]\n\t"
"buffer_load_dwordx4 v14, s[36:39], s54 offen lds\n\t"
"ds_write_b128 v68, v[72:75]\n\t"
"v_mfma_f32_16x16x32_bf16 v[236:239], a[64:67], a[28:31], v[236:239]\n\t"
"ds_read_b128 a[56:59], v11 offset:36864\n\t"
"v_mfma_f32_16x16x32_bf16 v[240:243], a[68:71], a[16:19], v[240:243]\n\t"
"ds_read_b64_tr_b16 a[64:65], v17 offset:20480\n\t"
"ds_read_b64_tr_b16 a[66:67], v15 offset:20992\n\t"
"ds_write_b128 v68, v[76:79] offset:128\n\t"
"v_mfma_f32_16x16x32_bf16 v[244:247], a[68:71], a[20:23], v[244:247]\n\t"
"v_mul_f32 v70, v66, v52\n\t"
"v_mul_f32 v71, v66, v53\n\t"
"v_mfma_f32_16x16x32_bf16 v[248:251], a[68:71], a[24:27], v[248:251]\n\t"
"v_fma_f32 v60, -v67, v53, v70\n\t"
"v_fma_f32 v61, v67, v52, v71\n\t"
"v_mul_f32 v70, v60, v52\n\t"
"v_mfma_f32_16x16x32_bf16 v[252:255], a[68:71], a[28:31], v[252:255]\n\t"
"ds_read_b128 a[60:63], v11 offset:38912\n\t"
"v_mul_f32 v71, v60, v53\n\t"
"v_fma_f32 v62, -v61, v53, v70\n\t"
"v_fma_f32 v63, v61, v52, v71\n\t"
"v_cvt_pk_bf16_f32 v82, v60, v62\n\t"
"v_cvt_pk_bf16_f32 v34, v61, v63\n\t"
"v_mul_f32 v70, v62, v58\n\t"
"v_mul_f32 v71, v62, v59\n\t"
"v_fma_f32 v64, -v63, v59, v70\n\t"
"v_fma_f32 v65, v63, v58, v71\n\t"
"v_mul_f32 v70, v64, v54\n\t"
"s_waitcnt lgkmcnt(8)\n\t"
"s_waitcnt lgkmcnt(0)\n\t"
"v_mfma_f32_16x16x32_bf16 v[112:115], a[72:75], a[32:35], v[112:115]\n\t"
"ds_read_b64_tr_b16 a[68:69], v18 offset:20480\n\t"
"ds_read_b64_tr_b16 a[70:71], v16 offset:20992\n\t"
"v_mfma_f32_16x16x32_bf16 v[116:119], a[72:75], a[36:39], v[116:119]\n\t"
"buffer_load_dwordx4 a[160:163], v43, s[44:47], 0 offen offset:0 sc1\n\t"
"v_mul_f32 v71, v64, v55\n\t"
"v_fma_f32 v66, -v65, v55, v70\n\t"
"v_mfma_f32_16x16x32_bf16 v[120:123], a[72:75], a[40:43], v[120:123]\n\t"
"v_fma_f32 v67, v65, v54, v71\n\t"
"v_cvt_pk_bf16_f32 v83, v64, v66\n\t"
"v_cvt_pk_bf16_f32 v35, v65, v67\n\t"
"v_mfma_f32_16x16x32_bf16 v[124:127], a[72:75], a[44:47], v[124:127]\n\t"
"v_xor_b32 v68, 16, v102\n\t"
"v_mfma_f32_16x16x32_bf16 v[128:131], a[76:79], a[32:35], v[128:131]\n\t"
"ds_read_b64_tr_b16 a[72:73], v15 offset:22528\n\t"
"ds_read_b64_tr_b16 a[74:75], v17 offset:23040\n\t"
"v_mfma_f32_16x16x32_bf16 v[132:135], a[76:79], a[36:39], v[132:135]\n\t"
"buffer_load_dwordx4 a[164:167], v43, s[44:47], 0 offen offset:1024 sc1\n\t"
"ds_write_b128 v68, v[80:83]\n\t"
"v_mfma_f32_16x16x32_bf16 v[136:139], a[76:79], a[40:43], v[136:139]\n\t"
"v_mfma_f32_16x16x32_bf16 v[140:143], a[76:79], a[44:47], v[140:143]\n\t"
"ds_write_b128 v68, v[32:35] offset:128\n\t"
"v_mfma_f32_16x16x32_bf16 v[144:147], a[64:67], a[32:35], v[144:147]\n\t"
"ds_read_b64_tr_b16 a[76:77], v16 offset:22528\n\t"
"ds_read_b64_tr_b16 a[78:79], v18 offset:23040\n\t"
"v_mul_f32 v70, v66, v54\n\t"
"v_mfma_f32_16x16x32_bf16 v[148:151], a[64:67], a[36:39], v[148:151]\n\t"
"buffer_load_dwordx4 a[168:171], v43, s[44:47], 0 offen offset:2048 sc1\n\t"
"v_mul_f32 v71, v66, v55\n\t"
"v_fma_f32 v60, -v67, v55, v70\n\t"
"v_mfma_f32_16x16x32_bf16 v[152:155], a[64:67], a[40:43], v[152:155]\n\t"
"v_fma_f32 v61, v67, v54, v71\n\t"
"v_mul_f32 v70, v60, v58\n\t"
"v_mul_f32 v71, v60, v59\n\t"
"v_mfma_f32_16x16x32_bf16 v[156:159], a[64:67], a[44:47], v[156:159]\n\t"
"s_waitcnt lgkmcnt(6)\n\t"
"v_fma_f32 v62, -v61, v59, v70\n\t"
"v_fma_f32 v63, v61, v58, v71\n\t"
"v_cvt_pk_bf16_f32 v74, v60, v62\n\t"
"v_mfma_f32_16x16x32_bf16 v[160:163], a[68:71], a[32:35], v[160:163]\n\t"
"ds_read_b64_tr_b16 a[64:65], v17 offset:22528\n\t"
"ds_read_b64_tr_b16 a[66:67], v15 offset:23040\n\t"
"v_cvt_pk_bf16_f32 v78, v61, v63\n\t"
"v_mfma_f32_16x16x32_bf16 v[164:167], a[68:71], a[36:39], v[164:167]\n\t"
"buffer_load_dwordx4 a[172:175], v43, s[44:47], 0 offen offset:3072 sc1\n\t"
"v_mul_f32 v70, v62, v54\n\t"
"v_mul_f32 v71, v62, v55\n\t"
"v_mfma_f32_16x16x32_bf16 v[168:171], a[68:71], a[40:43], v[168:171]\n\t"
"v_fma_f32 v64, -v63, v55, v70\n\t"
"v_fma_f32 v65, v63, v54, v71\n\t"
"v_mul_f32 v70, v64, v54\n\t"
"v_mfma_f32_16x16x32_bf16 v[172:175], a[68:71], a[44:47], v[172:175]\n\t"
"s_waitcnt lgkmcnt(6)\n\t"
"v_mul_f32 v71, v64, v55\n\t"
"v_fma_f32 v66, -v65, v55, v70\n\t"
"v_fma_f32 v67, v65, v54, v71\n\t"
"v_mfma_f32_16x16x32_bf16 v[176:179], a[72:75], a[32:35], v[176:179]\n\t"
"ds_read_b64_tr_b16 a[68:69], v18 offset:22528\n\t"
"ds_read_b64_tr_b16 a[70:71], v16 offset:23040\n\t"
"v_mfma_f32_16x16x32_bf16 v[180:183], a[72:75], a[36:39], v[180:183]\n\t"
"v_cvt_pk_bf16_f32 v75, v64, v66\n\t"
"v_cvt_pk_bf16_f32 v79, v65, v67\n\t"
"v_mul_f32 v70, v66, v58\n\t"
"v_mfma_f32_16x16x32_bf16 v[184:187], a[72:75], a[40:43], v[184:187]\n\t"
"v_mul_f32 v71, v66, v59\n\t"
"v_fma_f32 v60, -v67, v59, v70\n\t"
"v_fma_f32 v61, v67, v58, v71\n\t"
"v_mfma_f32_16x16x32_bf16 v[188:191], a[72:75], a[44:47], v[188:191]\n\t"
"s_waitcnt lgkmcnt(4)\n\t"
"v_mul_f32 v70, v60, v56\n\t"
"v_mul_f32 v71, v60, v57\n\t"
"v_fma_f32 v62, -v61, v57, v70\n\t"
"v_mfma_f32_16x16x32_bf16 v[192:195], a[76:79], a[32:35], v[192:195]\n\t"
"ds_read_b64_tr_b16 a[72:73], v19 offset:20480\n\t"
"ds_read_b64_tr_b16 a[74:75], v19 offset:20736\n\t"
"v_fma_f32 v63, v61, v56, v71\n\t"
"v_mfma_f32_16x16x32_bf16 v[196:199], a[76:79], a[36:39], v[196:199]\n\t"
"s_add_u32 m0, s87, 0\n\t"
"v_cvt_pk_bf16_f32 v72, v60, v62\n\t"
"v_cvt_pk_bf16_f32 v76, v61, v63\n\t"
"v_mul_f32 v70, v62, v58\n\t"
"v_mfma_f32_16x16x32_bf16 v[200:203], a[76:79], a[40:43], v[200:203]\n\t"
"buffer_load_dwordx4 v20, s[40:43], s66 offen offset:0 lds\n\t"
"v_mul_f32 v71, v62, v59\n\t"
"v_fma_f32 v64, -v63, v59, v70\n\t"
"v_mfma_f32_16x16x32_bf16 v[204:207], a[76:79], a[44:47], v[204:207]\n\t"
"s_waitcnt lgkmcnt(4)\n\t"
"v_fma_f32 v65, v63, v58, v71\n\t"
"v_mul_f32 v70, v64, v56\n\t"
"v_mul_f32 v71, v64, v57\n\t"
"v_mfma_f32_16x16x32_bf16 v[208:211], a[64:67], a[32:35], v[208:211]\n\t"
"ds_read_b64_tr_b16 a[76:77], v15 offset:30720\n\t"
"ds_read_b64_tr_b16 a[78:79], v17 offset:31232\n\t"
"v_fma_f32 v66, -v65, v57, v70\n\t"
"v_mfma_f32_16x16x32_bf16 v[212:215], a[64:67], a[36:39], v[212:215]\n\t"
"v_fma_f32 v67, v65, v56, v71\n\t"
"v_cvt_pk_bf16_f32 v73, v64, v66\n\t"
"v_mfma_f32_16x16x32_bf16 v[216:219], a[64:67], a[40:43], v[216:219]\n\t"
"v_cvt_pk_bf16_f32 v77, v65, v67\n\t"
"v_xor_b32 v68, 32, v102\n\t"
"v_mfma_f32_16x16x32_bf16 v[220:223], a[64:67], a[44:47], v[220:223]\n\t"
"s_waitcnt lgkmcnt(4)\n\t"
"ds_write_b128 v68, v[72:75]\n\t"
"v_mfma_f32_16x16x32_bf16 v[224:227], a[68:71], a[32:35], v[224:227]\n\t"
"ds_read_b64_tr_b16 a[64:65], v16 offset:30720\n\t"
"ds_read_b64_tr_b16 a[66:67], v18 offset:31232\n\t"
"v_mfma_f32_16x16x32_bf16 v[228:231], a[68:71], a[36:39], v[228:231]\n\t"
"ds_write_b128 v68, v[76:79] offset:128\n\t"
"v_mfma_f32_16x16x32_bf16 v[232:235], a[68:71], a[40:43], v[232:235]\n\t"
"v_mul_f32 v70, v66, v58\n\t"
"v_mul_f32 v71, v66, v59\n\t"
"v_fma_f32 v60, -v67, v59, v70\n\t"
"v_mfma_f32_16x16x32_bf16 v[236:239], a[68:71], a[44:47], v[236:239]\n\t"
"s_waitcnt lgkmcnt(6)\n\t"
"v_fma_f32 v61, v67, v58, v71\n\t"
"v_mul_f32 v70, v60, v56\n\t"
"v_mul_f32 v71, v60, v57\n\t"
"v_mfma_f32_16x16x32_bf16 v[240:243], a[72:75], a[32:35], v[240:243]\n\t"
"ds_read_b64_tr_b16 a[68:69], v17 offset:30720\n\t"
"ds_read_b64_tr_b16 a[70:71], v15 offset:31232\n\t"
"v_fma_f32 v62, -v61, v57, v70\n\t"
"v_mfma_f32_16x16x32_bf16 v[244:247], a[72:75], a[36:39], v[244:247]\n\t"
"s_add_u32 m0, s87, 1024\n\t"
"v_fma_f32 v63, v61, v56, v71\n\t"
"v_cvt_pk_bf16_f32 v80, v60, v62\n\t"
"v_cvt_pk_bf16_f32 v32, v61, v63\n\t"
"v_mfma_f32_16x16x32_bf16 v[248:251], a[72:75], a[40:43], v[248:251]\n\t"
"buffer_load_dwordx4 v21, s[40:43], s66 offen offset:0 lds\n\t"
"v_mul_f32 v70, v62, v58\n\t"
"v_mul_f32 v71, v62, v59\n\t"
"v_mfma_f32_16x16x32_bf16 v[252:255], a[72:75], a[44:47], v[252:255]\n\t"
"s_waitcnt lgkmcnt(6)\n\t"
"v_fma_f32 v64, -v63, v59, v70\n\t"
"v_fma_f32 v65, v63, v58, v71\n\t"
"v_mul_f32 v70, v64, v56\n\t"
"v_mfma_f32_16x16x32_bf16 v[112:115], a[76:79], a[48:51], v[112:115]\n\t"
"ds_read_b64_tr_b16 a[72:73], v18 offset:30720\n\t"
"ds_read_b64_tr_b16 a[74:75], v16 offset:31232\n\t"
"v_mfma_f32_16x16x32_bf16 v[116:119], a[76:79], a[52:55], v[116:119]\n\t"
"v_mul_f32 v71, v64, v57\n\t"
"v_fma_f32 v66, -v65, v57, v70\n\t"
"v_fma_f32 v67, v65, v56, v71\n\t"
"v_mfma_f32_16x16x32_bf16 v[120:123], a[76:79], a[56:59], v[120:123]\n\t"
"v_cvt_pk_bf16_f32 v81, v64, v66\n\t"
"v_cvt_pk_bf16_f32 v33, v65, v67\n\t"
"v_mul_f32 v70, v66, v56\n\t"
"v_mfma_f32_16x16x32_bf16 v[124:127], a[76:79], a[60:63], v[124:127]\n\t"
"s_waitcnt lgkmcnt(5)\n\t"
"v_mul_f32 v71, v66, v57\n\t"
"v_fma_f32 v60, -v67, v57, v70\n\t"
"v_fma_f32 v61, v67, v56, v71\n\t"
"v_mfma_f32_16x16x32_bf16 v[128:131], a[64:67], a[48:51], v[128:131]\n\t"
"ds_read_b64_tr_b16 a[76:77], v15 offset:32768\n\t"
"ds_read_b64_tr_b16 a[78:79], v17 offset:33280\n\t"
"v_mul_f32 v70, v60, v56\n\t"
"v_mfma_f32_16x16x32_bf16 v[132:135], a[64:67], a[52:55], v[132:135]\n\t"
"v_mul_f32 v71, v60, v57\n\t"
"v_fma_f32 v62, -v61, v57, v70\n\t"
"v_fma_f32 v63, v61, v56, v71\n\t"
"v_mfma_f32_16x16x32_bf16 v[136:139], a[64:67], a[56:59], v[136:139]\n\t"
"v_cvt_pk_bf16_f32 v82, v60, v62\n\t"
"v_cvt_pk_bf16_f32 v34, v61, v63\n\t"
"v_mul_f32 v70, v62, v56\n\t"
"v_mfma_f32_16x16x32_bf16 v[140:143], a[64:67], a[60:63], v[140:143]\n\t"
"s_waitcnt vmcnt(12)\n\t"
"s_waitcnt lgkmcnt(4)\n\t"
"ds_read_b128 a[0:3], v10 offset:0\n\t"
"v_mul_f32 v71, v62, v57\n\t"
"v_fma_f32 v64, -v63, v57, v70\n\t"
"v_mfma_f32_16x16x32_bf16 v[144:147], a[68:71], a[48:51], v[144:147]\n\t"
"ds_read_b64_tr_b16 a[64:65], v16 offset:32768\n\t"
"ds_read_b64_tr_b16 a[66:67], v18 offset:33280\n\t"
"v_fma_f32 v65, v63, v56, v71\n\t"
"v_mfma_f32_16x16x32_bf16 v[148:151], a[68:71], a[52:55], v[148:151]\n\t"
"s_add_u32 m0, s87, 1920\n\t"
"v_mul_f32 v70, v64, v56\n\t"
"v_mul_f32 v71, v64, v57\n\t"
"v_mfma_f32_16x16x32_bf16 v[152:155], a[68:71], a[56:59], v[152:155]\n\t"
"buffer_load_dwordx4 v20, s[40:43], s66 offen offset:128 lds\n\t"
"v_fma_f32 v66, -v65, v57, v70\n\t"
"v_fma_f32 v67, v65, v56, v71\n\t"
"v_mfma_f32_16x16x32_bf16 v[156:159], a[68:71], a[60:63], v[156:159]\n\t"
"s_waitcnt lgkmcnt(5)\n\t"
"ds_read_b128 a[4:7], v10 offset:2048\n\t"
"v_cvt_pk_bf16_f32 v83, v64, v66\n\t"
"v_cvt_pk_bf16_f32 v35, v65, v67\n\t"
"v_mfma_f32_16x16x32_bf16 v[160:163], a[72:75], a[48:51], v[160:163]\n\t"
"ds_read_b64_tr_b16 a[68:69], v17 offset:32768\n\t"
"ds_read_b64_tr_b16 a[70:71], v15 offset:33280\n\t"
"v_xor_b32 v68, 48, v102\n\t"
"v_mfma_f32_16x16x32_bf16 v[164:167], a[72:75], a[52:55], v[164:167]\n\t"
"ds_write_b128 v68, v[80:83]\n\t"
"v_mfma_f32_16x16x32_bf16 v[168:171], a[72:75], a[56:59], v[168:171]\n\t"
"v_mfma_f32_16x16x32_bf16 v[172:175], a[72:75], a[60:63], v[172:175]\n\t"
"s_waitcnt lgkmcnt(7)\n\t"
"ds_read_b128 a[8:11], v10 offset:4096\n\t"
"ds_write_b128 v68, v[32:35] offset:128\n\t"
"v_mfma_f32_16x16x32_bf16 v[176:179], a[76:79], a[48:51], v[176:179]\n\t"
"ds_read_b64_tr_b16 a[72:73], v18 offset:32768\n\t"
"ds_read_b64_tr_b16 a[74:75], v16 offset:33280\n\t"
"v_mfma_f32_16x16x32_bf16 v[180:183], a[76:79], a[52:55], v[180:183]\n\t"
"ds_read_b64_tr_b16 a[112:113], v103 offset:0\n\t"
"ds_read_b64_tr_b16 a[114:115], v104 offset:512\n\t"
"ds_read_b64_tr_b16 a[116:117], v105 offset:0\n\t"
"v_mfma_f32_16x16x32_bf16 v[184:187], a[76:79], a[56:59], v[184:187]\n\t"
"ds_read_b64_tr_b16 a[118:119], v106 offset:512\n\t"
"ds_read_b64_tr_b16 a[120:121], v103 offset:2048\n\t"
"ds_read_b64_tr_b16 a[122:123], v104 offset:2560\n\t"
"v_mfma_f32_16x16x32_bf16 v[188:191], a[76:79], a[60:63], v[188:191]\n\t"
"s_waitcnt lgkmcnt(14)\n\t"
"ds_read_b128 a[12:15], v10 offset:6144\n\t"
"ds_read_b64_tr_b16 a[124:125], v105 offset:2048\n\t"
"ds_read_b64_tr_b16 a[126:127], v106 offset:2560\n\t"
"v_mfma_f32_16x16x32_bf16 v[192:195], a[64:67], a[48:51], v[192:195]\n\t"
"ds_read_b64_tr_b16 a[76:77], v19 offset:30720\n\t"
"ds_read_b64_tr_b16 a[78:79], v19 offset:30976\n\t"
"v_bfe_u32 v60, v0, 2, 3\n\t"
"v_mfma_f32_16x16x32_bf16 v[196:199], a[64:67], a[52:55], v[196:199]\n\t"
"s_add_u32 m0, s87, 2944\n\t"
"s_mov_b32 s61, 0x76453120\n\t"
"v_lshlrev_b32 v60, 2, v60\n\t"
"v_bfe_u32 v60, s61, v60, 4\n\t"
"v_and_b32 v61, 3, v0\n\t"
"v_mfma_f32_16x16x32_bf16 v[200:203], a[64:67], a[56:59], v[200:203]\n\t"
"buffer_load_dwordx4 v21, s[40:43], s66 offen offset:128 lds\n\t"
"v_lshl_or_b32 v60, v60, 2, v61\n\t"
"v_and_b32 v61, 1, v0\n\t"
"v_mfma_f32_16x16x32_bf16 v[204:207], a[64:67], a[60:63], v[204:207]\n\t"
"s_waitcnt lgkmcnt(15)\n\t"
"ds_read_b128 a[16:19], v11 offset:0\n\t"
"v_mov_b32 v62, 0x05040706\n\t"
"v_mfma_f32_16x16x32_bf16 v[208:211], a[68:71], a[48:51], v[208:211]\n\t"
"s_waitcnt lgkmcnt(8)\n\t"
"v_mfma_f32_32x32x16_bf16 a[128:143], a[112:115], a[116:119], a[128:143]\n\t"
"ds_read_b64_tr_b16 a[112:113], v103 offset:4096\n\t"
"v_mfma_f32_16x16x32_bf16 v[212:215], a[68:71], a[52:55], v[212:215]\n\t"
"ds_read_b64_tr_b16 a[114:115], v104 offset:4608\n\t"
"ds_read_b64_tr_b16 a[116:117], v105 offset:4096\n\t"
"ds_read_b64_tr_b16 a[118:119], v106 offset:4608\n\t"
"v_mfma_f32_16x16x32_bf16 v[216:219], a[68:71], a[56:59], v[216:219]\n\t"
"v_mul_u32_u24 v61, 0x202, v61\n\t"
"v_lshl_add_u32 v107, v61, 16, v62\n\t"
"v_mfma_f32_16x16x32_bf16 v[220:223], a[68:71], a[60:63], v[220:223]\n\t"
"s_waitcnt vmcnt(8) lgkmcnt(5)\n\t"
"s_barrier\n\t"
"ds_read_b128 a[20:23], v11 offset:2048\n\t"
"s_add_u32 s62, s57, 4080\n\t"
"v_lshlrev_b32 v61, 4, v60\n\t"
"v_sub_u32 v108, s62, v61\n\t"
"v_mfma_f32_16x16x32_bf16 v[224:227], a[72:75], a[48:51], v[224:227]\n\t"
"ds_read_b64_tr_b16 a[64:65], v15 offset:40960\n\t"
"ds_read_b64_tr_b16 a[66:67], v17 offset:41472\n\t"
"v_add_u32 v61, 1, v60\n\t"
"v_mfma_f32_16x16x32_bf16 v[228:231], a[72:75], a[52:55], v[228:231]\n\t"
"s_add_u32 m0, s87, 4096\n\t"
"ds_read_b64_tr_b16 a[68:69], v16 offset:40960\n\t"
"ds_read_b64_tr_b16 a[70:71], v18 offset:41472\n\t"
"v_and_b32 v62, 28, v61\n\t"
"v_mfma_f32_16x16x32_bf16 v[232:235], a[72:75], a[56:59], v[232:235]\n\t"
"buffer_load_dwordx4 v22, s[40:43], s66 offen lds\n\t"
"v_bfe_u32 v62, s61, v62, 4\n\t"
"v_mfma_f32_16x16x32_bf16 v[236:239], a[72:75], a[60:63], v[236:239]\n\t"
"ds_read_b128 a[24:27], v11 offset:4096\n\t"
"v_mfma_f32_32x32x16_bf16 a[128:143], a[120:123], a[124:127], a[128:143]\n\t"
"v_mfma_f32_16x16x32_bf16 v[240:243], a[76:79], a[48:51], v[240:243]\n\t"
"ds_read_b64_tr_b16 a[72:73], v17 offset:40960\n\t"
"ds_read_b64_tr_b16 a[74:75], v15 offset:41472\n\t"
"ds_read_b64_tr_b16 a[120:121], v103 offset:6144\n\t"
"v_mfma_f32_16x16x32_bf16 v[244:247], a[76:79], a[52:55], v[244:247]\n\t"
"ds_read_b64_tr_b16 a[122:123], v104 offset:6656\n\t"
"ds_read_b64_tr_b16 a[124:125], v105 offset:6144\n\t"
"ds_read_b64_tr_b16 a[126:127], v106 offset:6656\n\t"
"v_mfma_f32_16x16x32_bf16 v[248:251], a[76:79], a[56:59], v[248:251]\n\t"
"v_and_b32 v63, 35, v61\n\t"
"v_lshl_or_b32 v62, v62, 2, v63\n\t"
"v_lshlrev_b32 v98, 2, v62\n\t"
"v_mfma_f32_16x16x32_bf16 v[252:255], a[76:79], a[60:63], v[252:255]\n\t"
"ds_read_b128 a[28:31], v11 offset:6144\n\t"
"v_mov_b32 v62, 0x05040100\n\t"
"v_mov_b32 v63, 0x05040302\n\t"
"v_cmp_eq_u32 vcc, 32, v61\n\t"
"v_mov_b32 v61, 0x05040504\n\t"
"s_nop 0\n\t"
"v_cndmask_b32 v62, v62, v63, vcc\n\t"
"v_cndmask_b32 v99, v62, v61, s[90:91]\n\t"
"s_add_u32 s61, s57, 8256\n\t"
"s_waitcnt lgkmcnt(13)\n\t"
"v_mfma_f32_32x32x16_bf16 a[128:143], a[112:115], a[116:119], a[128:143]\n\t"
"s_add_u32 s62, s57, 4064\n\t"
"v_mov_b32 v60, s61\n\t"
"s_waitcnt lgkmcnt(10)\n\t"
"s_waitcnt lgkmcnt(0)\n\t"
"v_mfma_f32_16x16x32_bf16 v[112:115], a[64:67], a[0:3], v[112:115]\n\t"
"ds_read_b64_tr_b16 a[76:77], v18 offset:40960\n\t"
"ds_read_b64_tr_b16 a[78:79], v16 offset:41472\n\t"
"v_mfma_f32_16x16x32_bf16 v[116:119], a[64:67], a[4:7], v[116:119]\n\t"
"s_waitcnt vmcnt(5)\n\t"
"ds_write_b128 v44, a[160:163] offset:0\n\t"
"v_mfma_f32_16x16x32_bf16 v[120:123], a[64:67], a[8:11], v[120:123]\n\t"
"v_mov_b32 v61, s62\n\t"
"v_mfma_f32_16x16x32_bf16 v[124:127], a[64:67], a[12:15], v[124:127]\n\t"
"v_mfma_f32_32x32x16_bf16 a[128:143], a[120:123], a[124:127], a[128:143]\n\t"
"v_mfma_f32_16x16x32_bf16 v[128:131], a[68:71], a[0:3], v[128:131]\n\t"
"ds_read_b64_tr_b16 a[64:65], v15 offset:43008\n\t"
"ds_read_b64_tr_b16 a[66:67], v17 offset:43520\n\t"
"v_mfma_f32_16x16x32_bf16 v[132:135], a[68:71], a[4:7], v[132:135]\n\t"
"s_add_u32 m0, s87, 20480\n\t"
"ds_write_b128 v44, a[164:167] offset:1024\n\t"
"v_mfma_f32_16x16x32_bf16 v[136:139], a[68:71], a[8:11], v[136:139]\n\t"
"buffer_load_dwordx4 v20, s[40:43], s67 offen offset:0 lds\n\t"
"v_cmp_gt_u32 vcc, 2, v0\n\t"
"v_mov_b32 v62, 196608\n\t"
"s_nop 0\n\t"
"v_cndmask_b32 v60, v61, v60, vcc\n\t"
"v_mfma_f32_16x16x32_bf16 v[140:143], a[68:71], a[12:15], v[140:143]\n\t"
"v_cmp_gt_u32 vcc, 4, v0\n\t"
"v_lshl_add_u32 v60, v0, 4, v60\n\t"
"v_mov_b32 v63, 0xffff\n\t"
"v_cndmask_b32 v109, v62, v60, vcc\n\t"
"v_mfma_f32_16x16x32_bf16 v[144:147], a[72:75], a[0:3], v[144:147]\n\t"
"ds_read_b64_tr_b16 a[68:69], v16 offset:43008\n\t"
"ds_read_b64_tr_b16 a[70:71], v18 offset:43520\n\t"
"v_mfma_f32_16x16x32_bf16 v[148:151], a[72:75], a[4:7], v[148:151]\n\t"
"ds_write_b128 v44, a[168:171] offset:2048\n\t"
"v_mfma_f32_16x16x32_bf16 v[152:155], a[72:75], a[8:11], v[152:155]\n\t"
"v_cmp_eq_u32 vcc, 0, v0\n\t"
"s_nop 1\n\t"
"v_cndmask_b32 v97, 0, v63, vcc\n\t"
"v_mfma_f32_16x16x32_bf16 v[156:159], a[72:75], a[12:15], v[156:159]\n\t"
"s_waitcnt lgkmcnt(7)\n\t"
"v_mfma_f32_16x16x32_bf16 v[160:163], a[76:79], a[0:3], v[160:163]\n\t"
"ds_read_b64_tr_b16 a[72:73], v17 offset:43008\n\t"
"ds_read_b64_tr_b16 a[74:75], v15 offset:43520\n\t"
"v_mfma_f32_16x16x32_bf16 v[164:167], a[76:79], a[4:7], v[164:167]\n\t"
"ds_write_b128 v44, a[172:175] offset:3072\n\t"
"v_mfma_f32_16x16x32_bf16 v[168:171], a[76:79], a[8:11], v[168:171]\n\t"
"v_accvgpr_read_b32 v50, a128\n\t"
"v_accvgpr_read_b32 v51, a129\n\t"
"v_accvgpr_read_b32 v52, a130\n\t"
"v_mfma_f32_16x16x32_bf16 v[172:175], a[76:79], a[12:15], v[172:175]\n\t"
"s_waitcnt lgkmcnt(7)\n\t"
"v_accvgpr_read_b32 v53, a131\n\t"
"v_accvgpr_read_b32 v54, a132\n\t"
"v_accvgpr_read_b32 v55, a133\n\t"
"v_mfma_f32_16x16x32_bf16 v[176:179], a[64:67], a[0:3], v[176:179]\n\t"
"ds_read_b64_tr_b16 a[76:77], v18 offset:43008\n\t"
"ds_read_b64_tr_b16 a[78:79], v16 offset:43520\n\t"
"v_accvgpr_read_b32 v56, a134\n\t"
"v_mfma_f32_16x16x32_bf16 v[180:183], a[64:67], a[4:7], v[180:183]\n\t"
"s_add_u32 m0, s87, 21504\n\t"
"v_accvgpr_read_b32 v57, a135\n\t"
"v_accvgpr_read_b32 v58, a136\n\t"
"v_accvgpr_read_b32 v59, a137\n\t"
"v_mfma_f32_16x16x32_bf16 v[184:187], a[64:67], a[8:11], v[184:187]\n\t"
"buffer_load_dwordx4 v21, s[40:43], s67 offen offset:0 lds\n\t"
"v_accvgpr_read_b32 v60, a138\n\t"
"v_accvgpr_read_b32 v61, a139\n\t"
"v_mfma_f32_16x16x32_bf16 v[188:191], a[64:67], a[12:15], v[188:191]\n\t"
"s_waitcnt lgkmcnt(6)\n\t"
"v_accvgpr_read_b32 v62, a140\n\t"
"v_accvgpr_read_b32 v63, a141\n\t"
"v_accvgpr_read_b32 v64, a142\n\t"
"v_mfma_f32_16x16x32_bf16 v[192:195], a[68:71], a[0:3], v[192:195]\n\t"
"ds_read_b64_tr_b16 a[64:65], v19 offset:40960\n\t"
"ds_read_b64_tr_b16 a[66:67], v19 offset:41216\n\t"
"v_mov_b32 v110, 0\n\t"
"v_mfma_f32_16x16x32_bf16 v[196:199], a[68:71], a[4:7], v[196:199]\n\t"
"v_mov_b32 v111, 0\n\t"
"v_accvgpr_read_b32 v65, a143\n\t"
"v_add_f32 v50, v50, v101\n\t"
"v_mfma_f32_16x16x32_bf16 v[200:203], a[68:71], a[8:11], v[200:203]\n\t"
"v_cvt_pk_bf16_f32 v66, v50, v51\n\t"
"v_cvt_pk_bf16_f32 v67, v52, v53\n\t"
"v_cvt_pk_bf16_f32 v68, v54, v55\n\t"
"v_mfma_f32_16x16x32_bf16 v[204:207], a[68:71], a[12:15], v[204:207]\n\t"
"s_waitcnt lgkmcnt(5)\n\t"
"ds_write_b64 v49, v[110:111]\n\t"
"v_mfma_f32_16x16x32_bf16 v[208:211], a[72:75], a[0:3], v[208:211]\n\t"
"ds_read_b64_tr_b16 a[68:69], v15 offset:51200\n\t"
"ds_read_b64_tr_b16 a[70:71], v17 offset:51712\n\t"
"v_cvt_pk_bf16_f32 v69, v56, v57\n\t"
"v_mfma_f32_16x16x32_bf16 v[212:215], a[72:75], a[4:7], v[212:215]\n\t"
"v_cvt_pk_bf16_f32 v70, v58, v59\n\t"
"v_cvt_pk_bf16_f32 v71, v60, v61\n\t"
"v_cvt_pk_bf16_f32 v72, v62, v63\n\t"
"v_mfma_f32_16x16x32_bf16 v[216:219], a[72:75], a[8:11], v[216:219]\n\t"
"v_cvt_pk_bf16_f32 v73, v64, v65\n\t"
"v_mfma_f32_16x16x32_bf16 v[220:223], a[72:75], a[12:15], v[220:223]\n\t"
"s_waitcnt lgkmcnt(5)\n\t"
"ds_bpermute_b32 v85, v98, v66\n\t"
"v_and_b32 v74, v97, v66\n\t"
"v_mov_b32 v75, 0\n\t"
"v_mfma_f32_16x16x32_bf16 v[224:227], a[76:79], a[0:3], v[224:227]\n\t"
"ds_read_b64_tr_b16 a[72:73], v16 offset:51200\n\t"
"ds_read_b64_tr_b16 a[74:75], v18 offset:51712\n\t"
"v_mov_b32 v76, 0\n\t"
"v_mfma_f32_16x16x32_bf16 v[228:231], a[76:79], a[4:7], v[228:231]\n\t"
"s_add_u32 m0, s87, 22400\n\t"
"v_mov_b32 v77, 0\n\t"
"v_mfma_f32_16x16x32_bf16 v[232:235], a[76:79], a[8:11], v[232:235]\n\t"
"buffer_load_dwordx4 v20, s[40:43], s67 offen offset:128 lds\n\t"
"ds_write_b128 v109, v[74:77]\n\t"
"v_mfma_f32_16x16x32_bf16 v[236:239], a[76:79], a[12:15], v[236:239]\n\t"
"s_waitcnt lgkmcnt(7)\n\t"
"v_mov_b32 v82, 0x3020302\n\t"
"v_mov_b32 v84, 0x1000100\n\t"
"v_mfma_f32_16x16x32_bf16 v[240:243], a[64:67], a[0:3], v[240:243]\n\t"
"ds_read_b64_tr_b16 a[76:77], v17 offset:51200\n\t"
"ds_read_b64_tr_b16 a[78:79], v15 offset:51712\n\t"
"v_cndmask_b32 v82, v82, v84, s[90:91]\n\t"
"v_mfma_f32_16x16x32_bf16 v[244:247], a[64:67], a[4:7], v[244:247]\n\t"
"v_perm_b32 v74, v70, v70, v82\n\t"
"v_mov_b32 v82, 0x3020302\n\t"
"v_cndmask_b32 v82, v107, v82, s[90:91]\n\t"
"v_mfma_f32_16x16x32_bf16 v[248:251], a[64:67], a[8:11], v[248:251]\n\t"
"v_perm_b32 v75, v70, v69, v82\n\t"
"v_mov_b32 v82, 0x5040504\n\t"
"v_mov_b32 v84, 0x1000302\n\t"
"v_mfma_f32_16x16x32_bf16 v[252:255], a[64:67], a[12:15], v[252:255]\n\t"
"s_waitcnt lgkmcnt(6)\n\t"
"v_cndmask_b32 v82, v82, v84, s[90:91]\n\t"
"v_perm_b32 v76, v70, v69, v82\n\t"
"v_mov_b32 v82, 0x5040504\n\t"
"v_mfma_f32_16x16x32_bf16 v[112:115], a[68:71], a[16:19], v[112:115]\n\t"
"ds_read_b64_tr_b16 a[64:65], v18 offset:51200\n\t"
"ds_read_b64_tr_b16 a[66:67], v16 offset:51712\n\t"
"v_mov_b32 v84, 0x1000100\n\t"
"v_mfma_f32_16x16x32_bf16 v[116:119], a[68:71], a[20:23], v[116:119]\n\t"
"v_cndmask_b32 v82, v82, v84, s[90:91]\n\t"
"v_perm_b32 v77, v70, v69, v82\n\t"
"v_subrev_u32 v83, 2048, v108\n\t"
"v_mfma_f32_16x16x32_bf16 v[120:123], a[68:71], a[24:27], v[120:123]\n\t"
"v_subrev_u32 v84, 1024, v108\n\t"
"v_cndmask_b32 v83, v83, v84, s[90:91]\n\t"
"v_mfma_f32_16x16x32_bf16 v[124:127], a[68:71], a[28:31], v[124:127]\n\t"
"s_waitcnt lgkmcnt(5)\n\t"
"ds_write_b128 v83, v[74:77]\n\t"
"s_mov_b32 s65, 0x1000100\n\t"
"v_mfma_f32_16x16x32_bf16 v[128:131], a[72:75], a[16:19], v[128:131]\n\t"
"ds_read_b64_tr_b16 a[68:69], v15 offset:53248\n\t"
"ds_read_b64_tr_b16 a[70:71], v17 offset:53760\n\t"
"v_perm_b32 v78, v74, v74, s65\n\t"
"v_mfma_f32_16x16x32_bf16 v[132:135], a[72:75], a[20:23], v[132:135]\n\t"
"s_add_u32 m0, s87, 23424\n\t"
"v_alignbit_b32 v79, v75, v74, 16\n\t"
"v_alignbit_b32 v80, v76, v75, 16\n\t"
"v_alignbit_b32 v81, v77, v76, 16\n\t"
"v_mfma_f32_16x16x32_bf16 v[136:139], a[72:75], a[24:27], v[136:139]\n\t"
"buffer_load_dwordx4 v21, s[40:43], s67 offen offset:128 lds\n\t"
"ds_write_b128 v83, v[78:81] offset:4160\n\t"
"v_mfma_f32_16x16x32_bf16 v[140:143], a[72:75], a[28:31], v[140:143]\n\t"
"s_waitcnt lgkmcnt(6)\n\t"
"s_mov_b32 s65, 0x3020302\n\t"
"v_perm_b32 v74, v71, v71, s65\n\t"
"v_mov_b32 v82, 0x1000100\n\t"
"v_mfma_f32_16x16x32_bf16 v[144:147], a[76:79], a[16:19], v[144:147]\n\t"
"ds_read_b64_tr_b16 a[72:73], v16 offset:53248\n\t"
"ds_read_b64_tr_b16 a[74:75], v18 offset:53760\n\t"
"v_cndmask_b32 v82, v107, v82, s[90:91]\n\t"
"v_mfma_f32_16x16x32_bf16 v[148:151], a[76:79], a[20:23], v[148:151]\n\t"
"v_perm_b32 v75, v71, v71, v82\n\t"
"v_mov_b32 v82, 0x5040504\n\t"
"v_mov_b32 v84, 0x3020504\n\t"
"v_mfma_f32_16x16x32_bf16 v[152:155], a[76:79], a[24:27], v[152:155]\n\t"
"v_cndmask_b32 v82, v82, v84, s[90:91]\n\t"
"v_perm_b32 v76, v71, v70, v82\n\t"
"v_mov_b32 v82, 0x5040504\n\t"
"v_mfma_f32_16x16x32_bf16 v[156:159], a[76:79], a[28:31], v[156:159]\n\t"
"s_waitcnt lgkmcnt(6)\n\t"
"v_mov_b32 v84, 0x3020302\n\t"
"v_cndmask_b32 v82, v82, v84, s[90:91]\n\t"
"v_perm_b32 v77, v71, v70, v82\n\t"
"v_mfma_f32_16x16x32_bf16 v[160:163], a[64:67], a[16:19], v[160:163]\n\t"
"ds_read_b64_tr_b16 a[76:77], v17 offset:53248\n\t"
"ds_read_b64_tr_b16 a[78:79], v15 offset:53760\n\t"
"v_subrev_u32 v83, 2560, v108\n\t"
"v_mfma_f32_16x16x32_bf16 v[164:167], a[64:67], a[20:23], v[164:167]\n\t"
"v_subrev_u32 v84, 1536, v108\n\t"
"v_cndmask_b32 v83, v83, v84, s[90:91]\n\t"
"v_mfma_f32_16x16x32_bf16 v[168:171], a[64:67], a[24:27], v[168:171]\n\t"
"ds_write_b128 v83, v[74:77]\n\t"
"s_mov_b32 s65, 0x1000100\n\t"
"v_mfma_f32_16x16x32_bf16 v[172:175], a[64:67], a[28:31], v[172:175]\n\t"
"s_waitcnt lgkmcnt(6)\n\t"
"v_perm_b32 v78, v74, v74, s65\n\t"
"v_alignbit_b32 v79, v75, v74, 16\n\t"
"v_alignbit_b32 v80, v76, v75, 16\n\t"
"v_mfma_f32_16x16x32_bf16 v[176:179], a[68:71], a[16:19], v[176:179]\n\t"
"ds_read_b64_tr_b16 a[64:65], v18 offset:53248\n\t"
"ds_read_b64_tr_b16 a[66:67], v16 offset:53760\n\t"
"v_alignbit_b32 v81, v77, v76, 16\n\t"
"v_mfma_f32_16x16x32_bf16 v[180:183], a[68:71], a[20:23], v[180:183]\n\t"
"s_add_u32 m0, s87, 24576\n\t"
"ds_write_b128 v83, v[78:81] offset:4160\n\t"
"s_mov_b32 s65, 0x3020302\n\t"
"v_mfma_f32_16x16x32_bf16 v[184:187], a[68:71], a[24:27], v[184:187]\n\t"
"buffer_load_dwordx4 v22, s[40:43], s67 offen lds\n\t"
"v_perm_b32 v74, v72, v72, s65\n\t"
"v_perm_b32 v75, v72, v72, v107\n\t"
"v_mfma_f32_16x16x32_bf16 v[188:191], a[68:71], a[28:31], v[188:191]\n\t"
"s_waitcnt lgkmcnt(6)\n\t"
"s_mov_b32 s65, 0x1000100\n\t"
"v_perm_b32 v76, v72, v72, s65\n\t"
"v_mfma_f32_16x16x32_bf16 v[192:195], a[72:75], a[16:19], v[192:195]\n\t"
"ds_read_b64_tr_b16 a[68:69], v19 offset:51200\n\t"
"ds_read_b64_tr_b16 a[70:71], v19 offset:51456\n\t"
"s_mov_b32 s65, 0x1000100\n\t"
"v_perm_b32 v77, v72, v72, s65\n\t"
"v_mfma_f32_16x16x32_bf16 v[196:199], a[72:75], a[20:23], v[196:199]\n\t"
"v_subrev_u32 v83, 3072, v108\n\t"
"v_mov_b32 v84, 196608\n\t"
"v_cndmask_b32 v83, v83, v84, s[90:91]\n\t"
"v_mfma_f32_16x16x32_bf16 v[200:203], a[72:75], a[24:27], v[200:203]\n\t"
"ds_write_b128 v83, v[74:77]\n\t"
"s_mov_b32 s65, 0x1000100\n\t"
"v_mfma_f32_16x16x32_bf16 v[204:207], a[72:75], a[28:31], v[204:207]\n\t"
"s_waitcnt lgkmcnt(7)\n\t"
"v_perm_b32 v78, v74, v74, s65\n\t"
"v_alignbit_b32 v79, v75, v74, 16\n\t"
"v_alignbit_b32 v80, v76, v75, 16\n\t"
"v_mfma_f32_16x16x32_bf16 v[208:211], a[76:79], a[16:19], v[208:211]\n\t"
"v_alignbit_b32 v81, v77, v76, 16\n\t"
"v_mfma_f32_16x16x32_bf16 v[212:215], a[76:79], a[20:23], v[212:215]\n\t"
"ds_write_b128 v83, v[78:81] offset:4160\n\t"
"s_mov_b32 s65, 0x3020302\n\t"
"v_mfma_f32_16x16x32_bf16 v[216:219], a[76:79], a[24:27], v[216:219]\n\t"
"v_perm_b32 v74, v73, v73, s65\n\t"
"v_perm_b32 v75, v73, v73, v107\n\t"
"v_mfma_f32_16x16x32_bf16 v[220:223], a[76:79], a[28:31], v[220:223]\n\t"
"s_waitcnt lgkmcnt(2)\n\t"
"s_barrier\n\t"
"s_mov_b32 s65, 0x1000100\n\t"
"v_perm_b32 v76, v73, v73, s65\n\t"
"v_mfma_f32_16x16x32_bf16 v[224:227], a[64:67], a[16:19], v[224:227]\n\t"
"s_mov_b32 s65, 0x1000100\n\t"
"v_perm_b32 v77, v73, v73, s65\n\t"
"v_subrev_u32 v83, 3584, v108\n\t"
"v_mov_b32 v84, 196608\n\t"
"v_mfma_f32_16x16x32_bf16 v[228:231], a[64:67], a[20:23], v[228:231]\n\t"
"v_cndmask_b32 v83, v83, v84, s[90:91]\n\t"
"v_mfma_f32_16x16x32_bf16 v[232:235], a[64:67], a[24:27], v[232:235]\n\t"
"ds_write_b128 v83, v[74:77]\n\t"
"s_mov_b32 s65, 0x1000100\n\t"
"v_mfma_f32_16x16x32_bf16 v[236:239], a[64:67], a[28:31], v[236:239]\n\t"
"v_perm_b32 v78, v74, v74, s65\n\t"
"v_alignbit_b32 v79, v75, v74, 16\n\t"
"v_alignbit_b32 v80, v76, v75, 16\n\t"
"v_mfma_f32_16x16x32_bf16 v[240:243], a[68:71], a[16:19], v[240:243]\n\t"
"v_alignbit_b32 v81, v77, v76, 16\n\t"
"v_mfma_f32_16x16x32_bf16 v[244:247], a[68:71], a[20:23], v[244:247]\n\t"
"ds_write_b128 v83, v[78:81] offset:4160\n\t"
"v_mfma_f32_16x16x32_bf16 v[248:251], a[68:71], a[24:27], v[248:251]\n\t"
"v_mov_b32 v82, 0x5040706\n\t"
"v_mov_b32 v84, 0x1000302\n\t"
"v_cndmask_b32 v82, v82, v84, s[90:91]\n\t"
"v_mfma_f32_16x16x32_bf16 v[252:255], a[68:71], a[28:31], v[252:255]\n\t"
"v_perm_b32 v74, v69, v68, v82\n\t"
"v_mov_b32 v82, 0x5040706\n\t"
"v_mov_b32 v84, 0x3020302\n\t"
"v_cndmask_b32 v82, v82, v84, s[90:91]\n\t"
"v_perm_b32 v75, v68, v67, v82\n\t"
"s_mov_b32 s65, 0x1000302\n\t"
"v_perm_b32 v76, v67, v67, s65\n\t"
"v_mov_b32 v82, 0x1000302\n\t"
"v_mov_b32 v84, 0x3020302\n\t"
"v_cndmask_b32 v82, v82, v84, s[90:91]\n\t"
"v_perm_b32 v77, v66, v66, v82\n\t"
"v_subrev_u32 v83, 0, v108\n\t"
"v_subrev_u32 v84, 512, v108\n\t"
"v_cndmask_b32 v83, v83, v84, s[90:91]\n\t"
"ds_write_b128 v83, v[74:77]\n\t"
"v_perm_b32 v78, v74, v85, v99\n\t"
"v_alignbit_b32 v79, v75, v74, 16\n\t"
"v_alignbit_b32 v80, v76, v75, 16\n\t"
"v_alignbit_b32 v81, v77, v76, 16\n\t"
"ds_write_b128 v83, v[78:81] offset:4160\n\t"
"ds_read_b128 a[28:31], v42 offset:4032\n\t"
"ds_read_b128 a[24:27], v42 offset:3520\n\t"
"ds_read_b128 a[20:23], v42 offset:3008\n\t"
"ds_read_b128 a[16:19], v42 offset:2496\n\t"
"ds_read_b128 a[12:15], v42 offset:1984\n\t"
"ds_read_b128 a[8:11], v42 offset:1472\n\t"
"ds_read_b128 a[4:7], v42 offset:960\n\t"
"ds_read_b128 a[0:3], v42 offset:448\n\t"
"ds_read2_b32 a[32:33], v38 offset0:240 offset1:241\n\t"
"ds_read2_b32 a[34:35], v38 offset0:242 offset1:243\n\t"
"ds_read2_b32 a[36:37], v38 offset0:112 offset1:113\n\t"
"ds_read2_b32 a[38:39], v38 offset0:114 offset1:115\n\t"
"ds_read2_b32 a[40:41], v39 offset0:240 offset1:241\n\t"
"ds_read2_b32 a[42:43], v39 offset0:242 offset1:243\n\t"
"s_waitcnt lgkmcnt(4)\n\t"
"v_mfma_f32_16x16x32_bf16 v[78:81], a[32:35], a[28:31], 0\n\t"
"ds_read2_b32 a[44:45], v39 offset0:112 offset1:113\n\t"
"ds_read2_b32 a[46:47], v39 offset0:114 offset1:115\n\t"
"ds_read_b128 a[28:31], v42 offset:3968\n\t"
"v_mfma_f32_16x16x32_bf16 v[74:77], a[32:35], a[24:27], 0\n\t"
"v_mov_b32 v107, 2146435072\n\t"
"v_cmp_gt_u32 vcc, 16, v0\n\t"
"v_lshrrev_b32 v108, 2, v0\n\t"
"v_lshlrev_b32 v108, 9, v108\n\t"
"v_add_u32 v108, v108, v36\n\t"
"v_sub_u32 v109, v30, v31\n\t"
"v_add_u32 v108, v108, v109\n\t"
"v_cndmask_b32 v108, v107, v108, vcc\n\t"
"v_mfma_f32_16x16x32_bf16 v[70:73], a[32:35], a[20:23], 0\n\t"
"v_mfma_f32_16x16x32_bf16 v[66:69], a[32:35], a[16:19], 0\n\t"
"v_cndmask_b32 v107, v36, v107, s[90:91]\n\t"
"v_mul_f32 v86, s88, v240\n\t"
"v_mul_f32 v87, s88, v241\n\t"
"v_mfma_f32_16x16x32_bf16 v[62:65], a[32:35], a[12:15], 0\n\t"
"v_mul_f32 v88, s88, v242\n\t"
"v_mul_f32 v89, s88, v243\n\t"
"v_exp_f32 v86, v86\n\t"
"v_mfma_f32_16x16x32_bf16 v[58:61], a[32:35], a[8:11], 0\n\t"
"v_exp_f32 v87, v87\n\t"
"v_exp_f32 v88, v88\n\t"
"v_mfma_f32_16x16x32_bf16 v[54:57], a[32:35], a[4:7], 0\n\t"
"v_exp_f32 v89, v89\n\t"
"v_fma_f32 v90, v86, 0.5, 1.0\n\t"
"v_fma_f32 v91, v87, 0.5, 1.0\n\t"
"v_mfma_f32_16x16x32_bf16 v[50:53], a[32:35], a[0:3], 0\n\t"
"s_waitcnt lgkmcnt(5)\n\t"
"v_fma_f32 v92, v88, 0.5, 1.0\n\t"
"v_fma_f32 v93, v89, 0.5, 1.0\n\t"
"v_fma_f32 v90, v90, v86, 1.0\n\t"
"v_mfma_f32_16x16x32_bf16 v[78:81], a[36:39], a[24:27], v[78:81]\n\t"
"ds_read2_b32 a[32:33], v40 offset0:240 offset1:241\n\t"
"ds_read2_b32 a[34:35], v40 offset0:242 offset1:243\n\t"
"ds_read_b128 a[24:27], v42 offset:3456\n\t"
"v_fma_f32 v91, v91, v87, 1.0\n\t"
"v_mfma_f32_16x16x32_bf16 v[74:77], a[36:39], a[20:23], v[74:77]\n\t"
"v_fma_f32 v92, v92, v88, 1.0\n\t"
"v_fma_f32 v93, v93, v89, 1.0\n\t"
"v_rcp_f32 v90, v90\n\t"
"v_mfma_f32_16x16x32_bf16 v[70:73], a[36:39], a[16:19], v[70:73]\n\t"
"v_rcp_f32 v91, v91\n\t"
"v_mfma_f32_16x16x32_bf16 v[66:69], a[36:39], a[12:15], v[66:69]\n\t"
"v_rcp_f32 v92, v92\n\t"
"v_rcp_f32 v93, v93\n\t"
"v_mfma_f32_16x16x32_bf16 v[62:65], a[36:39], a[8:11], v[62:65]\n\t"
"v_fma_f32 v86, -v240, v90, v240\n\t"
"v_fma_f32 v87, -v241, v91, v241\n\t"
"v_fma_f32 v88, -v242, v92, v242\n\t"
"v_fma_f32 v89, -v243, v93, v243\n\t"
"v_mfma_f32_16x16x32_bf16 v[58:61], a[36:39], a[4:7], v[58:61]\n\t"
"v_cvt_pk_bf16_f32 v240, v86, v87\n\t"
"v_cvt_pk_bf16_f32 v241, v88, v89\n\t"
"v_mfma_f32_16x16x32_bf16 v[54:57], a[36:39], a[0:3], v[54:57]\n\t"
"s_waitcnt lgkmcnt(6)\n\t"
"buffer_store_dwordx2 v[240:241], v30, s[44:47], s84 offen offset:0 sc1\n\t"
"v_mul_f32 v94, s88, v244\n\t"
"v_mul_f32 v95, s88, v245\n\t"
"v_mul_f32 v96, s88, v246\n\t"
"v_mfma_f32_16x16x32_bf16 v[78:81], a[40:43], a[20:23], v[78:81]\n\t"
"ds_read2_b32 a[36:37], v40 offset0:112 offset1:113\n\t"
"ds_read2_b32 a[38:39], v40 offset0:114 offset1:115\n\t"
"ds_read_b128 a[20:23], v42 offset:2944\n\t"
"v_mul_f32 v97, s88, v247\n\t"
"v_mfma_f32_16x16x32_bf16 v[74:77], a[40:43], a[16:19], v[74:77]\n\t"
"v_exp_f32 v94, v94\n\t"
"v_exp_f32 v95, v95\n\t"
"v_mfma_f32_16x16x32_bf16 v[70:73], a[40:43], a[12:15], v[70:73]\n\t"
"v_exp_f32 v96, v96\n\t"
"v_mfma_f32_16x16x32_bf16 v[66:69], a[40:43], a[8:11], v[66:69]\n\t"
"v_exp_f32 v97, v97\n\t"
"v_fma_f32 v98, v94, 0.5, 1.0\n\t"
"v_fma_f32 v99, v95, 0.5, 1.0\n\t"
"v_mfma_f32_16x16x32_bf16 v[62:65], a[40:43], a[4:7], v[62:65]\n\t"
"v_fma_f32 v100, v96, 0.5, 1.0\n\t"
"v_fma_f32 v101, v97, 0.5, 1.0\n\t"
"v_fma_f32 v98, v98, v94, 1.0\n\t"
"v_fma_f32 v99, v99, v95, 1.0\n\t"
"v_mfma_f32_16x16x32_bf16 v[58:61], a[40:43], a[0:3], v[58:61]\n\t"
"s_waitcnt lgkmcnt(7)\n\t"
"v_fma_f32 v100, v100, v96, 1.0\n\t"
"v_fma_f32 v101, v101, v97, 1.0\n\t"
"v_rcp_f32 v98, v98\n\t"
"v_mfma_f32_16x16x32_bf16 v[78:81], a[44:47], a[16:19], v[78:81]\n\t"
"ds_read2_b32 a[40:41], v41 offset0:240 offset1:241\n\t"
"ds_read2_b32 a[42:43], v41 offset0:242 offset1:243\n\t"
"ds_read_b128 a[16:19], v42 offset:2432\n\t"
"v_mfma_f32_16x16x32_bf16 v[74:77], a[44:47], a[12:15], v[74:77]\n\t"
"v_rcp_f32 v99, v99\n\t"
"v_rcp_f32 v100, v100\n\t"
"v_mfma_f32_16x16x32_bf16 v[70:73], a[44:47], a[8:11], v[70:73]\n\t"
"v_rcp_f32 v101, v101\n\t"
"v_fma_f32 v94, -v244, v98, v244\n\t"
"v_fma_f32 v95, -v245, v99, v245\n\t"
"v_mfma_f32_16x16x32_bf16 v[66:69], a[44:47], a[4:7], v[66:69]\n\t"
"v_fma_f32 v96, -v246, v100, v246\n\t"
"v_fma_f32 v97, -v247, v101, v247\n\t"
"v_cvt_pk_bf16_f32 v244, v94, v95\n\t"
"v_cvt_pk_bf16_f32 v245, v96, v97\n\t"
"v_mfma_f32_16x16x32_bf16 v[62:65], a[44:47], a[0:3], v[62:65]\n\t"
"s_waitcnt lgkmcnt(7)\n\t"
"buffer_store_dwordx2 v[244:245], v30, s[44:47], s84 offen offset:1024 sc1\n\t"
"v_mul_f32 v86, s88, v248\n\t"
"v_mul_f32 v87, s88, v249\n\t"
"v_mfma_f32_16x16x32_bf16 v[78:81], a[32:35], a[12:15], v[78:81]\n\t"
"ds_read2_b32 a[44:45], v41 offset0:112 offset1:113\n\t"
"ds_read2_b32 a[46:47], v41 offset0:114 offset1:115\n\t"
"ds_read_b128 a[12:15], v42 offset:1920\n\t"
"v_mul_f32 v88, s88, v250\n\t"
"v_mfma_f32_16x16x32_bf16 v[74:77], a[32:35], a[8:11], v[74:77]\n\t"
"v_mul_f32 v89, s88, v251\n\t"
"v_exp_f32 v86, v86\n\t"
"v_mfma_f32_16x16x32_bf16 v[70:73], a[32:35], a[4:7], v[70:73]\n\t"
"v_exp_f32 v87, v87\n\t"
"v_exp_f32 v88, v88\n\t"
"v_mfma_f32_16x16x32_bf16 v[66:69], a[32:35], a[0:3], v[66:69]\n\t"
"s_waitcnt lgkmcnt(7)\n\t"
"v_exp_f32 v89, v89\n\t"
"v_fma_f32 v90, v86, 0.5, 1.0\n\t"
"v_fma_f32 v91, v87, 0.5, 1.0\n\t"
"v_mfma_f32_16x16x32_bf16 v[78:81], a[36:39], a[8:11], v[78:81]\n\t"
"ds_read2_b32 a[32:33], v38 offset0:224 offset1:225\n\t"
"ds_read2_b32 a[34:35], v38 offset0:226 offset1:227\n\t"
"ds_read_b128 a[8:11], v42 offset:1408\n\t"
"v_fma_f32 v92, v88, 0.5, 1.0\n\t"
"v_mfma_f32_16x16x32_bf16 v[74:77], a[36:39], a[4:7], v[74:77]\n\t"
"v_fma_f32 v93, v89, 0.5, 1.0\n\t"
"v_fma_f32 v90, v90, v86, 1.0\n\t"
"v_fma_f32 v91, v91, v87, 1.0\n\t"
"v_mfma_f32_16x16x32_bf16 v[70:73], a[36:39], a[0:3], v[70:73]\n\t"
"s_waitcnt lgkmcnt(7)\n\t"
"v_fma_f32 v92, v92, v88, 1.0\n\t"
"v_fma_f32 v93, v93, v89, 1.0\n\t"
"v_rcp_f32 v90, v90\n\t"
"v_mfma_f32_16x16x32_bf16 v[78:81], a[40:43], a[4:7], v[78:81]\n\t"
"ds_read2_b32 a[36:37], v38 offset0:96 offset1:97\n\t"
"ds_read2_b32 a[38:39], v38 offset0:98 offset1:99\n\t"
"ds_read_b128 a[4:7], v42 offset:896\n\t"
"v_mfma_f32_16x16x32_bf16 v[74:77], a[40:43], a[0:3], v[74:77]\n\t"
"s_waitcnt lgkmcnt(7)\n\t"
"v_rcp_f32 v91, v91\n\t"
"v_rcp_f32 v92, v92\n\t"
"v_mfma_f32_16x16x32_bf16 v[78:81], a[44:47], a[0:3], v[78:81]\n\t"
"s_waitcnt lgkmcnt(4)\n\t"
"ds_read2_b32 a[40:41], v39 offset0:224 offset1:225\n\t"
"ds_read2_b32 a[42:43], v39 offset0:226 offset1:227\n\t"
"ds_read_b128 a[0:3], v42 offset:384\n\t"
"v_mfma_f32_16x16x32_bf16 v[78:81], a[32:35], a[28:31], v[78:81]\n\t"
"ds_read2_b32 a[44:45], v39 offset0:96 offset1:97\n\t"
"ds_read2_b32 a[46:47], v39 offset0:98 offset1:99\n\t"
"ds_read_b128 a[28:31], v42 offset:3904\n\t"
"v_mfma_f32_16x16x32_bf16 v[74:77], a[32:35], a[24:27], v[74:77]\n\t"
"v_rcp_f32 v93, v93\n\t"
"v_fma_f32 v86, -v248, v90, v248\n\t"
"v_fma_f32 v87, -v249, v91, v249\n\t"
"v_mfma_f32_16x16x32_bf16 v[70:73], a[32:35], a[20:23], v[70:73]\n\t"
"v_fma_f32 v88, -v250, v92, v250\n\t"
"v_fma_f32 v89, -v251, v93, v251\n\t"
"v_cvt_pk_bf16_f32 v248, v86, v87\n\t"
"v_cvt_pk_bf16_f32 v249, v88, v89\n\t"
"v_mfma_f32_16x16x32_bf16 v[66:69], a[32:35], a[16:19], v[66:69]\n\t"
"s_waitcnt lgkmcnt(9)\n\t"
"buffer_store_dwordx2 v[248:249], v30, s[44:47], s84 offen offset:2048 sc1\n\t"
"v_mul_f32 v94, s88, v252\n\t"
"v_mul_f32 v95, s88, v253\n\t"
"v_mul_f32 v96, s88, v254\n\t"
"v_mfma_f32_16x16x32_bf16 v[62:65], a[32:35], a[12:15], v[62:65]\n\t"
"v_mul_f32 v97, s88, v255\n\t"
"v_exp_f32 v94, v94\n\t"
"v_mfma_f32_16x16x32_bf16 v[58:61], a[32:35], a[8:11], v[58:61]\n\t"
"s_waitcnt lgkmcnt(6)\n\t"
"v_exp_f32 v95, v95\n\t"
"v_exp_f32 v96, v96\n\t"
"v_mfma_f32_16x16x32_bf16 v[54:57], a[32:35], a[4:7], v[54:57]\n\t"
"s_waitcnt lgkmcnt(3)\n\t"
"v_exp_f32 v97, v97\n\t"
"v_fma_f32 v98, v94, 0.5, 1.0\n\t"
"v_fma_f32 v99, v95, 0.5, 1.0\n\t"
"v_mfma_f32_16x16x32_bf16 v[50:53], a[32:35], a[0:3], v[50:53]\n\t"
"v_fma_f32 v100, v96, 0.5, 1.0\n\t"
"v_fma_f32 v101, v97, 0.5, 1.0\n\t"
"v_fma_f32 v98, v98, v94, 1.0\n\t"
"v_fma_f32 v99, v99, v95, 1.0\n\t"
"v_mfma_f32_16x16x32_bf16 v[78:81], a[36:39], a[24:27], v[78:81]\n\t"
"ds_read2_b32 a[32:33], v40 offset0:224 offset1:225\n\t"
"ds_read2_b32 a[34:35], v40 offset0:226 offset1:227\n\t"
"ds_read_b128 a[24:27], v42 offset:3392\n\t"
"v_mfma_f32_16x16x32_bf16 v[74:77], a[36:39], a[20:23], v[74:77]\n\t"
"v_fma_f32 v100, v100, v96, 1.0\n\t"
"v_fma_f32 v101, v101, v97, 1.0\n\t"
"v_rcp_f32 v98, v98\n\t"
"v_mfma_f32_16x16x32_bf16 v[70:73], a[36:39], a[16:19], v[70:73]\n\t"
"v_rcp_f32 v99, v99\n\t"
"v_rcp_f32 v100, v100\n\t"
"v_mfma_f32_16x16x32_bf16 v[66:69], a[36:39], a[12:15], v[66:69]\n\t"
"v_rcp_f32 v101, v101\n\t"
"v_fma_f32 v94, -v252, v98, v252\n\t"
"v_mfma_f32_16x16x32_bf16 v[62:65], a[36:39], a[8:11], v[62:65]\n\t"
"v_fma_f32 v95, -v253, v99, v253\n\t"
"v_fma_f32 v96, -v254, v100, v254\n\t"
"v_fma_f32 v97, -v255, v101, v255\n\t"
"v_cvt_pk_bf16_f32 v252, v94, v95\n\t"
"v_mfma_f32_16x16x32_bf16 v[58:61], a[36:39], a[4:7], v[58:61]\n\t"
"v_cvt_pk_bf16_f32 v253, v96, v97\n\t"
"v_mfma_f32_16x16x32_bf16 v[54:57], a[36:39], a[0:3], v[54:57]\n\t"
"buffer_store_dwordx2 v[252:253], v30, s[44:47], s84 offen offset:3072 sc1\n\t"
"v_mul_f32 v86, s88, v112\n\t"
"v_mul_f32 v87, s88, v113\n\t"
"v_mul_f32 v88, s88, v114\n\t"
"v_mfma_f32_16x16x32_bf16 v[78:81], a[40:43], a[20:23], v[78:81]\n\t"
"ds_read2_b32 a[36:37], v40 offset0:96 offset1:97\n\t"
"ds_read2_b32 a[38:39], v40 offset0:98 offset1:99\n\t"
"ds_read_b128 a[20:23], v42 offset:2880\n\t"
"v_mul_f32 v89, s88, v115\n\t"
"v_mfma_f32_16x16x32_bf16 v[74:77], a[40:43], a[16:19], v[74:77]\n\t"
"v_exp_f32 v86, v86\n\t"
"v_exp_f32 v87, v87\n\t"
"v_mfma_f32_16x16x32_bf16 v[70:73], a[40:43], a[12:15], v[70:73]\n\t"
"v_exp_f32 v88, v88\n\t"
"v_mfma_f32_16x16x32_bf16 v[66:69], a[40:43], a[8:11], v[66:69]\n\t"
"v_exp_f32 v89, v89\n\t"
"v_fma_f32 v90, v86, 0.5, 1.0\n\t"
"v_fma_f32 v91, v87, 0.5, 1.0\n\t"
"v_mfma_f32_16x16x32_bf16 v[62:65], a[40:43], a[4:7], v[62:65]\n\t"
"v_fma_f32 v92, v88, 0.5, 1.0\n\t"
"v_fma_f32 v93, v89, 0.5, 1.0\n\t"
"v_fma_f32 v90, v90, v86, 1.0\n\t"
"v_fma_f32 v91, v91, v87, 1.0\n\t"
"v_mfma_f32_16x16x32_bf16 v[58:61], a[40:43], a[0:3], v[58:61]\n\t"
"s_waitcnt lgkmcnt(7)\n\t"
"v_fma_f32 v92, v92, v88, 1.0\n\t"
"v_fma_f32 v93, v93, v89, 1.0\n\t"
"v_rcp_f32 v90, v90\n\t"
"v_mfma_f32_16x16x32_bf16 v[78:81], a[44:47], a[16:19], v[78:81]\n\t"
"ds_read2_b32 a[40:41], v41 offset0:224 offset1:225\n\t"
"ds_read2_b32 a[42:43], v41 offset0:226 offset1:227\n\t"
"ds_read_b128 a[16:19], v42 offset:2368\n\t"
"v_mfma_f32_16x16x32_bf16 v[74:77], a[44:47], a[12:15], v[74:77]\n\t"
"v_rcp_f32 v91, v91\n\t"
"v_rcp_f32 v92, v92\n\t"
"v_mfma_f32_16x16x32_bf16 v[70:73], a[44:47], a[8:11], v[70:73]\n\t"
"v_rcp_f32 v93, v93\n\t"
"v_fma_f32 v86, -v112, v90, v112\n\t"
"v_fma_f32 v87, -v113, v91, v113\n\t"
"v_mfma_f32_16x16x32_bf16 v[66:69], a[44:47], a[4:7], v[66:69]\n\t"
"v_fma_f32 v88, -v114, v92, v114\n\t"
"v_fma_f32 v89, -v115, v93, v115\n\t"
"v_cvt_pk_bf16_f32 v112, v86, v87\n\t"
"v_cvt_pk_bf16_f32 v113, v88, v89\n\t"
"v_mfma_f32_16x16x32_bf16 v[62:65], a[44:47], a[0:3], v[62:65]\n\t"
"s_waitcnt lgkmcnt(7)\n\t"
"v_mul_f32 v94, s88, v128\n\t"
"v_mul_f32 v95, s88, v129\n\t"
"v_mul_f32 v96, s88, v130\n\t"
"v_mfma_f32_16x16x32_bf16 v[78:81], a[32:35], a[12:15], v[78:81]\n\t"
"ds_read2_b32 a[44:45], v41 offset0:96 offset1:97\n\t"
"ds_read2_b32 a[46:47], v41 offset0:98 offset1:99\n\t"
"ds_read_b128 a[12:15], v42 offset:1856\n\t"
"v_mfma_f32_16x16x32_bf16 v[74:77], a[32:35], a[8:11], v[74:77]\n\t"
"s_add_u32 m0, s87, 40960\n\t"
"s_nop 0\n\t"
"buffer_load_dwordx4 v20, s[40:43], s68 offen offset:0 lds\n\t"
"v_mul_f32 v97, s88, v131\n\t"
"v_mfma_f32_16x16x32_bf16 v[70:73], a[32:35], a[4:7], v[70:73]\n\t"
"v_exp_f32 v94, v94\n\t"
"v_exp_f32 v95, v95\n\t"
"v_mfma_f32_16x16x32_bf16 v[66:69], a[32:35], a[0:3], v[66:69]\n\t"
"s_waitcnt lgkmcnt(7)\n\t"
"v_exp_f32 v96, v96\n\t"
"v_exp_f32 v97, v97\n\t"
"v_mfma_f32_16x16x32_bf16 v[78:81], a[36:39], a[8:11], v[78:81]\n\t"
"ds_read2_b32 a[32:33], v38 offset0:208 offset1:209\n\t"
"ds_read2_b32 a[34:35], v38 offset0:210 offset1:211\n\t"
"ds_read_b128 a[8:11], v42 offset:1344\n\t"
"v_fma_f32 v98, v94, 0.5, 1.0\n\t"
"v_mfma_f32_16x16x32_bf16 v[74:77], a[36:39], a[4:7], v[74:77]\n\t"
"v_fma_f32 v99, v95, 0.5, 1.0\n\t"
"v_fma_f32 v100, v96, 0.5, 1.0\n\t"
"v_fma_f32 v101, v97, 0.5, 1.0\n\t"
"v_mfma_f32_16x16x32_bf16 v[70:73], a[36:39], a[0:3], v[70:73]\n\t"
"s_waitcnt lgkmcnt(7)\n\t"
"v_fma_f32 v98, v98, v94, 1.0\n\t"
"v_fma_f32 v99, v99, v95, 1.0\n\t"
"v_fma_f32 v100, v100, v96, 1.0\n\t"
"v_fma_f32 v101, v101, v97, 1.0\n\t"
"v_mfma_f32_16x16x32_bf16 v[78:81], a[40:43], a[4:7], v[78:81]\n\t"
"ds_read2_b32 a[36:37], v38 offset0:80 offset1:81\n\t"
"ds_read2_b32 a[38:39], v38 offset0:82 offset1:83\n\t"
"ds_read_b128 a[4:7], v42 offset:832\n\t"
"v_mfma_f32_16x16x32_bf16 v[74:77], a[40:43], a[0:3], v[74:77]\n\t"
"s_waitcnt lgkmcnt(7)\n\t"
"v_rcp_f32 v98, v98\n\t"
"v_rcp_f32 v99, v99\n\t"
"v_mfma_f32_16x16x32_bf16 v[78:81], a[44:47], a[0:3], v[78:81]\n\t"
"s_waitcnt lgkmcnt(4)\n\t"
"ds_read2_b32 a[40:41], v39 offset0:208 offset1:209\n\t"
"ds_read2_b32 a[42:43], v39 offset0:210 offset1:211\n\t"
"ds_read_b128 a[0:3], v42 offset:320\n\t"
"v_mfma_f32_16x16x32_bf16 v[78:81], a[32:35], a[28:31], v[78:81]\n\t"
"ds_read2_b32 a[44:45], v39 offset0:80 offset1:81\n\t"
"ds_read2_b32 a[46:47], v39 offset0:82 offset1:83\n\t"
"ds_read_b128 a[28:31], v42 offset:3840\n\t"
"v_mfma_f32_16x16x32_bf16 v[74:77], a[32:35], a[24:27], v[74:77]\n\t"
"v_rcp_f32 v100, v100\n\t"
"v_rcp_f32 v101, v101\n\t"
"v_mfma_f32_16x16x32_bf16 v[70:73], a[32:35], a[20:23], v[70:73]\n\t"
"v_fma_f32 v94, -v128, v98, v128\n\t"
"v_fma_f32 v95, -v129, v99, v129\n\t"
"v_fma_f32 v96, -v130, v100, v130\n\t"
"v_fma_f32 v97, -v131, v101, v131\n\t"
"v_mfma_f32_16x16x32_bf16 v[66:69], a[32:35], a[16:19], v[66:69]\n\t"
"s_waitcnt lgkmcnt(9)\n\t"
"v_cvt_pk_bf16_f32 v114, v94, v95\n\t"
"v_cvt_pk_bf16_f32 v115, v96, v97\n\t"
"v_mfma_f32_16x16x32_bf16 v[62:65], a[32:35], a[12:15], v[62:65]\n\t"
"s_add_u32 m0, s87, 41984\n\t"
"s_nop 0\n\t"
"buffer_load_dwordx4 v21, s[40:43], s68 offen offset:0 lds\n\t"
"buffer_store_dwordx4 v[112:115], v28, s[44:47], s82 offen offset:0 sc1\n\t"
"v_mul_f32 v86, s88, v116\n\t"
"v_mfma_f32_16x16x32_bf16 v[58:61], a[32:35], a[8:11], v[58:61]\n\t"
"s_waitcnt lgkmcnt(6)\n\t"
"v_mul_f32 v87, s88, v117\n\t"
"v_mul_f32 v88, s88, v118\n\t"
"v_mul_f32 v89, s88, v119\n\t"
"v_mfma_f32_16x16x32_bf16 v[54:57], a[32:35], a[4:7], v[54:57]\n\t"
"s_waitcnt lgkmcnt(3)\n\t"
"v_exp_f32 v86, v86\n\t"
"v_exp_f32 v87, v87\n\t"
"v_mfma_f32_16x16x32_bf16 v[50:53], a[32:35], a[0:3], v[50:53]\n\t"
"v_exp_f32 v88, v88\n\t"
"v_exp_f32 v89, v89\n\t"
"v_mfma_f32_16x16x32_bf16 v[78:81], a[36:39], a[24:27], v[78:81]\n\t"
"ds_read2_b32 a[32:33], v40 offset0:208 offset1:209\n\t"
"ds_read2_b32 a[34:35], v40 offset0:210 offset1:211\n\t"
"ds_read_b128 a[24:27], v42 offset:3328\n\t"
"v_fma_f32 v90, v86, 0.5, 1.0\n\t"
"v_mfma_f32_16x16x32_bf16 v[74:77], a[36:39], a[20:23], v[74:77]\n\t"
"v_fma_f32 v91, v87, 0.5, 1.0\n\t"
"v_fma_f32 v92, v88, 0.5, 1.0\n\t"
"v_fma_f32 v93, v89, 0.5, 1.0\n\t"
"v_mfma_f32_16x16x32_bf16 v[70:73], a[36:39], a[16:19], v[70:73]\n\t"
"v_fma_f32 v90, v90, v86, 1.0\n\t"
"v_fma_f32 v91, v91, v87, 1.0\n\t"
"v_fma_f32 v92, v92, v88, 1.0\n\t"
"v_fma_f32 v93, v93, v89, 1.0\n\t"
"v_mfma_f32_16x16x32_bf16 v[66:69], a[36:39], a[12:15], v[66:69]\n\t"
"v_rcp_f32 v90, v90\n\t"
"v_rcp_f32 v91, v91\n\t"
"v_mfma_f32_16x16x32_bf16 v[62:65], a[36:39], a[8:11], v[62:65]\n\t"
"v_rcp_f32 v92, v92\n\t"
"v_rcp_f32 v93, v93\n\t"
"v_mfma_f32_16x16x32_bf16 v[58:61], a[36:39], a[4:7], v[58:61]\n\t"
"v_fma_f32 v86, -v116, v90, v116\n\t"
"v_mfma_f32_16x16x32_bf16 v[54:57], a[36:39], a[0:3], v[54:57]\n\t"
"s_add_u32 m0, s87, 42880\n\t"
"s_nop 0\n\t"
"buffer_load_dwordx4 v20, s[40:43], s68 offen offset:128 lds\n\t"
"v_fma_f32 v87, -v117, v91, v117\n\t"
"v_fma_f32 v88, -v118, v92, v118\n\t"
"v_mfma_f32_16x16x32_bf16 v[78:81], a[40:43], a[20:23], v[78:81]\n\t"
"ds_read2_b32 a[36:37], v40 offset0:80 offset1:81\n\t"
"ds_read2_b32 a[38:39], v40 offset0:82 offset1:83\n\t"
"ds_read_b128 a[20:23], v42 offset:2816\n\t"
"v_fma_f32 v89, -v119, v93, v119\n\t"
"v_mfma_f32_16x16x32_bf16 v[74:77], a[40:43], a[16:19], v[74:77]\n\t"
"v_cvt_pk_bf16_f32 v116, v86, v87\n\t"
"v_cvt_pk_bf16_f32 v117, v88, v89\n\t"
"v_mul_f32 v94, s88, v132\n\t"
"v_mul_f32 v95, s88, v133\n\t"
"v_mfma_f32_16x16x32_bf16 v[70:73], a[40:43], a[12:15], v[70:73]\n\t"
"v_mul_f32 v96, s88, v134\n\t"
"v_mul_f32 v97, s88, v135\n\t"
"v_mfma_f32_16x16x32_bf16 v[66:69], a[40:43], a[8:11], v[66:69]\n\t"
"v_exp_f32 v94, v94\n\t"
"v_exp_f32 v95, v95\n\t"
"v_mfma_f32_16x16x32_bf16 v[62:65], a[40:43], a[4:7], v[62:65]\n\t"
"v_exp_f32 v96, v96\n\t"
"v_exp_f32 v97, v97\n\t"
"v_mfma_f32_16x16x32_bf16 v[58:61], a[40:43], a[0:3], v[58:61]\n\t"
"s_waitcnt lgkmcnt(7)\n\t"
"v_fma_f32 v98, v94, 0.5, 1.0\n\t"
"v_fma_f32 v99, v95, 0.5, 1.0\n\t"
"v_fma_f32 v100, v96, 0.5, 1.0\n\t"
"v_fma_f32 v101, v97, 0.5, 1.0\n\t"
"v_mfma_f32_16x16x32_bf16 v[78:81], a[44:47], a[16:19], v[78:81]\n\t"
"ds_read2_b32 a[40:41], v41 offset0:208 offset1:209\n\t"
"ds_read2_b32 a[42:43], v41 offset0:210 offset1:211\n\t"
"ds_read_b128 a[16:19], v42 offset:2304\n\t"
"v_mfma_f32_16x16x32_bf16 v[74:77], a[44:47], a[12:15], v[74:77]\n\t"
"v_fma_f32 v98, v98, v94, 1.0\n\t"
"v_fma_f32 v99, v99, v95, 1.0\n\t"
"v_fma_f32 v100, v100, v96, 1.0\n\t"
"v_fma_f32 v101, v101, v97, 1.0\n\t"
"v_mfma_f32_16x16x32_bf16 v[70:73], a[44:47], a[8:11], v[70:73]\n\t"
"v_rcp_f32 v98, v98\n\t"
"v_rcp_f32 v99, v99\n\t"
"v_mfma_f32_16x16x32_bf16 v[66:69], a[44:47], a[4:7], v[66:69]\n\t"
"s_add_u32 m0, s87, 43904\n\t"
"s_nop 0\n\t"
"buffer_load_dwordx4 v21, s[40:43], s68 offen offset:128 lds\n\t"
"v_rcp_f32 v100, v100\n\t"
"v_mfma_f32_16x16x32_bf16 v[62:65], a[44:47], a[0:3], v[62:65]\n\t"
"s_waitcnt lgkmcnt(7)\n\t"
"v_rcp_f32 v101, v101\n\t"
"v_fma_f32 v94, -v132, v98, v132\n\t"
"v_mfma_f32_16x16x32_bf16 v[78:81], a[32:35], a[12:15], v[78:81]\n\t"
"ds_read2_b32 a[44:45], v41 offset0:80 offset1:81\n\t"
"ds_read2_b32 a[46:47], v41 offset0:82 offset1:83\n\t"
"ds_read_b128 a[12:15], v42 offset:1792\n\t"
"v_fma_f32 v95, -v133, v99, v133\n\t"
"v_mfma_f32_16x16x32_bf16 v[74:77], a[32:35], a[8:11], v[74:77]\n\t"
"v_fma_f32 v96, -v134, v100, v134\n\t"
"v_fma_f32 v97, -v135, v101, v135\n\t"
"v_cvt_pk_bf16_f32 v118, v94, v95\n\t"
"v_cvt_pk_bf16_f32 v119, v96, v97\n\t"
"v_mfma_f32_16x16x32_bf16 v[70:73], a[32:35], a[4:7], v[70:73]\n\t"
"buffer_store_dwordx4 v[116:119], v28, s[44:47], s82 offen offset:1024 sc1\n\t"
"v_mul_f32 v86, s88, v120\n\t"
"v_mul_f32 v87, s88, v121\n\t"
"v_mfma_f32_16x16x32_bf16 v[66:69], a[32:35], a[0:3], v[66:69]\n\t"
"s_waitcnt lgkmcnt(7)\n\t"
"v_mul_f32 v88, s88, v122\n\t"
"v_mul_f32 v89, s88, v123\n\t"
"v_exp_f32 v86, v86\n\t"
"v_mfma_f32_16x16x32_bf16 v[78:81], a[36:39], a[8:11], v[78:81]\n\t"
"ds_read2_b32 a[32:33], v38 offset0:192 offset1:193\n\t"
"ds_read2_b32 a[34:35], v38 offset0:194 offset1:195\n\t"
"ds_read_b128 a[8:11], v42 offset:1280\n\t"
"v_mfma_f32_16x16x32_bf16 v[74:77], a[36:39], a[4:7], v[74:77]\n\t"
"v_exp_f32 v87, v87\n\t"
"v_exp_f32 v88, v88\n\t"
"v_mfma_f32_16x16x32_bf16 v[70:73], a[36:39], a[0:3], v[70:73]\n\t"
"s_waitcnt lgkmcnt(7)\n\t"
"v_exp_f32 v89, v89\n\t"
"v_fma_f32 v90, v86, 0.5, 1.0\n\t"
"v_fma_f32 v91, v87, 0.5, 1.0\n\t"
"v_mfma_f32_16x16x32_bf16 v[78:81], a[40:43], a[4:7], v[78:81]\n\t"
"ds_read2_b32 a[36:37], v38 offset0:64 offset1:65\n\t"
"ds_read2_b32 a[38:39], v38 offset0:66 offset1:67\n\t"
"ds_read_b128 a[4:7], v42 offset:768\n\t"
"v_fma_f32 v92, v88, 0.5, 1.0\n\t"
"v_mfma_f32_16x16x32_bf16 v[74:77], a[40:43], a[0:3], v[74:77]\n\t"
"s_waitcnt lgkmcnt(7)\n\t"
"v_fma_f32 v93, v89, 0.5, 1.0\n\t"
"v_fma_f32 v90, v90, v86, 1.0\n\t"
"v_mfma_f32_16x16x32_bf16 v[78:81], a[44:47], a[0:3], v[78:81]\n\t"
"s_waitcnt lgkmcnt(4)\n\t"
"ds_read2_b32 a[40:41], v39 offset0:192 offset1:193\n\t"
"ds_read2_b32 a[42:43], v39 offset0:194 offset1:195\n\t"
"ds_read_b128 a[0:3], v42 offset:256\n\t"
"v_mfma_f32_16x16x32_bf16 v[78:81], a[32:35], a[28:31], v[78:81]\n\t"
"ds_read2_b32 a[44:45], v39 offset0:64 offset1:65\n\t"
"ds_read2_b32 a[46:47], v39 offset0:66 offset1:67\n\t"
"ds_read_b128 a[28:31], v42 offset:3776\n\t"
"v_mfma_f32_16x16x32_bf16 v[74:77], a[32:35], a[24:27], v[74:77]\n\t"
"s_add_u32 m0, s87, 45056\n\t"
"s_nop 0\n\t"
"buffer_load_dwordx4 v22, s[40:43], s68 offen lds\n\t"
"v_fma_f32 v91, v91, v87, 1.0\n\t"
"v_fma_f32 v92, v92, v88, 1.0\n\t"
"v_mfma_f32_16x16x32_bf16 v[70:73], a[32:35], a[20:23], v[70:73]\n\t"
"v_fma_f32 v93, v93, v89, 1.0\n\t"
"v_rcp_f32 v90, v90\n\t"
"v_mfma_f32_16x16x32_bf16 v[66:69], a[32:35], a[16:19], v[66:69]\n\t"
"s_waitcnt lgkmcnt(9)\n\t"
"v_rcp_f32 v91, v91\n\t"
"v_rcp_f32 v92, v92\n\t"
"v_mfma_f32_16x16x32_bf16 v[62:65], a[32:35], a[12:15], v[62:65]\n\t"
"v_rcp_f32 v93, v93\n\t"
"v_fma_f32 v86, -v120, v90, v120\n\t"
"v_fma_f32 v87, -v121, v91, v121\n\t"
"v_mfma_f32_16x16x32_bf16 v[58:61], a[32:35], a[8:11], v[58:61]\n\t"
"s_waitcnt lgkmcnt(6)\n\t"
"v_fma_f32 v88, -v122, v92, v122\n\t"
"v_fma_f32 v89, -v123, v93, v123\n\t"
"v_cvt_pk_bf16_f32 v120, v86, v87\n\t"
"v_cvt_pk_bf16_f32 v121, v88, v89\n\t"
"v_mfma_f32_16x16x32_bf16 v[54:57], a[32:35], a[4:7], v[54:57]\n\t"
"s_waitcnt lgkmcnt(3)\n\t"
"v_mul_f32 v94, s88, v136\n\t"
"v_mul_f32 v95, s88, v137\n\t"
"v_mul_f32 v96, s88, v138\n\t"
"v_mfma_f32_16x16x32_bf16 v[50:53], a[32:35], a[0:3], v[50:53]\n\t"
"v_mul_f32 v97, s88, v139\n\t"
"v_exp_f32 v94, v94\n\t"
"v_mfma_f32_16x16x32_bf16 v[78:81], a[36:39], a[24:27], v[78:81]\n\t"
"ds_read2_b32 a[32:33], v40 offset0:192 offset1:193\n\t"
"ds_read2_b32 a[34:35], v40 offset0:194 offset1:195\n\t"
"ds_read_b128 a[24:27], v42 offset:3264\n\t"
"v_mfma_f32_16x16x32_bf16 v[74:77], a[36:39], a[20:23], v[74:77]\n\t"
"v_exp_f32 v95, v95\n\t"
"v_exp_f32 v96, v96\n\t"
"v_mfma_f32_16x16x32_bf16 v[70:73], a[36:39], a[16:19], v[70:73]\n\t"
"v_exp_f32 v97, v97\n\t"
"v_fma_f32 v98, v94, 0.5, 1.0\n\t"
"v_fma_f32 v99, v95, 0.5, 1.0\n\t"
"v_mfma_f32_16x16x32_bf16 v[66:69], a[36:39], a[12:15], v[66:69]\n\t"
"v_fma_f32 v100, v96, 0.5, 1.0\n\t"
"v_fma_f32 v101, v97, 0.5, 1.0\n\t"
"v_fma_f32 v98, v98, v94, 1.0\n\t"
"v_fma_f32 v99, v99, v95, 1.0\n\t"
"v_mfma_f32_16x16x32_bf16 v[62:65], a[36:39], a[8:11], v[62:65]\n\t"
"v_fma_f32 v100, v100, v96, 1.0\n\t"
"v_fma_f32 v101, v101, v97, 1.0\n\t"
"v_mfma_f32_16x16x32_bf16 v[58:61], a[36:39], a[4:7], v[58:61]\n\t"
"v_rcp_f32 v98, v98\n\t"
"v_rcp_f32 v99, v99\n\t"
"v_mfma_f32_16x16x32_bf16 v[54:57], a[36:39], a[0:3], v[54:57]\n\t"
"v_rcp_f32 v100, v100\n\t"
"v_rcp_f32 v101, v101\n\t"
"v_mfma_f32_16x16x32_bf16 v[78:81], a[40:43], a[20:23], v[78:81]\n\t"
"ds_read2_b32 a[36:37], v40 offset0:64 offset1:65\n\t"
"ds_read2_b32 a[38:39], v40 offset0:66 offset1:67\n\t"
"ds_read_b128 a[20:23], v42 offset:2752\n\t"
"v_fma_f32 v94, -v136, v98, v136\n\t"
"v_mfma_f32_16x16x32_bf16 v[74:77], a[40:43], a[16:19], v[74:77]\n\t"
"v_fma_f32 v95, -v137, v99, v137\n\t"
"v_fma_f32 v96, -v138, v100, v138\n\t"
"v_fma_f32 v97, -v139, v101, v139\n\t"
"v_mfma_f32_16x16x32_bf16 v[70:73], a[40:43], a[12:15], v[70:73]\n\t"
"v_cvt_pk_bf16_f32 v122, v94, v95\n\t"
"v_cvt_pk_bf16_f32 v123, v96, v97\n\t"
"v_mfma_f32_16x16x32_bf16 v[66:69], a[40:43], a[8:11], v[66:69]\n\t"
"buffer_store_dwordx4 v[120:123], v28, s[44:47], s82 offen offset:2048 sc1\n\t"
"v_mul_f32 v86, s88, v124\n\t"
"v_mul_f32 v87, s88, v125\n\t"
"v_mul_f32 v88, s88, v126\n\t"
"v_mfma_f32_16x16x32_bf16 v[62:65], a[40:43], a[4:7], v[62:65]\n\t"
"v_mul_f32 v89, s88, v127\n\t"
"v_exp_f32 v86, v86\n\t"
"v_mfma_f32_16x16x32_bf16 v[58:61], a[40:43], a[0:3], v[58:61]\n\t"
"s_waitcnt lgkmcnt(7)\n\t"
"v_exp_f32 v87, v87\n\t"
"v_exp_f32 v88, v88\n\t"
"v_mfma_f32_16x16x32_bf16 v[78:81], a[44:47], a[16:19], v[78:81]\n\t"
"ds_read2_b32 a[40:41], v41 offset0:192 offset1:193\n\t"
"ds_read2_b32 a[42:43], v41 offset0:194 offset1:195\n\t"
"ds_read_b128 a[16:19], v42 offset:2240\n\t"
"v_mfma_f32_16x16x32_bf16 v[74:77], a[44:47], a[12:15], v[74:77]\n\t"
"v_exp_f32 v89, v89\n\t"
"v_fma_f32 v90, v86, 0.5, 1.0\n\t"
"v_fma_f32 v91, v87, 0.5, 1.0\n\t"
"v_mfma_f32_16x16x32_bf16 v[70:73], a[44:47], a[8:11], v[70:73]\n\t"
"v_fma_f32 v92, v88, 0.5, 1.0\n\t"
"v_fma_f32 v93, v89, 0.5, 1.0\n\t"
"v_fma_f32 v90, v90, v86, 1.0\n\t"
"v_fma_f32 v91, v91, v87, 1.0\n\t"
"v_mfma_f32_16x16x32_bf16 v[66:69], a[44:47], a[4:7], v[66:69]\n\t"
"v_fma_f32 v92, v92, v88, 1.0\n\t"
"v_fma_f32 v93, v93, v89, 1.0\n\t"
"v_rcp_f32 v90, v90\n\t"
"v_mfma_f32_16x16x32_bf16 v[62:65], a[44:47], a[0:3], v[62:65]\n\t"
"s_waitcnt lgkmcnt(7)\n\t"
"v_rcp_f32 v91, v91\n\t"
"v_mfma_f32_16x16x32_bf16 v[78:81], a[32:35], a[12:15], v[78:81]\n\t"
"ds_read2_b32 a[44:45], v41 offset0:64 offset1:65\n\t"
"ds_read2_b32 a[46:47], v41 offset0:66 offset1:67\n\t"
"ds_read_b128 a[12:15], v42 offset:1728\n\t"
"v_mfma_f32_16x16x32_bf16 v[74:77], a[32:35], a[8:11], v[74:77]\n\t"
"v_rcp_f32 v92, v92\n\t"
"v_rcp_f32 v93, v93\n\t"
"v_mfma_f32_16x16x32_bf16 v[70:73], a[32:35], a[4:7], v[70:73]\n\t"
"s_waitcnt vmcnt(8)\n\t"
"v_cndmask_b32 v111, 1, 1, s[10:11]\n\t"
"v_add_u32 v111, v111, v37\n\t"
"buffer_store_dword v111, v47, s[44:47], 0 offen sc1\n\t"
"v_mfma_f32_16x16x32_bf16 v[66:69], a[32:35], a[0:3], v[66:69]\n\t"
"s_waitcnt lgkmcnt(7)\n\t"
"v_fma_f32 v86, -v124, v90, v124\n\t"
"v_fma_f32 v87, -v125, v91, v125\n\t"
"v_fma_f32 v88, -v126, v92, v126\n\t"
"v_mfma_f32_16x16x32_bf16 v[78:81], a[36:39], a[8:11], v[78:81]\n\t"
"ds_read2_b32 a[32:33], v38 offset0:176 offset1:177\n\t"
"ds_read2_b32 a[34:35], v38 offset0:178 offset1:179\n\t"
"ds_read_b128 a[8:11], v42 offset:1216\n\t"
"v_mfma_f32_16x16x32_bf16 v[74:77], a[36:39], a[4:7], v[74:77]\n\t"
"v_fma_f32 v89, -v127, v93, v127\n\t"
"v_cvt_pk_bf16_f32 v124, v86, v87\n\t"
"v_cvt_pk_bf16_f32 v125, v88, v89\n\t"
"v_mul_f32 v94, s88, v140\n\t"
"v_mfma_f32_16x16x32_bf16 v[70:73], a[36:39], a[0:3], v[70:73]\n\t"
"s_waitcnt lgkmcnt(7)\n\t"
"v_mul_f32 v95, s88, v141\n\t"
"v_mul_f32 v96, s88, v142\n\t"
"v_mul_f32 v97, s88, v143\n\t"
"v_mfma_f32_16x16x32_bf16 v[78:81], a[40:43], a[4:7], v[78:81]\n\t"
"ds_read2_b32 a[36:37], v38 offset0:48 offset1:49\n\t"
"ds_read2_b32 a[38:39], v38 offset0:50 offset1:51\n\t"
"ds_read_b128 a[4:7], v42 offset:704\n\t"
"v_mfma_f32_16x16x32_bf16 v[74:77], a[40:43], a[0:3], v[74:77]\n\t"
"s_waitcnt lgkmcnt(7)\n\t"
"v_exp_f32 v94, v94\n\t"
"v_exp_f32 v95, v95\n\t"
"v_mfma_f32_16x16x32_bf16 v[78:81], a[44:47], a[0:3], v[78:81]\n\t"
"ds_read2_b32 a[40:41], v39 offset0:176 offset1:177\n\t"
"ds_read2_b32 a[42:43], v39 offset0:178 offset1:179\n\t"
"ds_read_b128 a[0:3], v42 offset:192\n\t"
"v_exp_f32 v96, v96\n\t"
"v_exp_f32 v97, v97\n\t"
"v_fma_f32 v98, v94, 0.5, 1.0\n\t"
"v_fma_f32 v99, v95, 0.5, 1.0\n\t"
"v_fma_f32 v100, v96, 0.5, 1.0\n\t"
"v_fma_f32 v101, v97, 0.5, 1.0\n\t"
"v_fma_f32 v98, v98, v94, 1.0\n\t"
"v_fma_f32 v99, v99, v95, 1.0\n\t"
"v_fma_f32 v100, v100, v96, 1.0\n\t"
"v_fma_f32 v101, v101, v97, 1.0\n\t"
"v_rcp_f32 v98, v98\n\t"
"v_rcp_f32 v99, v99\n\t"
"v_rcp_f32 v100, v100\n\t"
"v_rcp_f32 v101, v101\n\t"
"v_fma_f32 v94, -v140, v98, v140\n\t"
"v_fma_f32 v95, -v141, v99, v141\n\t"
"v_fma_f32 v96, -v142, v100, v142\n\t"
"v_fma_f32 v97, -v143, v101, v143\n\t"
"v_cvt_pk_bf16_f32 v126, v94, v95\n\t"
"v_cvt_pk_bf16_f32 v127, v96, v97\n\t"
"buffer_store_dwordx4 v[124:127], v28, s[44:47], s82 offen offset:3072 sc1\n\t"
"s_waitcnt lgkmcnt(7)\n\t"
"v_mfma_f32_16x16x32_bf16 v[78:81], a[32:35], a[28:31], v[78:81]\n\t"
"ds_read2_b32 a[44:45], v39 offset0:48 offset1:49\n\t"
"ds_read2_b32 a[46:47], v39 offset0:50 offset1:51\n\t"
"ds_read_b128 a[28:31], v42 offset:3712\n\t"
"v_mfma_f32_16x16x32_bf16 v[74:77], a[32:35], a[24:27], v[74:77]\n\t"
"v_mov_b32 v111, 2146435072\n\t"
"v_cndmask_b32 v47, v111, v47, s[10:11]\n\t"
"v_mfma_f32_16x16x32_bf16 v[70:73], a[32:35], a[20:23], v[70:73]\n\t"
"v_mul_f32 v86, s88, v144\n\t"
"v_mul_f32 v87, s88, v145\n\t"
"v_mfma_f32_16x16x32_bf16 v[66:69], a[32:35], a[16:19], v[66:69]\n\t"
"s_waitcnt lgkmcnt(9)\n\t"
"v_mul_f32 v88, s88, v146\n\t"
"v_mul_f32 v89, s88, v147\n\t"
"v_mfma_f32_16x16x32_bf16 v[62:65], a[32:35], a[12:15], v[62:65]\n\t"
"v_exp_f32 v86, v86\n\t"
"v_mfma_f32_16x16x32_bf16 v[58:61], a[32:35], a[8:11], v[58:61]\n\t"
"s_waitcnt lgkmcnt(6)\n\t"
"v_exp_f32 v87, v87\n\t"
"v_mfma_f32_16x16x32_bf16 v[54:57], a[32:35], a[4:7], v[54:57]\n\t"
"s_waitcnt lgkmcnt(3)\n\t"
"s_add_u32 m0, s87, 61440\n\t"
"s_nop 0\n\t"
"buffer_load_dwordx4 v20, s[40:43], s69 offen offset:0 lds\n\t"
"v_mfma_f32_16x16x32_bf16 v[50:53], a[32:35], a[0:3], v[50:53]\n\t"
"v_exp_f32 v88, v88\n\t"
"v_mfma_f32_16x16x32_bf16 v[78:81], a[36:39], a[24:27], v[78:81]\n\t"
"ds_read2_b32 a[32:33], v40 offset0:176 offset1:177\n\t"
"ds_read2_b32 a[34:35], v40 offset0:178 offset1:179\n\t"
"ds_read_b128 a[24:27], v42 offset:3200\n\t"
"v_mfma_f32_16x16x32_bf16 v[74:77], a[36:39], a[20:23], v[74:77]\n\t"
"v_exp_f32 v89, v89\n\t"
"v_fma_f32 v90, v86, 0.5, 1.0\n\t"
"v_mfma_f32_16x16x32_bf16 v[70:73], a[36:39], a[16:19], v[70:73]\n\t"
"v_fma_f32 v91, v87, 0.5, 1.0\n\t"
"v_fma_f32 v92, v88, 0.5, 1.0\n\t"
"v_mfma_f32_16x16x32_bf16 v[66:69], a[36:39], a[12:15], v[66:69]\n\t"
"v_fma_f32 v93, v89, 0.5, 1.0\n\t"
"v_fma_f32 v90, v90, v86, 1.0\n\t"
"v_fma_f32 v91, v91, v87, 1.0\n\t"
"v_mfma_f32_16x16x32_bf16 v[62:65], a[36:39], a[8:11], v[62:65]\n\t"
"v_fma_f32 v92, v92, v88, 1.0\n\t"
"v_fma_f32 v93, v93, v89, 1.0\n\t"
"v_mfma_f32_16x16x32_bf16 v[58:61], a[36:39], a[4:7], v[58:61]\n\t"
"v_rcp_f32 v90, v90\n\t"
"v_mfma_f32_16x16x32_bf16 v[54:57], a[36:39], a[0:3], v[54:57]\n\t"
"v_rcp_f32 v91, v91\n\t"
"v_mfma_f32_16x16x32_bf16 v[78:81], a[40:43], a[20:23], v[78:81]\n\t"
"ds_read2_b32 a[36:37], v40 offset0:48 offset1:49\n\t"
"ds_read2_b32 a[38:39], v40 offset0:50 offset1:51\n\t"
"ds_read_b128 a[20:23], v42 offset:2688\n\t"
"v_mfma_f32_16x16x32_bf16 v[74:77], a[40:43], a[16:19], v[74:77]\n\t"
"v_rcp_f32 v92, v92\n\t"
"v_mfma_f32_16x16x32_bf16 v[70:73], a[40:43], a[12:15], v[70:73]\n\t"
"v_rcp_f32 v93, v93\n\t"
"v_fma_f32 v86, -v144, v90, v144\n\t"
"v_mfma_f32_16x16x32_bf16 v[66:69], a[40:43], a[8:11], v[66:69]\n\t"
"v_fma_f32 v87, -v145, v91, v145\n\t"
"v_fma_f32 v88, -v146, v92, v146\n\t"
"v_mfma_f32_16x16x32_bf16 v[62:65], a[40:43], a[4:7], v[62:65]\n\t"
"s_add_u32 m0, s87, 62464\n\t"
"s_nop 0\n\t"
"buffer_load_dwordx4 v21, s[40:43], s69 offen offset:0 lds\n\t"
"v_fma_f32 v89, -v147, v93, v147\n\t"
"v_mfma_f32_16x16x32_bf16 v[58:61], a[40:43], a[0:3], v[58:61]\n\t"
"s_waitcnt lgkmcnt(7)\n\t"
"v_cvt_pk_bf16_f32 v144, v86, v87\n\t"
"v_cvt_pk_bf16_f32 v145, v88, v89\n\t"
"v_mul_f32 v94, s88, v160\n\t"
"v_mfma_f32_16x16x32_bf16 v[78:81], a[44:47], a[16:19], v[78:81]\n\t"
"ds_read2_b32 a[40:41], v41 offset0:176 offset1:177\n\t"
"ds_read2_b32 a[42:43], v41 offset0:178 offset1:179\n\t"
"ds_read_b128 a[16:19], v42 offset:2176\n\t"
"v_mfma_f32_16x16x32_bf16 v[74:77], a[44:47], a[12:15], v[74:77]\n\t"
"v_mul_f32 v95, s88, v161\n\t"
"v_mul_f32 v96, s88, v162\n\t"
"v_mfma_f32_16x16x32_bf16 v[70:73], a[44:47], a[8:11], v[70:73]\n\t"
"v_mul_f32 v97, s88, v163\n\t"
"v_exp_f32 v94, v94\n\t"
"v_mfma_f32_16x16x32_bf16 v[66:69], a[44:47], a[4:7], v[66:69]\n\t"
"v_exp_f32 v95, v95\n\t"
"v_mfma_f32_16x16x32_bf16 v[62:65], a[44:47], a[0:3], v[62:65]\n\t"
"s_waitcnt lgkmcnt(7)\n\t"
"v_exp_f32 v96, v96\n\t"
"v_mfma_f32_16x16x32_bf16 v[78:81], a[32:35], a[12:15], v[78:81]\n\t"
"ds_read2_b32 a[44:45], v41 offset0:48 offset1:49\n\t"
"ds_read2_b32 a[46:47], v41 offset0:50 offset1:51\n\t"
"ds_read_b128 a[12:15], v42 offset:1664\n\t"
"v_mfma_f32_16x16x32_bf16 v[74:77], a[32:35], a[8:11], v[74:77]\n\t"
"v_exp_f32 v97, v97\n\t"
"v_fma_f32 v98, v94, 0.5, 1.0\n\t"
"v_mfma_f32_16x16x32_bf16 v[70:73], a[32:35], a[4:7], v[70:73]\n\t"
"v_fma_f32 v99, v95, 0.5, 1.0\n\t"
"v_fma_f32 v100, v96, 0.5, 1.0\n\t"
"v_mfma_f32_16x16x32_bf16 v[66:69], a[32:35], a[0:3], v[66:69]\n\t"
"s_waitcnt lgkmcnt(7)\n\t"
"v_fma_f32 v101, v97, 0.5, 1.0\n\t"
"v_fma_f32 v98, v98, v94, 1.0\n\t"
"v_fma_f32 v99, v99, v95, 1.0\n\t"
"v_mfma_f32_16x16x32_bf16 v[78:81], a[36:39], a[8:11], v[78:81]\n\t"
"ds_read2_b32 a[32:33], v38 offset0:160 offset1:161\n\t"
"ds_read2_b32 a[34:35], v38 offset0:162 offset1:163\n\t"
"ds_read_b128 a[8:11], v42 offset:1152\n\t"
"v_mfma_f32_16x16x32_bf16 v[74:77], a[36:39], a[4:7], v[74:77]\n\t"
"s_add_u32 m0, s87, 63360\n\t"
"s_nop 0\n\t"
"buffer_load_dwordx4 v20, s[40:43], s69 offen offset:128 lds\n\t"
"v_mfma_f32_16x16x32_bf16 v[70:73], a[36:39], a[0:3], v[70:73]\n\t"
"s_waitcnt lgkmcnt(7)\n\t"
"v_fma_f32 v100, v100, v96, 1.0\n\t"
"v_fma_f32 v101, v101, v97, 1.0\n\t"
"v_mfma_f32_16x16x32_bf16 v[78:81], a[40:43], a[4:7], v[78:81]\n\t"
"ds_read2_b32 a[36:37], v38 offset0:32 offset1:33\n\t"
"ds_read2_b32 a[38:39], v38 offset0:34 offset1:35\n\t"
"ds_read_b128 a[4:7], v42 offset:640\n\t"
"v_mfma_f32_16x16x32_bf16 v[74:77], a[40:43], a[0:3], v[74:77]\n\t"
"s_waitcnt lgkmcnt(7)\n\t"
"v_rcp_f32 v98, v98\n\t"
"v_mfma_f32_16x16x32_bf16 v[78:81], a[44:47], a[0:3], v[78:81]\n\t"
"s_waitcnt lgkmcnt(4)\n\t"
"ds_read2_b32 a[40:41], v39 offset0:160 offset1:161\n\t"
"ds_read2_b32 a[42:43], v39 offset0:162 offset1:163\n\t"
"ds_read_b128 a[0:3], v42 offset:128\n\t"
"v_mfma_f32_16x16x32_bf16 v[78:81], a[32:35], a[28:31], v[78:81]\n\t"
"ds_read2_b32 a[44:45], v39 offset0:32 offset1:33\n\t"
"ds_read2_b32 a[46:47], v39 offset0:34 offset1:35\n\t"
"ds_read_b128 a[28:31], v42 offset:3648\n\t"
"v_mfma_f32_16x16x32_bf16 v[74:77], a[32:35], a[24:27], v[74:77]\n\t"
"v_rcp_f32 v99, v99\n\t"
"v_mfma_f32_16x16x32_bf16 v[70:73], a[32:35], a[20:23], v[70:73]\n\t"
"v_rcp_f32 v100, v100\n\t"
"v_mfma_f32_16x16x32_bf16 v[66:69], a[32:35], a[16:19], v[66:69]\n\t"
"s_waitcnt lgkmcnt(9)\n\t"
"v_rcp_f32 v101, v101\n\t"
"v_fma_f32 v94, -v160, v98, v160\n\t"
"v_mfma_f32_16x16x32_bf16 v[62:65], a[32:35], a[12:15], v[62:65]\n\t"
"v_fma_f32 v95, -v161, v99, v161\n\t"
"v_fma_f32 v96, -v162, v100, v162\n\t"
"v_fma_f32 v97, -v163, v101, v163\n\t"
"v_mfma_f32_16x16x32_bf16 v[58:61], a[32:35], a[8:11], v[58:61]\n\t"
"s_waitcnt lgkmcnt(6)\n\t"
"v_cvt_pk_bf16_f32 v146, v94, v95\n\t"
"v_cvt_pk_bf16_f32 v147, v96, v97\n\t"
"v_mfma_f32_16x16x32_bf16 v[54:57], a[32:35], a[4:7], v[54:57]\n\t"
"s_waitcnt lgkmcnt(3)\n\t"
"buffer_store_dwordx4 v[144:147], v29, s[44:47], s82 offen offset:0 sc1\n\t"
"v_mul_f32 v86, s88, v148\n\t"
"v_mul_f32 v87, s88, v149\n\t"
"v_mfma_f32_16x16x32_bf16 v[50:53], a[32:35], a[0:3], v[50:53]\n\t"
"v_mul_f32 v88, s88, v150\n\t"
"v_mul_f32 v89, s88, v151\n\t"
"v_mfma_f32_16x16x32_bf16 v[78:81], a[36:39], a[24:27], v[78:81]\n\t"
"ds_read2_b32 a[32:33], v40 offset0:160 offset1:161\n\t"
"ds_read2_b32 a[34:35], v40 offset0:162 offset1:163\n\t"
"ds_read_b128 a[24:27], v42 offset:3136\n\t"
"v_mfma_f32_16x16x32_bf16 v[74:77], a[36:39], a[20:23], v[74:77]\n\t"
"s_add_u32 m0, s87, 64384\n\t"
"s_nop 0\n\t"
"buffer_load_dwordx4 v21, s[40:43], s69 offen offset:128 lds\n\t"
"v_mfma_f32_16x16x32_bf16 v[70:73], a[36:39], a[16:19], v[70:73]\n\t"
"v_exp_f32 v86, v86\n\t"
"v_mfma_f32_16x16x32_bf16 v[66:69], a[36:39], a[12:15], v[66:69]\n\t"
"v_exp_f32 v87, v87\n\t"
"v_mfma_f32_16x16x32_bf16 v[62:65], a[36:39], a[8:11], v[62:65]\n\t"
"v_exp_f32 v88, v88\n\t"
"v_mfma_f32_16x16x32_bf16 v[58:61], a[36:39], a[4:7], v[58:61]\n\t"
"v_exp_f32 v89, v89\n\t"
"v_fma_f32 v90, v86, 0.5, 1.0\n\t"
"v_mfma_f32_16x16x32_bf16 v[54:57], a[36:39], a[0:3], v[54:57]\n\t"
"v_fma_f32 v91, v87, 0.5, 1.0\n\t"
"v_fma_f32 v92, v88, 0.5, 1.0\n\t"
"v_fma_f32 v93, v89, 0.5, 1.0\n\t"
"v_mfma_f32_16x16x32_bf16 v[78:81], a[40:43], a[20:23], v[78:81]\n\t"
"ds_read2_b32 a[36:37], v40 offset0:32 offset1:33\n\t"
"ds_read2_b32 a[38:39], v40 offset0:34 offset1:35\n\t"
"ds_read_b128 a[20:23], v42 offset:2624\n\t"
"v_mfma_f32_16x16x32_bf16 v[74:77], a[40:43], a[16:19], v[74:77]\n\t"
"v_fma_f32 v90, v90, v86, 1.0\n\t"
"v_fma_f32 v91, v91, v87, 1.0\n\t"
"v_mfma_f32_16x16x32_bf16 v[70:73], a[40:43], a[12:15], v[70:73]\n\t"
"v_fma_f32 v92, v92, v88, 1.0\n\t"
"v_fma_f32 v93, v93, v89, 1.0\n\t"
"v_mfma_f32_16x16x32_bf16 v[66:69], a[40:43], a[8:11], v[66:69]\n\t"
"v_rcp_f32 v90, v90\n\t"
"v_mfma_f32_16x16x32_bf16 v[62:65], a[40:43], a[4:7], v[62:65]\n\t"
"v_rcp_f32 v91, v91\n\t"
"v_mfma_f32_16x16x32_bf16 v[58:61], a[40:43], a[0:3], v[58:61]\n\t"
"s_waitcnt lgkmcnt(7)\n\t"
"v_rcp_f32 v92, v92\n\t"
"v_mfma_f32_16x16x32_bf16 v[78:81], a[44:47], a[16:19], v[78:81]\n\t"
"ds_read2_b32 a[40:41], v41 offset0:160 offset1:161\n\t"
"ds_read2_b32 a[42:43], v41 offset0:162 offset1:163\n\t"
"ds_read_b128 a[16:19], v42 offset:2112\n\t"
"v_mfma_f32_16x16x32_bf16 v[74:77], a[44:47], a[12:15], v[74:77]\n\t"
"v_rcp_f32 v93, v93\n\t"
"v_fma_f32 v86, -v148, v90, v148\n\t"
"v_mfma_f32_16x16x32_bf16 v[70:73], a[44:47], a[8:11], v[70:73]\n\t"
"v_fma_f32 v87, -v149, v91, v149\n\t"
"v_mfma_f32_16x16x32_bf16 v[66:69], a[44:47], a[4:7], v[66:69]\n\t"
"s_add_u32 m0, s87, 65536\n\t"
"s_nop 0\n\t"
"buffer_load_dwordx4 v22, s[40:43], s69 offen lds\n\t"
"v_fma_f32 v88, -v150, v92, v150\n\t"
"v_mfma_f32_16x16x32_bf16 v[62:65], a[44:47], a[0:3], v[62:65]\n\t"
"s_waitcnt lgkmcnt(7)\n\t"
"v_fma_f32 v89, -v151, v93, v151\n\t"
"v_cvt_pk_bf16_f32 v148, v86, v87\n\t"
"v_cvt_pk_bf16_f32 v149, v88, v89\n\t"
"v_mfma_f32_16x16x32_bf16 v[78:81], a[32:35], a[12:15], v[78:81]\n\t"
"ds_read2_b32 a[44:45], v41 offset0:32 offset1:33\n\t"
"ds_read2_b32 a[46:47], v41 offset0:34 offset1:35\n\t"
"ds_read_b128 a[12:15], v42 offset:1600\n\t"
"v_mfma_f32_16x16x32_bf16 v[74:77], a[32:35], a[8:11], v[74:77]\n\t"
"v_mul_f32 v94, s88, v164\n\t"
"v_mul_f32 v95, s88, v165\n\t"
"v_mfma_f32_16x16x32_bf16 v[70:73], a[32:35], a[4:7], v[70:73]\n\t"
"v_mul_f32 v96, s88, v166\n\t"
"v_mul_f32 v97, s88, v167\n\t"
"v_mfma_f32_16x16x32_bf16 v[66:69], a[32:35], a[0:3], v[66:69]\n\t"
"s_waitcnt lgkmcnt(7)\n\t"
"v_exp_f32 v94, v94\n\t"
"v_mfma_f32_16x16x32_bf16 v[78:81], a[36:39], a[8:11], v[78:81]\n\t"
"ds_read2_b32 a[32:33], v38 offset0:144 offset1:145\n\t"
"ds_read2_b32 a[34:35], v38 offset0:146 offset1:147\n\t"
"ds_read_b128 a[8:11], v42 offset:1088\n\t"
"v_mfma_f32_16x16x32_bf16 v[74:77], a[36:39], a[4:7], v[74:77]\n\t"
"v_exp_f32 v95, v95\n\t"
"v_mfma_f32_16x16x32_bf16 v[70:73], a[36:39], a[0:3], v[70:73]\n\t"
"s_waitcnt lgkmcnt(7)\n\t"
"v_exp_f32 v96, v96\n\t"
"v_mfma_f32_16x16x32_bf16 v[78:81], a[40:43], a[4:7], v[78:81]\n\t"
"ds_read2_b32 a[36:37], v38 offset0:16 offset1:17\n\t"
"ds_read2_b32 a[38:39], v38 offset0:18 offset1:19\n\t"
"ds_read_b128 a[4:7], v42 offset:576\n\t"
"v_mfma_f32_16x16x32_bf16 v[74:77], a[40:43], a[0:3], v[74:77]\n\t"
"s_waitcnt lgkmcnt(7)\n\t"
"v_exp_f32 v97, v97\n\t"
"v_fma_f32 v98, v94, 0.5, 1.0\n\t"
"v_mfma_f32_16x16x32_bf16 v[78:81], a[44:47], a[0:3], v[78:81]\n\t"
"s_waitcnt lgkmcnt(4)\n\t"
"ds_read2_b32 a[40:41], v39 offset0:144 offset1:145\n\t"
"ds_read2_b32 a[42:43], v39 offset0:146 offset1:147\n\t"
"ds_read_b128 a[0:3], v42 offset:64\n\t"
"v_mfma_f32_16x16x32_bf16 v[78:81], a[32:35], a[28:31], v[78:81]\n\t"
"ds_read2_b32 a[44:45], v39 offset0:16 offset1:17\n\t"
"ds_read2_b32 a[46:47], v39 offset0:18 offset1:19\n\t"
"ds_read_b128 a[28:31], v42 offset:3584\n\t"
"v_mfma_f32_16x16x32_bf16 v[74:77], a[32:35], a[24:27], v[74:77]\n\t"
"v_fma_f32 v99, v95, 0.5, 1.0\n\t"
"v_fma_f32 v100, v96, 0.5, 1.0\n\t"
"v_mfma_f32_16x16x32_bf16 v[70:73], a[32:35], a[20:23], v[70:73]\n\t"
"v_fma_f32 v101, v97, 0.5, 1.0\n\t"
"v_fma_f32 v98, v98, v94, 1.0\n\t"
"v_mfma_f32_16x16x32_bf16 v[66:69], a[32:35], a[16:19], v[66:69]\n\t"
"s_waitcnt lgkmcnt(9)\n\t"
"s_add_u32 m0, s87, 81920\n\t"
"s_nop 0\n\t"
"buffer_load_dwordx4 v20, s[40:43], s70 offen offset:0 lds\n\t"
"v_fma_f32 v99, v99, v95, 1.0\n\t"
"v_mfma_f32_16x16x32_bf16 v[62:65], a[32:35], a[12:15], v[62:65]\n\t"
"v_fma_f32 v100, v100, v96, 1.0\n\t"
"v_fma_f32 v101, v101, v97, 1.0\n\t"
"v_mfma_f32_16x16x32_bf16 v[58:61], a[32:35], a[8:11], v[58:61]\n\t"
"s_waitcnt lgkmcnt(6)\n\t"
"v_rcp_f32 v98, v98\n\t"
"v_mfma_f32_16x16x32_bf16 v[54:57], a[32:35], a[4:7], v[54:57]\n\t"
"s_waitcnt lgkmcnt(3)\n\t"
"v_rcp_f32 v99, v99\n\t"
"v_mfma_f32_16x16x32_bf16 v[50:53], a[32:35], a[0:3], v[50:53]\n\t"
"v_rcp_f32 v100, v100\n\t"
"v_mfma_f32_16x16x32_bf16 v[78:81], a[36:39], a[24:27], v[78:81]\n\t"
"ds_read2_b32 a[32:33], v40 offset0:144 offset1:145\n\t"
"ds_read2_b32 a[34:35], v40 offset0:146 offset1:147\n\t"
"ds_read_b128 a[24:27], v42 offset:3072\n\t"
"v_mfma_f32_16x16x32_bf16 v[74:77], a[36:39], a[20:23], v[74:77]\n\t"
"v_rcp_f32 v101, v101\n\t"
"v_fma_f32 v94, -v164, v98, v164\n\t"
"v_mfma_f32_16x16x32_bf16 v[70:73], a[36:39], a[16:19], v[70:73]\n\t"
"v_fma_f32 v95, -v165, v99, v165\n\t"
"v_fma_f32 v96, -v166, v100, v166\n\t"
"v_mfma_f32_16x16x32_bf16 v[66:69], a[36:39], a[12:15], v[66:69]\n\t"
"v_fma_f32 v97, -v167, v101, v167\n\t"
"v_cvt_pk_bf16_f32 v150, v94, v95\n\t"
"v_cvt_pk_bf16_f32 v151, v96, v97\n\t"
"v_mfma_f32_16x16x32_bf16 v[62:65], a[36:39], a[8:11], v[62:65]\n\t"
"buffer_store_dwordx4 v[148:151], v29, s[44:47], s82 offen offset:1024 sc1\n\t"
"v_mul_f32 v86, s88, v152\n\t"
"v_mul_f32 v87, s88, v153\n\t"
"v_mfma_f32_16x16x32_bf16 v[58:61], a[36:39], a[4:7], v[58:61]\n\t"
"v_mul_f32 v88, s88, v154\n\t"
"v_mfma_f32_16x16x32_bf16 v[54:57], a[36:39], a[0:3], v[54:57]\n\t"
"s_add_u32 m0, s87, 82944\n\t"
"s_nop 0\n\t"
"buffer_load_dwordx4 v21, s[40:43], s70 offen offset:0 lds\n\t"
"v_mul_f32 v89, s88, v155\n\t"
"v_mfma_f32_16x16x32_bf16 v[78:81], a[40:43], a[20:23], v[78:81]\n\t"
"ds_read2_b32 a[36:37], v40 offset0:16 offset1:17\n\t"
"ds_read2_b32 a[38:39], v40 offset0:18 offset1:19\n\t"
"ds_read_b128 a[20:23], v42 offset:2560\n\t"
"v_mfma_f32_16x16x32_bf16 v[74:77], a[40:43], a[16:19], v[74:77]\n\t"
"v_exp_f32 v86, v86\n\t"
"v_mfma_f32_16x16x32_bf16 v[70:73], a[40:43], a[12:15], v[70:73]\n\t"
"v_exp_f32 v87, v87\n\t"
"v_mfma_f32_16x16x32_bf16 v[66:69], a[40:43], a[8:11], v[66:69]\n\t"
"v_exp_f32 v88, v88\n\t"
"v_mfma_f32_16x16x32_bf16 v[62:65], a[40:43], a[4:7], v[62:65]\n\t"
"v_exp_f32 v89, v89\n\t"
"v_fma_f32 v90, v86, 0.5, 1.0\n\t"
"v_mfma_f32_16x16x32_bf16 v[58:61], a[40:43], a[0:3], v[58:61]\n\t"
"s_waitcnt lgkmcnt(7)\n\t"
"v_fma_f32 v91, v87, 0.5, 1.0\n\t"
"v_fma_f32 v92, v88, 0.5, 1.0\n\t"
"v_fma_f32 v93, v89, 0.5, 1.0\n\t"
"v_mfma_f32_16x16x32_bf16 v[78:81], a[44:47], a[16:19], v[78:81]\n\t"
"ds_read2_b32 a[40:41], v41 offset0:144 offset1:145\n\t"
"ds_read2_b32 a[42:43], v41 offset0:146 offset1:147\n\t"
"ds_read_b128 a[16:19], v42 offset:2048\n\t"
"v_mfma_f32_16x16x32_bf16 v[74:77], a[44:47], a[12:15], v[74:77]\n\t"
"v_fma_f32 v90, v90, v86, 1.0\n\t"
"v_fma_f32 v91, v91, v87, 1.0\n\t"
"v_mfma_f32_16x16x32_bf16 v[70:73], a[44:47], a[8:11], v[70:73]\n\t"
"v_fma_f32 v92, v92, v88, 1.0\n\t"
"v_fma_f32 v93, v93, v89, 1.0\n\t"
"v_mfma_f32_16x16x32_bf16 v[66:69], a[44:47], a[4:7], v[66:69]\n\t"
"v_rcp_f32 v90, v90\n\t"
"v_mfma_f32_16x16x32_bf16 v[62:65], a[44:47], a[0:3], v[62:65]\n\t"
"s_waitcnt lgkmcnt(7)\n\t"
"v_rcp_f32 v91, v91\n\t"
"v_mfma_f32_16x16x32_bf16 v[78:81], a[32:35], a[12:15], v[78:81]\n\t"
"ds_read2_b32 a[44:45], v41 offset0:16 offset1:17\n\t"
"ds_read2_b32 a[46:47], v41 offset0:18 offset1:19\n\t"
"ds_read_b128 a[12:15], v42 offset:1536\n\t"
"v_mfma_f32_16x16x32_bf16 v[74:77], a[32:35], a[8:11], v[74:77]\n\t"
"v_rcp_f32 v92, v92\n\t"
"v_mfma_f32_16x16x32_bf16 v[70:73], a[32:35], a[4:7], v[70:73]\n\t"
"v_rcp_f32 v93, v93\n\t"
"v_mfma_f32_16x16x32_bf16 v[66:69], a[32:35], a[0:3], v[66:69]\n\t"
"s_waitcnt lgkmcnt(7)\n\t"
"s_add_u32 m0, s87, 83840\n\t"
"s_nop 0\n\t"
"buffer_load_dwordx4 v20, s[40:43], s70 offen offset:128 lds\n\t"
"v_fma_f32 v86, -v152, v90, v152\n\t"
"v_mfma_f32_16x16x32_bf16 v[78:81], a[36:39], a[8:11], v[78:81]\n\t"
"ds_read2_b32 a[32:33], v38 offset0:128 offset1:129\n\t"
"ds_read2_b32 a[34:35], v38 offset0:130 offset1:131\n\t"
"ds_read_b128 a[8:11], v42 offset:1024\n\t"
"v_mfma_f32_16x16x32_bf16 v[74:77], a[36:39], a[4:7], v[74:77]\n\t"
"v_fma_f32 v87, -v153, v91, v153\n\t"
"v_fma_f32 v88, -v154, v92, v154\n\t"
"v_mfma_f32_16x16x32_bf16 v[70:73], a[36:39], a[0:3], v[70:73]\n\t"
"s_waitcnt lgkmcnt(7)\n\t"
"v_fma_f32 v89, -v155, v93, v155\n\t"
"v_cvt_pk_bf16_f32 v152, v86, v87\n\t"
"v_cvt_pk_bf16_f32 v153, v88, v89\n\t"
"v_mfma_f32_16x16x32_bf16 v[78:81], a[40:43], a[4:7], v[78:81]\n\t"
"ds_read2_b32 a[36:37], v38 offset0:0 offset1:1\n\t"
"ds_read2_b32 a[38:39], v38 offset0:2 offset1:3\n\t"
"ds_read_b128 a[4:7], v42 offset:512\n\t"
"v_mfma_f32_16x16x32_bf16 v[74:77], a[40:43], a[0:3], v[74:77]\n\t"
"s_waitcnt lgkmcnt(7)\n\t"
"v_mul_f32 v94, s88, v168\n\t"
"v_mul_f32 v95, s88, v169\n\t"
"v_mfma_f32_16x16x32_bf16 v[78:81], a[44:47], a[0:3], v[78:81]\n\t"
"s_waitcnt lgkmcnt(4)\n\t"
"ds_read2_b32 a[40:41], v39 offset0:128 offset1:129\n\t"
"ds_read2_b32 a[42:43], v39 offset0:130 offset1:131\n\t"
"ds_read_b128 a[0:3], v42 offset:0\n\t"
"v_mfma_f32_16x16x32_bf16 v[78:81], a[32:35], a[28:31], v[78:81]\n\t"
"ds_read2_b32 a[44:45], v39 offset0:0 offset1:1\n\t"
"ds_read2_b32 a[46:47], v39 offset0:2 offset1:3\n\t"
"v_mul_f32 v96, s88, v170\n\t"
"v_mfma_f32_16x16x32_bf16 v[74:77], a[32:35], a[24:27], v[74:77]\n\t"
"v_mul_f32 v97, s88, v171\n\t"
"v_mfma_f32_16x16x32_bf16 v[70:73], a[32:35], a[20:23], v[70:73]\n\t"
"v_exp_f32 v94, v94\n\t"
"v_mfma_f32_16x16x32_bf16 v[66:69], a[32:35], a[16:19], v[66:69]\n\t"
"s_waitcnt lgkmcnt(8)\n\t"
"v_exp_f32 v95, v95\n\t"
"v_mfma_f32_16x16x32_bf16 v[62:65], a[32:35], a[12:15], v[62:65]\n\t"
"v_exp_f32 v96, v96\n\t"
"v_mfma_f32_16x16x32_bf16 v[58:61], a[32:35], a[8:11], v[58:61]\n\t"
"s_waitcnt lgkmcnt(5)\n\t"
"v_exp_f32 v97, v97\n\t"
"v_fma_f32 v98, v94, 0.5, 1.0\n\t"
"v_mfma_f32_16x16x32_bf16 v[54:57], a[32:35], a[4:7], v[54:57]\n\t"
"s_waitcnt lgkmcnt(2)\n\t"
"v_fma_f32 v99, v95, 0.5, 1.0\n\t"
"v_fma_f32 v100, v96, 0.5, 1.0\n\t"
"v_mfma_f32_16x16x32_bf16 v[50:53], a[32:35], a[0:3], v[50:53]\n\t"
"s_add_u32 m0, s87, 84864\n\t"
"s_nop 0\n\t"
"buffer_load_dwordx4 v21, s[40:43], s70 offen offset:128 lds\n\t"
"v_fma_f32 v101, v97, 0.5, 1.0\n\t"
"v_mfma_f32_16x16x32_bf16 v[78:81], a[36:39], a[24:27], v[78:81]\n\t"
"ds_read2_b32 a[32:33], v40 offset0:128 offset1:129\n\t"
"ds_read2_b32 a[34:35], v40 offset0:130 offset1:131\n\t"
"v_fma_f32 v98, v98, v94, 1.0\n\t"
"v_mfma_f32_16x16x32_bf16 v[74:77], a[36:39], a[20:23], v[74:77]\n\t"
"v_fma_f32 v99, v99, v95, 1.0\n\t"
"v_fma_f32 v100, v100, v96, 1.0\n\t"
"v_mfma_f32_16x16x32_bf16 v[70:73], a[36:39], a[16:19], v[70:73]\n\t"
"v_fma_f32 v101, v101, v97, 1.0\n\t"
"v_rcp_f32 v98, v98\n\t"
"v_mfma_f32_16x16x32_bf16 v[66:69], a[36:39], a[12:15], v[66:69]\n\t"
"v_rcp_f32 v99, v99\n\t"
"v_mfma_f32_16x16x32_bf16 v[62:65], a[36:39], a[8:11], v[62:65]\n\t"
"v_rcp_f32 v100, v100\n\t"
"v_mfma_f32_16x16x32_bf16 v[58:61], a[36:39], a[4:7], v[58:61]\n\t"
"v_rcp_f32 v101, v101\n\t"
"v_fma_f32 v94, -v168, v98, v168\n\t"
"v_mfma_f32_16x16x32_bf16 v[54:57], a[36:39], a[0:3], v[54:57]\n\t"
"v_fma_f32 v95, -v169, v99, v169\n\t"
"v_fma_f32 v96, -v170, v100, v170\n\t"
"v_fma_f32 v97, -v171, v101, v171\n\t"
"v_mfma_f32_16x16x32_bf16 v[78:81], a[40:43], a[20:23], v[78:81]\n\t"
"ds_read2_b32 a[36:37], v40 offset0:0 offset1:1\n\t"
"ds_read2_b32 a[38:39], v40 offset0:2 offset1:3\n\t"
"v_mfma_f32_16x16x32_bf16 v[74:77], a[40:43], a[16:19], v[74:77]\n\t"
"v_cvt_pk_bf16_f32 v154, v94, v95\n\t"
"v_cvt_pk_bf16_f32 v155, v96, v97\n\t"
"v_mfma_f32_16x16x32_bf16 v[70:73], a[40:43], a[12:15], v[70:73]\n\t"
"buffer_store_dwordx4 v[152:155], v29, s[44:47], s82 offen offset:2048 sc1\n\t"
"v_mul_f32 v86, s88, v156\n\t"
"v_mfma_f32_16x16x32_bf16 v[66:69], a[40:43], a[8:11], v[66:69]\n\t"
"s_add_u32 m0, s87, 86016\n\t"
"s_nop 0\n\t"
"buffer_load_dwordx4 v22, s[40:43], s70 offen lds\n\t"
"v_mul_f32 v87, s88, v157\n\t"
"v_mfma_f32_16x16x32_bf16 v[62:65], a[40:43], a[4:7], v[62:65]\n\t"
"v_mul_f32 v88, s88, v158\n\t"
"v_mul_f32 v89, s88, v159\n\t"
"v_mfma_f32_16x16x32_bf16 v[58:61], a[40:43], a[0:3], v[58:61]\n\t"
"s_waitcnt lgkmcnt(4)\n\t"
"v_exp_f32 v86, v86\n\t"
"v_mfma_f32_16x16x32_bf16 v[78:81], a[44:47], a[16:19], v[78:81]\n\t"
"ds_read2_b32 a[40:41], v41 offset0:128 offset1:129\n\t"
"ds_read2_b32 a[42:43], v41 offset0:130 offset1:131\n\t"
"v_mfma_f32_16x16x32_bf16 v[74:77], a[44:47], a[12:15], v[74:77]\n\t"
"v_exp_f32 v87, v87\n\t"
"v_mfma_f32_16x16x32_bf16 v[70:73], a[44:47], a[8:11], v[70:73]\n\t"
"v_exp_f32 v88, v88\n\t"
"v_mfma_f32_16x16x32_bf16 v[66:69], a[44:47], a[4:7], v[66:69]\n\t"
"v_exp_f32 v89, v89\n\t"
"v_fma_f32 v90, v86, 0.5, 1.0\n\t"
"v_mfma_f32_16x16x32_bf16 v[62:65], a[44:47], a[0:3], v[62:65]\n\t"
"s_waitcnt lgkmcnt(4)\n\t"
"v_fma_f32 v91, v87, 0.5, 1.0\n\t"
"v_fma_f32 v92, v88, 0.5, 1.0\n\t"
"v_fma_f32 v93, v89, 0.5, 1.0\n\t"
"v_mfma_f32_16x16x32_bf16 v[78:81], a[32:35], a[12:15], v[78:81]\n\t"
"ds_read2_b32 a[44:45], v41 offset0:0 offset1:1\n\t"
"ds_read2_b32 a[46:47], v41 offset0:2 offset1:3\n\t"
"v_mfma_f32_16x16x32_bf16 v[74:77], a[32:35], a[8:11], v[74:77]\n\t"
"v_fma_f32 v90, v90, v86, 1.0\n\t"
"v_fma_f32 v91, v91, v87, 1.0\n\t"
"v_fma_f32 v92, v92, v88, 1.0\n\t"
"v_mfma_f32_16x16x32_bf16 v[70:73], a[32:35], a[4:7], v[70:73]\n\t"
"v_fma_f32 v93, v93, v89, 1.0\n\t"
"v_rcp_f32 v90, v90\n\t"
"v_mfma_f32_16x16x32_bf16 v[66:69], a[32:35], a[0:3], v[66:69]\n\t"
"s_waitcnt lgkmcnt(4)\n\t"
"v_rcp_f32 v91, v91\n\t"
"v_mfma_f32_16x16x32_bf16 v[78:81], a[36:39], a[8:11], v[78:81]\n\t"
"v_rcp_f32 v92, v92\n\t"
"v_mfma_f32_16x16x32_bf16 v[74:77], a[36:39], a[4:7], v[74:77]\n\t"
"buffer_load_dword v110, v46, s[44:47], 0 offen sc1\n\t"
"v_rcp_f32 v93, v93\n\t"
"v_mfma_f32_16x16x32_bf16 v[70:73], a[36:39], a[0:3], v[70:73]\n\t"
"s_waitcnt lgkmcnt(2)\n\t"
"v_fma_f32 v86, -v156, v90, v156\n\t"
"v_fma_f32 v87, -v157, v91, v157\n\t"
"v_fma_f32 v88, -v158, v92, v158\n\t"
"v_mfma_f32_16x16x32_bf16 v[78:81], a[40:43], a[4:7], v[78:81]\n\t"
"v_fma_f32 v89, -v159, v93, v159\n\t"
"v_cvt_pk_bf16_f32 v156, v86, v87\n\t"
"v_mfma_f32_16x16x32_bf16 v[74:77], a[40:43], a[0:3], v[74:77]\n\t"
"s_waitcnt lgkmcnt(0)\n\t"
"v_cvt_pk_bf16_f32 v157, v88, v89\n\t"
"v_mul_f32 v94, s88, v172\n\t"
"v_mul_f32 v95, s88, v173\n\t"
"v_mfma_f32_16x16x32_bf16 v[78:81], a[44:47], a[0:3], v[78:81]\n\t"
"v_mul_f32 v96, s88, v174\n\t"
"v_mul_f32 v97, s88, v175\n\t"
"v_exp_f32 v94, v94\n\t"
"v_exp_f32 v95, v95\n\t"
"v_exp_f32 v96, v96\n\t"
"v_exp_f32 v97, v97\n\t"
"v_fma_f32 v98, v94, 0.5, 1.0\n\t"
"v_fma_f32 v99, v95, 0.5, 1.0\n\t"
"v_fma_f32 v100, v96, 0.5, 1.0\n\t"
"v_fma_f32 v101, v97, 0.5, 1.0\n\t"
"v_fma_f32 v98, v98, v94, 1.0\n\t"
"v_fma_f32 v99, v99, v95, 1.0\n\t"
"v_fma_f32 v100, v100, v96, 1.0\n\t"
"v_fma_f32 v101, v101, v97, 1.0\n\t"
"v_rcp_f32 v98, v98\n\t"
"v_rcp_f32 v99, v99\n\t"
"v_rcp_f32 v100, v100\n\t"
"v_rcp_f32 v101, v101\n\t"
"v_fma_f32 v94, -v172, v98, v172\n\t"
"v_fma_f32 v95, -v173, v99, v173\n\t"
"v_fma_f32 v96, -v174, v100, v174\n\t"
"v_fma_f32 v97, -v175, v101, v175\n\t"
"v_cvt_pk_bf16_f32 v158, v94, v95\n\t"
"v_cvt_pk_bf16_f32 v159, v96, v97\n\t"
"buffer_store_dwordx4 v[156:159], v29, s[44:47], s82 offen offset:3072 sc1\n\t"
"s_barrier\n\t"
"ds_read_b64_tr_b16 a[64:65], v23 offset:0\n\t"
"ds_read_b64_tr_b16 a[66:67], v23 offset:5120\n\t"
"ds_read_b64_tr_b16 a[68:69], v24 offset:0\n\t"
"ds_read_b64_tr_b16 a[70:71], v24 offset:5120\n\t"
"ds_read_b64_tr_b16 a[72:73], v25 offset:0\n\t"
"ds_read_b64_tr_b16 a[74:75], v25 offset:5120\n\t"
"s_waitcnt lgkmcnt(4)\n\t"
"v_mfma_f32_16x16x32_bf16 a[112:115], a[64:67], v[112:115], 0\n\t"
"ds_read_b64_tr_b16 a[76:77], v26 offset:0\n\t"
"ds_read_b64_tr_b16 a[78:79], v26 offset:5120\n\t"
"v_cvt_pk_bf16_f32 v50, v50, v51\n\t"
"v_cvt_pk_bf16_f32 v51, v52, v53\n\t"
"v_cvt_pk_bf16_f32 v54, v54, v55\n\t"
"v_mfma_f32_16x16x32_bf16 a[116:119], a[64:67], v[116:119], 0\n\t"
"v_cvt_pk_bf16_f32 v55, v56, v57\n\t"
"v_cvt_pk_bf16_f32 v58, v58, v59\n\t"
"v_cvt_pk_bf16_f32 v59, v60, v61\n\t"
"v_cvt_pk_bf16_f32 v62, v62, v63\n\t"
"v_cvt_pk_bf16_f32 v63, v64, v65\n\t"
"v_mfma_f32_16x16x32_bf16 a[120:123], a[64:67], v[120:123], 0\n\t"
"v_cvt_pk_bf16_f32 v66, v66, v67\n\t"
"v_cvt_pk_bf16_f32 v67, v68, v69\n\t"
"v_cvt_pk_bf16_f32 v70, v70, v71\n\t"
"v_cvt_pk_bf16_f32 v71, v72, v73\n\t"
"v_cvt_pk_bf16_f32 v74, v74, v75\n\t"
"v_mfma_f32_16x16x32_bf16 a[124:127], a[64:67], v[124:127], 0\n\t"
"s_waitcnt lgkmcnt(4)\n\t"
"v_cvt_pk_bf16_f32 v75, v76, v77\n\t"
"v_cvt_pk_bf16_f32 v78, v78, v79\n\t"
"v_cvt_pk_bf16_f32 v79, v80, v81\n\t"
"v_mul_f32 v86, s88, v176\n\t"
"v_mul_f32 v87, s88, v177\n\t"
"v_mfma_f32_16x16x32_bf16 a[128:131], a[68:71], v[112:115], 0\n\t"
"ds_read_b64_tr_b16 a[64:65], v23 offset:2048\n\t"
"ds_read_b64_tr_b16 a[66:67], v23 offset:7168\n\t"
"v_mul_f32 v88, s88, v178\n\t"
"v_mul_f32 v89, s88, v179\n\t"
"v_mfma_f32_16x16x32_bf16 a[132:135], a[68:71], v[116:119], 0\n\t"
"v_exp_f32 v86, v86\n\t"
"v_exp_f32 v87, v87\n\t"
"v_exp_f32 v88, v88\n\t"
"v_mfma_f32_16x16x32_bf16 a[136:139], a[68:71], v[120:123], 0\n\t"
"v_exp_f32 v89, v89\n\t"
"v_fma_f32 v90, v86, 0.5, 1.0\n\t"
"v_fma_f32 v91, v87, 0.5, 1.0\n\t"
"v_fma_f32 v92, v88, 0.5, 1.0\n\t"
"v_mfma_f32_16x16x32_bf16 a[140:143], a[68:71], v[124:127], 0\n\t"
"s_waitcnt lgkmcnt(4)\n\t"
"v_fma_f32 v93, v89, 0.5, 1.0\n\t"
"v_fma_f32 v90, v90, v86, 1.0\n\t"
"v_fma_f32 v91, v91, v87, 1.0\n\t"
"v_fma_f32 v92, v92, v88, 1.0\n\t"
"v_fma_f32 v93, v93, v89, 1.0\n\t"
"v_mfma_f32_16x16x32_bf16 a[144:147], a[72:75], v[112:115], 0\n\t"
"ds_read_b64_tr_b16 a[68:69], v24 offset:2048\n\t"
"ds_read_b64_tr_b16 a[70:71], v24 offset:7168\n\t"
"v_rcp_f32 v90, v90\n\t"
"v_mfma_f32_16x16x32_bf16 a[148:151], a[72:75], v[116:119], 0\n\t"
"v_rcp_f32 v91, v91\n\t"
"v_rcp_f32 v92, v92\n\t"
"v_rcp_f32 v93, v93\n\t"
"v_mfma_f32_16x16x32_bf16 a[152:155], a[72:75], v[120:123], 0\n\t"
"v_fma_f32 v86, -v176, v90, v176\n\t"
"v_fma_f32 v87, -v177, v91, v177\n\t"
"v_fma_f32 v88, -v178, v92, v178\n\t"
"v_fma_f32 v89, -v179, v93, v179\n\t"
"v_cvt_pk_bf16_f32 v176, v86, v87\n\t"
"v_mfma_f32_16x16x32_bf16 a[156:159], a[72:75], v[124:127], 0\n\t"
"s_waitcnt lgkmcnt(4)\n\t"
"v_cvt_pk_bf16_f32 v177, v88, v89\n\t"
"v_mul_f32 v94, s88, v192\n\t"
"v_mul_f32 v95, s88, v193\n\t"
"v_mul_f32 v96, s88, v194\n\t"
"v_mul_f32 v97, s88, v195\n\t"
"v_mfma_f32_16x16x32_bf16 a[160:163], a[76:79], v[112:115], 0\n\t"
"ds_read_b64_tr_b16 a[72:73], v25 offset:2048\n\t"
"ds_read_b64_tr_b16 a[74:75], v25 offset:7168\n\t"
"v_exp_f32 v94, v94\n\t"
"v_mfma_f32_16x16x32_bf16 a[164:167], a[76:79], v[116:119], 0\n\t"
"v_exp_f32 v95, v95\n\t"
"v_exp_f32 v96, v96\n\t"
"v_exp_f32 v97, v97\n\t"
"v_mfma_f32_16x16x32_bf16 a[168:171], a[76:79], v[120:123], 0\n\t"
"v_fma_f32 v98, v94, 0.5, 1.0\n\t"
"v_fma_f32 v99, v95, 0.5, 1.0\n\t"
"v_fma_f32 v100, v96, 0.5, 1.0\n\t"
"v_fma_f32 v101, v97, 0.5, 1.0\n\t"
"v_fma_f32 v98, v98, v94, 1.0\n\t"
"v_mfma_f32_16x16x32_bf16 a[172:175], a[76:79], v[124:127], 0\n\t"
"s_waitcnt lgkmcnt(4)\n\t"
"v_fma_f32 v99, v99, v95, 1.0\n\t"
"v_fma_f32 v100, v100, v96, 1.0\n\t"
"v_fma_f32 v101, v101, v97, 1.0\n\t"
"v_rcp_f32 v98, v98\n\t"
"v_mfma_f32_16x16x32_bf16 a[176:179], a[64:67], v[112:115], 0\n\t"
"ds_read_b64_tr_b16 a[76:77], v26 offset:2048\n\t"
"ds_read_b64_tr_b16 a[78:79], v26 offset:7168\n\t"
"v_rcp_f32 v99, v99\n\t"
"v_mfma_f32_16x16x32_bf16 a[180:183], a[64:67], v[116:119], 0\n\t"
"v_rcp_f32 v100, v100\n\t"
"v_rcp_f32 v101, v101\n\t"
"v_fma_f32 v94, -v192, v98, v192\n\t"
"v_fma_f32 v95, -v193, v99, v193\n\t"
"v_mfma_f32_16x16x32_bf16 a[184:187], a[64:67], v[120:123], 0\n\t"
"v_fma_f32 v96, -v194, v100, v194\n\t"
"v_fma_f32 v97, -v195, v101, v195\n\t"
"v_cvt_pk_bf16_f32 v178, v94, v95\n\t"
"v_cvt_pk_bf16_f32 v179, v96, v97\n\t"
"v_mfma_f32_16x16x32_bf16 a[188:191], a[64:67], v[124:127], 0\n\t"
"s_waitcnt lgkmcnt(4)\n\t"
"buffer_store_dwordx4 v[176:179], v28, s[44:47], s83 offen offset:0 sc1\n\t"
"v_mul_f32 v86, s88, v180\n\t"
"v_mul_f32 v87, s88, v181\n\t"
"v_mul_f32 v88, s88, v182\n\t"
"v_mfma_f32_16x16x32_bf16 a[192:195], a[68:71], v[112:115], 0\n\t"
"ds_read_b64_tr_b16 a[64:65], v27 offset:0\n\t"
"ds_read_b64_tr_b16 a[66:67], v27 offset:5120\n\t"
"s_waitcnt vmcnt(1)\n\t"
"v_cndmask_b32 v111, 1, 2, s[10:11]\n\t"
"v_add_u32 v111, v111, v37\n\t"
"buffer_store_dword v111, v47, s[44:47], 0 offen sc1\n\t"
"v_mfma_f32_16x16x32_bf16 a[196:199], a[68:71], v[116:119], 0\n\t"
"s_mov_b32 s92, 0x700\n\t"
"s_mov_b32 s93, 0\n\t"
"v_sub_u32 v110, v110, v48\n\t"
"v_subrev_u32 v110, 1, v110\n\t"
"v_cmp_lt_u32 vcc, 3, v110\n\t"
"s_nop 2\n\t"
"s_and_b64 vcc, vcc, s[92:93]\n\t"
"s_cbranch_scc0 LP2_ok%=\n\t"
"s_mov_b32 s94, 0\n\t"
"LP2_spin%=:\n\t"
"buffer_load_dword v37, v46, s[44:47], s89 offen sc1\n\t"
"s_waitcnt vmcnt(0)\n\t"
"v_cndmask_b32 v111, 1, 2, s[10:11]\n\t"
"v_add_u32 v111, v111, v37\n\t"
"buffer_store_dword v111, v47, s[44:47], 0 offen sc1\n\t"
"buffer_load_dword v110, v46, s[44:47], 0 offen sc1\n\t"
"s_waitcnt vmcnt(0)\n\t"
"s_add_u32 s94, s94, 1\n\t"
"s_cmp_gt_u32 s94, 0x4000\n\t"
"s_cbranch_scc1 LP2_out%=\n\t"
"v_sub_u32 v110, v110, v48\n\t"
"v_subrev_u32 v110, 1, v110\n\t"
"v_cmp_lt_u32 vcc, 3, v110\n\t"
"s_nop 2\n\t"
"s_and_b64 vcc, vcc, s[92:93]\n\t"
"s_cbranch_scc1 LP2_spin%=\n\t"
"LP2_out%=:\n\t"
"LP2_ok%=:\n\t"
"buffer_load_dword v37, v46, s[44:47], s89 offen sc1\n\t"
"v_mfma_f32_16x16x32_bf16 a[200:203], a[68:71], v[120:123], 0\n\t"
"s_add_u32 s95, s75, 4096\n\t"
"v_mul_f32 v89, s88, v183\n\t"
"v_exp_f32 v86, v86\n\t"
"v_mfma_f32_16x16x32_bf16 a[204:207], a[68:71], v[124:127], 0\n\t"
"s_waitcnt lgkmcnt(4)\n\t"
"v_exp_f32 v87, v87\n\t"
"v_exp_f32 v88, v88\n\t"
"v_mfma_f32_16x16x32_bf16 a[208:211], a[72:75], v[112:115], 0\n\t"
"ds_read_b64_tr_b16 a[68:69], v23 offset:10240\n\t"
"ds_read_b64_tr_b16 a[70:71], v23 offset:15360\n\t"
"v_exp_f32 v89, v89\n\t"
"v_fma_f32 v90, v86, 0.5, 1.0\n\t"
"v_fma_f32 v91, v87, 0.5, 1.0\n\t"
"v_mfma_f32_16x16x32_bf16 a[212:215], a[72:75], v[116:119], 0\n\t"
"v_fma_f32 v92, v88, 0.5, 1.0\n\t"
"v_fma_f32 v93, v89, 0.5, 1.0\n\t"
"v_mfma_f32_16x16x32_bf16 a[216:219], a[72:75], v[120:123], 0\n\t"
"buffer_load_dwordx2 v[128:129], v31, s[44:47], s75 offen offset:0 sc1\n\t"
"v_fma_f32 v90, v90, v86, 1.0\n\t"
"v_fma_f32 v91, v91, v87, 1.0\n\t"
"v_fma_f32 v92, v92, v88, 1.0\n\t"
"v_fma_f32 v93, v93, v89, 1.0\n\t"
"v_mfma_f32_16x16x32_bf16 a[220:223], a[72:75], v[124:127], 0\n\t"
"s_waitcnt lgkmcnt(4)\n\t"
"v_rcp_f32 v90, v90\n\t"
"v_rcp_f32 v91, v91\n\t"
"v_rcp_f32 v92, v92\n\t"
"v_mfma_f32_16x16x32_bf16 a[224:227], a[76:79], v[112:115], 0\n\t"
"ds_read_b64_tr_b16 a[72:73], v24 offset:10240\n\t"
"ds_read_b64_tr_b16 a[74:75], v24 offset:15360\n\t"
"v_rcp_f32 v93, v93\n\t"
"v_fma_f32 v86, -v180, v90, v180\n\t"
"v_mfma_f32_16x16x32_bf16 a[228:231], a[76:79], v[116:119], 0\n\t"
"buffer_load_dwordx2 v[130:131], v31, s[44:47], s75 offen offset:512 sc1\n\t"
"v_fma_f32 v87, -v181, v91, v181\n\t"
"v_fma_f32 v88, -v182, v92, v182\n\t"
"v_mfma_f32_16x16x32_bf16 a[232:235], a[76:79], v[120:123], 0\n\t"
"buffer_store_dwordx2 v[50:51], v45, s[44:47], 0 offen offset:0\n\t"
"v_fma_f32 v89, -v183, v93, v183\n\t"
"v_cvt_pk_bf16_f32 v180, v86, v87\n\t"
"v_cvt_pk_bf16_f32 v181, v88, v89\n\t"
"v_mul_f32 v94, s88, v196\n\t"
"v_mul_f32 v95, s88, v197\n\t"
"v_mfma_f32_16x16x32_bf16 a[236:239], a[76:79], v[124:127], 0\n\t"
"s_waitcnt lgkmcnt(4)\n\t"
"v_mul_f32 v96, s88, v198\n\t"
"v_mfma_f32_16x16x32_bf16 a[240:243], a[64:67], v[112:115], 0\n\t"
"ds_read_b64_tr_b16 a[76:77], v25 offset:10240\n\t"
"ds_read_b64_tr_b16 a[78:79], v25 offset:15360\n\t"
"buffer_load_dwordx2 v[132:133], v31, s[44:47], s75 offen offset:1024 sc1\n\t"
"v_mul_f32 v97, s88, v199\n\t"
"v_exp_f32 v94, v94\n\t"
"v_mfma_f32_16x16x32_bf16 a[244:247], a[64:67], v[116:119], 0\n\t"
"v_exp_f32 v95, v95\n\t"
"v_exp_f32 v96, v96\n\t"
"v_mfma_f32_16x16x32_bf16 a[248:251], a[64:67], v[120:123], 0\n\t"
"v_exp_f32 v97, v97\n\t"
"v_fma_f32 v98, v94, 0.5, 1.0\n\t"
"v_fma_f32 v99, v95, 0.5, 1.0\n\t"
"v_fma_f32 v100, v96, 0.5, 1.0\n\t"
"v_fma_f32 v101, v97, 0.5, 1.0\n\t"
"v_mfma_f32_16x16x32_bf16 a[252:255], a[64:67], v[124:127], 0\n\t"
"s_waitcnt lgkmcnt(4)\n\t"
"buffer_load_dwordx2 v[134:135], v31, s[44:47], s75 offen offset:1536 sc1\n\t"
"v_fma_f32 v98, v98, v94, 1.0\n\t"
"v_fma_f32 v99, v99, v95, 1.0\n\t"
"v_fma_f32 v100, v100, v96, 1.0\n\t"
"v_mfma_f32_16x16x32_bf16 a[112:115], a[68:71], v[144:147], a[112:115]\n\t"
"ds_read_b64_tr_b16 a[64:65], v26 offset:10240\n\t"
"ds_read_b64_tr_b16 a[66:67], v26 offset:15360\n\t"
"buffer_store_dwordx2 v[54:55], v45, s[44:47], 0 offen offset:512\n\t"
"v_fma_f32 v101, v101, v97, 1.0\n\t"
"v_rcp_f32 v98, v98\n\t"
"v_mfma_f32_16x16x32_bf16 a[116:119], a[68:71], v[148:151], a[116:119]\n\t"
"v_rcp_f32 v99, v99\n\t"
"v_rcp_f32 v100, v100\n\t"
"v_mfma_f32_16x16x32_bf16 a[120:123], a[68:71], v[152:155], a[120:123]\n\t"
"v_rcp_f32 v101, v101\n\t"
"v_fma_f32 v94, -v196, v98, v196\n\t"
"v_mfma_f32_16x16x32_bf16 a[124:127], a[68:71], v[156:159], a[124:127]\n\t"
"s_waitcnt lgkmcnt(4)\n\t"
"buffer_load_dwordx2 v[136:137], v31, s[44:47], s75 offen offset:2048 sc1\n\t"
"v_fma_f32 v95, -v197, v99, v197\n\t"
"v_fma_f32 v96, -v198, v100, v198\n\t"
"v_fma_f32 v97, -v199, v101, v199\n\t"
"v_cvt_pk_bf16_f32 v182, v94, v95\n\t"
"v_cvt_pk_bf16_f32 v183, v96, v97\n\t"
"v_mfma_f32_16x16x32_bf16 a[128:131], a[72:75], v[144:147], a[128:131]\n\t"
"ds_read_b64_tr_b16 a[68:69], v23 offset:12288\n\t"
"ds_read_b64_tr_b16 a[70:71], v23 offset:17408\n\t"
"buffer_store_dwordx4 v[180:183], v28, s[44:47], s83 offen offset:1024 sc1\n\t"
"v_mul_f32 v86, s88, v184\n\t"
"v_mul_f32 v87, s88, v185\n\t"
"v_mfma_f32_16x16x32_bf16 a[132:135], a[72:75], v[148:151], a[132:135]\n\t"
"v_mul_f32 v88, s88, v186\n\t"
"v_mfma_f32_16x16x32_bf16 a[136:139], a[72:75], v[152:155], a[136:139]\n\t"
"buffer_load_dwordx2 v[138:139], v31, s[44:47], s75 offen offset:2560 sc1\n\t"
"v_mul_f32 v89, s88, v187\n\t"
"v_exp_f32 v86, v86\n\t"
"v_exp_f32 v87, v87\n\t"
"v_mfma_f32_16x16x32_bf16 a[140:143], a[72:75], v[156:159], a[140:143]\n\t"
"s_waitcnt lgkmcnt(4)\n\t"
"buffer_store_dwordx2 v[58:59], v45, s[44:47], 0 offen offset:1024\n\t"
"v_exp_f32 v88, v88\n\t"
"v_exp_f32 v89, v89\n\t"
"v_mfma_f32_16x16x32_bf16 a[144:147], a[76:79], v[144:147], a[144:147]\n\t"
"ds_read_b64_tr_b16 a[72:73], v24 offset:12288\n\t"
"ds_read_b64_tr_b16 a[74:75], v24 offset:17408\n\t"
"v_fma_f32 v90, v86, 0.5, 1.0\n\t"
"v_fma_f32 v91, v87, 0.5, 1.0\n\t"
"v_fma_f32 v92, v88, 0.5, 1.0\n\t"
"v_mfma_f32_16x16x32_bf16 a[148:151], a[76:79], v[148:151], a[148:151]\n\t"
"buffer_load_dwordx2 v[140:141], v31, s[44:47], s75 offen offset:3072 sc1\n\t"
"v_fma_f32 v93, v89, 0.5, 1.0\n\t"
"v_fma_f32 v90, v90, v86, 1.0\n\t"
"v_fma_f32 v91, v91, v87, 1.0\n\t"
"v_fma_f32 v92, v92, v88, 1.0\n\t"
"v_mfma_f32_16x16x32_bf16 a[152:155], a[76:79], v[152:155], a[152:155]\n\t"
"v_fma_f32 v93, v93, v89, 1.0\n\t"
"v_rcp_f32 v90, v90\n\t"
"v_rcp_f32 v91, v91\n\t"
"v_mfma_f32_16x16x32_bf16 a[156:159], a[76:79], v[156:159], a[156:159]\n\t"
"s_waitcnt lgkmcnt(4)\n\t"
"v_rcp_f32 v92, v92\n\t"
"v_rcp_f32 v93, v93\n\t"
"v_mfma_f32_16x16x32_bf16 a[160:163], a[64:67], v[144:147], a[160:163]\n\t"
"ds_read_b64_tr_b16 a[76:77], v25 offset:12288\n\t"
"ds_read_b64_tr_b16 a[78:79], v25 offset:17408\n\t"
"buffer_load_dwordx2 v[142:143], v31, s[44:47], s75 offen offset:3584 sc1\n\t"
"v_fma_f32 v86, -v184, v90, v184\n\t"
"v_fma_f32 v87, -v185, v91, v185\n\t"
"v_fma_f32 v88, -v186, v92, v186\n\t"
"v_mfma_f32_16x16x32_bf16 a[164:167], a[64:67], v[148:151], a[164:167]\n\t"
"v_fma_f32 v89, -v187, v93, v187\n\t"
"v_mfma_f32_16x16x32_bf16 a[168:171], a[64:67], v[152:155], a[168:171]\n\t"
"buffer_store_dwordx2 v[62:63], v45, s[44:47], 0 offen offset:1536\n\t"
"v_cvt_pk_bf16_f32 v184, v86, v87\n\t"
"v_cvt_pk_bf16_f32 v185, v88, v89\n\t"
"v_mul_f32 v94, s88, v200\n\t"
"v_mul_f32 v95, s88, v201\n\t"
"v_mul_f32 v96, s88, v202\n\t"
"v_mfma_f32_16x16x32_bf16 a[172:175], a[64:67], v[156:159], a[172:175]\n\t"
"s_waitcnt lgkmcnt(4)\n\t"
"buffer_load_dwordx2 v[160:161], v31, s[44:47], s95 offen offset:0 sc1\n\t"
"v_mul_f32 v97, s88, v203\n\t"
"v_exp_f32 v94, v94\n\t"
"v_mfma_f32_16x16x32_bf16 a[176:179], a[68:71], v[144:147], a[176:179]\n\t"
"ds_read_b64_tr_b16 a[64:65], v26 offset:12288\n\t"
"ds_read_b64_tr_b16 a[66:67], v26 offset:17408\n\t"
"v_exp_f32 v95, v95\n\t"
"v_exp_f32 v96, v96\n\t"
"v_mfma_f32_16x16x32_bf16 a[180:183], a[68:71], v[148:151], a[180:183]\n\t"
"v_exp_f32 v97, v97\n\t"
"v_fma_f32 v98, v94, 0.5, 1.0\n\t"
"v_fma_f32 v99, v95, 0.5, 1.0\n\t"
"v_fma_f32 v100, v96, 0.5, 1.0\n\t"
"v_mfma_f32_16x16x32_bf16 a[184:187], a[68:71], v[152:155], a[184:187]\n\t"
"buffer_load_dwordx2 v[162:163], v31, s[44:47], s95 offen offset:512 sc1\n\t"
"v_fma_f32 v101, v97, 0.5, 1.0\n\t"
"v_fma_f32 v98, v98, v94, 1.0\n\t"
"v_fma_f32 v99, v99, v95, 1.0\n\t"
"v_fma_f32 v100, v100, v96, 1.0\n\t"
"v_mfma_f32_16x16x32_bf16 a[188:191], a[68:71], v[156:159], a[188:191]\n\t"
"s_waitcnt lgkmcnt(4)\n\t"
"v_fma_f32 v101, v101, v97, 1.0\n\t"
"v_mfma_f32_16x16x32_bf16 a[192:195], a[72:75], v[144:147], a[192:195]\n\t"
"ds_read_b64_tr_b16 a[68:69], v27 offset:10240\n\t"
"ds_read_b64_tr_b16 a[70:71], v27 offset:15360\n\t"
"buffer_store_dwordx2 v[66:67], v45, s[44:47], 0 offen offset:2048\n\t"
"v_rcp_f32 v98, v98\n\t"
"v_mfma_f32_16x16x32_bf16 a[196:199], a[72:75], v[148:151], a[196:199]\n\t"
"v_rcp_f32 v99, v99\n\t"
"v_rcp_f32 v100, v100\n\t"
"v_rcp_f32 v101, v101\n\t"
"v_mfma_f32_16x16x32_bf16 a[200:203], a[72:75], v[152:155], a[200:203]\n\t"
"buffer_load_dwordx2 v[164:165], v31, s[44:47], s95 offen offset:1024 sc1\n\t"
"v_fma_f32 v94, -v200, v98, v200\n\t"
"v_fma_f32 v95, -v201, v99, v201\n\t"
"v_fma_f32 v96, -v202, v100, v202\n\t"
"v_fma_f32 v97, -v203, v101, v203\n\t"
"v_mfma_f32_16x16x32_bf16 a[204:207], a[72:75], v[156:159], a[204:207]\n\t"
"s_waitcnt lgkmcnt(4)\n\t"
"v_cvt_pk_bf16_f32 v186, v94, v95\n\t"
"v_cvt_pk_bf16_f32 v187, v96, v97\n\t"
"v_mfma_f32_16x16x32_bf16 a[208:211], a[76:79], v[144:147], a[208:211]\n\t"
"s_waitcnt lgkmcnt(0)\n\t"
"s_barrier\n\t"
"ds_read_b64_tr_b16 a[72:73], v23 offset:20480\n\t"
"ds_read_b64_tr_b16 a[74:75], v23 offset:25600\n\t"
"buffer_store_dwordx4 v[184:187], v28, s[44:47], s83 offen offset:2048 sc1\n\t"
"v_mul_f32 v86, s88, v188\n\t"
"v_mul_f32 v87, s88, v189\n\t"
"v_mfma_f32_16x16x32_bf16 a[212:215], a[76:79], v[148:151], a[212:215]\n\t"
"buffer_load_dwordx2 v[166:167], v31, s[44:47], s95 offen offset:1536 sc1\n\t"
"v_mul_f32 v88, s88, v190\n\t"
"v_mul_f32 v89, s88, v191\n\t"
"v_exp_f32 v86, v86\n\t"
"v_mfma_f32_16x16x32_bf16 a[216:219], a[76:79], v[152:155], a[216:219]\n\t"
"v_exp_f32 v87, v87\n\t"
"v_exp_f32 v88, v88\n\t"
"v_mfma_f32_16x16x32_bf16 a[220:223], a[76:79], v[156:159], a[220:223]\n\t"
"buffer_store_dwordx2 v[70:71], v45, s[44:47], 0 offen offset:2560\n\t"
"v_exp_f32 v89, v89\n\t"
"v_fma_f32 v90, v86, 0.5, 1.0\n\t"
"v_fma_f32 v91, v87, 0.5, 1.0\n\t"
"v_fma_f32 v92, v88, 0.5, 1.0\n\t"
"v_mfma_f32_16x16x32_bf16 a[224:227], a[64:67], v[144:147], a[224:227]\n\t"
"ds_read_b64_tr_b16 a[76:77], v24 offset:20480\n\t"
"ds_read_b64_tr_b16 a[78:79], v24 offset:25600\n\t"
"buffer_load_dwordx2 v[168:169], v31, s[44:47], s95 offen offset:2048 sc1\n\t"
"v_fma_f32 v93, v89, 0.5, 1.0\n\t"
"v_fma_f32 v90, v90, v86, 1.0\n\t"
"v_mfma_f32_16x16x32_bf16 a[228:231], a[64:67], v[148:151], a[228:231]\n\t"
"v_fma_f32 v91, v91, v87, 1.0\n\t"
"v_fma_f32 v92, v92, v88, 1.0\n\t"
"v_fma_f32 v93, v93, v89, 1.0\n\t"
"v_rcp_f32 v90, v90\n\t"
"v_mfma_f32_16x16x32_bf16 a[232:235], a[64:67], v[152:155], a[232:235]\n\t"
"v_rcp_f32 v91, v91\n\t"
"v_rcp_f32 v92, v92\n\t"
"v_mfma_f32_16x16x32_bf16 a[236:239], a[64:67], v[156:159], a[236:239]\n\t"
"buffer_load_dwordx2 v[170:171], v31, s[44:47], s95 offen offset:2560 sc1\n\t"
"v_rcp_f32 v93, v93\n\t"
"v_fma_f32 v86, -v188, v90, v188\n\t"
"v_fma_f32 v87, -v189, v91, v189\n\t"
"v_fma_f32 v88, -v190, v92, v190\n\t"
"v_mfma_f32_16x16x32_bf16 a[240:243], a[68:71], v[144:147], a[240:243]\n\t"
"ds_read_b64_tr_b16 a[64:65], v25 offset:20480\n\t"
"ds_read_b64_tr_b16 a[66:67], v25 offset:25600\n\t"
"v_fma_f32 v89, -v191, v93, v191\n\t"
"v_cvt_pk_bf16_f32 v188, v86, v87\n\t"
"v_mfma_f32_16x16x32_bf16 a[244:247], a[68:71], v[148:151], a[244:247]\n\t"
"buffer_store_dwordx2 v[74:75], v45, s[44:47], 0 offen offset:3072\n\t"
"v_cvt_pk_bf16_f32 v189, v88, v89\n\t"
"v_mul_f32 v94, s88, v204\n\t"
"v_mul_f32 v95, s88, v205\n\t"
"v_mfma_f32_16x16x32_bf16 a[248:251], a[68:71], v[152:155], a[248:251]\n\t"
"buffer_load_dwordx2 v[172:173], v31, s[44:47], s95 offen offset:3072 sc1\n\t"
"v_mul_f32 v96, s88, v206\n\t"
"v_mul_f32 v97, s88, v207\n\t"
"v_exp_f32 v94, v94\n\t"
"v_mfma_f32_16x16x32_bf16 a[252:255], a[68:71], v[156:159], a[252:255]\n\t"
"v_exp_f32 v95, v95\n\t"
"v_exp_f32 v96, v96\n\t"
"v_exp_f32 v97, v97\n\t"
"v_fma_f32 v98, v94, 0.5, 1.0\n\t"
"v_fma_f32 v99, v95, 0.5, 1.0\n\t"
"v_fma_f32 v100, v96, 0.5, 1.0\n\t"
"buffer_load_dwordx2 v[174:175], v31, s[44:47], s95 offen offset:3584 sc1\n\t"
"v_fma_f32 v101, v97, 0.5, 1.0\n\t"
"v_fma_f32 v98, v98, v94, 1.0\n\t"
"v_fma_f32 v99, v99, v95, 1.0\n\t"
"v_fma_f32 v100, v100, v96, 1.0\n\t"
"v_fma_f32 v101, v101, v97, 1.0\n\t"
"v_rcp_f32 v98, v98\n\t"
"buffer_store_dwordx2 v[78:79], v45, s[44:47], 0 offen offset:3584\n\t"
"v_rcp_f32 v99, v99\n\t"
"v_rcp_f32 v100, v100\n\t"
"v_rcp_f32 v101, v101\n\t"
"v_fma_f32 v94, -v204, v98, v204\n\t"
"v_fma_f32 v95, -v205, v99, v205\n\t"
"v_fma_f32 v96, -v206, v100, v206\n\t"
"v_fma_f32 v97, -v207, v101, v207\n\t"
"v_cvt_pk_bf16_f32 v190, v94, v95\n\t"
"v_cvt_pk_bf16_f32 v191, v96, v97\n\t"
"buffer_store_dwordx4 v[188:191], v28, s[44:47], s83 offen offset:3072 sc1\n\t"
"s_waitcnt lgkmcnt(4)\n\t"
"v_mfma_f32_16x16x32_bf16 a[112:115], a[72:75], v[176:179], a[112:115]\n\t"
"ds_read_b64_tr_b16 a[68:69], v26 offset:20480\n\t"
"ds_read_b64_tr_b16 a[70:71], v26 offset:25600\n\t"
"v_add_u32 v108, 4096, v107\n\t"
"v_mfma_f32_16x16x32_bf16 a[116:119], a[72:75], v[180:183], a[116:119]\n\t"
"v_mul_f32 v86, s88, v208\n\t"
"v_mul_f32 v87, s88, v209\n\t"
"v_mul_f32 v88, s88, v210\n\t"
"v_mfma_f32_16x16x32_bf16 a[120:123], a[72:75], v[184:187], a[120:123]\n\t"
"buffer_load_dword v110, v46, s[44:47], 0 offen sc1\n\t"
"v_mul_f32 v89, s88, v211\n\t"
"v_exp_f32 v86, v86\n\t"
"v_mfma_f32_16x16x32_bf16 a[124:127], a[72:75], v[188:191], a[124:127]\n\t"
"s_waitcnt lgkmcnt(4)\n\t"
"v_exp_f32 v87, v87\n\t"
"v_mfma_f32_16x16x32_bf16 a[128:131], a[76:79], v[176:179], a[128:131]\n\t"
"ds_read_b64_tr_b16 a[72:73], v23 offset:22528\n\t"
"ds_read_b64_tr_b16 a[74:75], v23 offset:27648\n\t"
"v_exp_f32 v88, v88\n\t"
"v_mfma_f32_16x16x32_bf16 a[132:135], a[76:79], v[180:183], a[132:135]\n\t"
"v_exp_f32 v89, v89\n\t"
"v_fma_f32 v90, v86, 0.5, 1.0\n\t"
"v_mfma_f32_16x16x32_bf16 a[136:139], a[76:79], v[184:187], a[136:139]\n\t"
"v_fma_f32 v91, v87, 0.5, 1.0\n\t"
"v_fma_f32 v92, v88, 0.5, 1.0\n\t"
"v_fma_f32 v93, v89, 0.5, 1.0\n\t"
"v_fma_f32 v90, v90, v86, 1.0\n\t"
"v_mfma_f32_16x16x32_bf16 a[140:143], a[76:79], v[188:191], a[140:143]\n\t"
"s_waitcnt lgkmcnt(4)\n\t"
"v_fma_f32 v91, v91, v87, 1.0\n\t"
"v_fma_f32 v92, v92, v88, 1.0\n\t"
"v_fma_f32 v93, v93, v89, 1.0\n\t"
"v_mfma_f32_16x16x32_bf16 a[144:147], a[64:67], v[176:179], a[144:147]\n\t"
"ds_read_b64_tr_b16 a[76:77], v24 offset:22528\n\t"
"ds_read_b64_tr_b16 a[78:79], v24 offset:27648\n\t"
"v_mfma_f32_16x16x32_bf16 a[148:151], a[64:67], v[180:183], a[148:151]\n\t"
"v_rcp_f32 v90, v90\n\t"
"v_rcp_f32 v91, v91\n\t"
"v_mfma_f32_16x16x32_bf16 a[152:155], a[64:67], v[184:187], a[152:155]\n\t"
"v_rcp_f32 v92, v92\n\t"
"v_mfma_f32_16x16x32_bf16 a[156:159], a[64:67], v[188:191], a[156:159]\n\t"
"s_waitcnt lgkmcnt(4)\n\t"
"v_rcp_f32 v93, v93\n\t"
"v_fma_f32 v86, -v208, v90, v208\n\t"
"v_fma_f32 v87, -v209, v91, v209\n\t"
"v_mfma_f32_16x16x32_bf16 a[160:163], a[68:71], v[176:179], a[160:163]\n\t"
"ds_read_b64_tr_b16 a[64:65], v25 offset:22528\n\t"
"ds_read_b64_tr_b16 a[66:67], v25 offset:27648\n\t"
"v_fma_f32 v88, -v210, v92, v210\n\t"
"v_mfma_f32_16x16x32_bf16 a[164:167], a[68:71], v[180:183], a[164:167]\n\t"
"v_fma_f32 v89, -v211, v93, v211\n\t"
"v_cvt_pk_bf16_f32 v208, v86, v87\n\t"
"v_cvt_pk_bf16_f32 v209, v88, v89\n\t"
"v_mul_f32 v94, s88, v224\n\t"
"v_mfma_f32_16x16x32_bf16 a[168:171], a[68:71], v[184:187], a[168:171]\n\t"
"v_mul_f32 v95, s88, v225\n\t"
"v_mul_f32 v96, s88, v226\n\t"
"v_mul_f32 v97, s88, v227\n\t"
"v_mfma_f32_16x16x32_bf16 a[172:175], a[68:71], v[188:191], a[172:175]\n\t"
"s_waitcnt lgkmcnt(4)\n\t"
"v_exp_f32 v94, v94\n\t"
"v_mfma_f32_16x16x32_bf16 a[176:179], a[72:75], v[176:179], a[176:179]\n\t"
"ds_read_b64_tr_b16 a[68:69], v26 offset:22528\n\t"
"ds_read_b64_tr_b16 a[70:71], v26 offset:27648\n\t"
"v_exp_f32 v95, v95\n\t"
"v_mfma_f32_16x16x32_bf16 a[180:183], a[72:75], v[180:183], a[180:183]\n\t"
"v_exp_f32 v96, v96\n\t"
"v_mfma_f32_16x16x32_bf16 a[184:187], a[72:75], v[184:187], a[184:187]\n\t"
"v_exp_f32 v97, v97\n\t"
"v_fma_f32 v98, v94, 0.5, 1.0\n\t"
"v_fma_f32 v99, v95, 0.5, 1.0\n\t"
"v_mfma_f32_16x16x32_bf16 a[188:191], a[72:75], v[188:191], a[188:191]\n\t"
"s_waitcnt lgkmcnt(4)\n\t"
"v_fma_f32 v100, v96, 0.5, 1.0\n\t"
"v_fma_f32 v101, v97, 0.5, 1.0\n\t"
"v_fma_f32 v98, v98, v94, 1.0\n\t"
"v_mfma_f32_16x16x32_bf16 a[192:195], a[76:79], v[176:179], a[192:195]\n\t"
"ds_read_b64_tr_b16 a[72:73], v27 offset:20480\n\t"
"ds_read_b64_tr_b16 a[74:75], v27 offset:25600\n\t"
"v_fma_f32 v99, v99, v95, 1.0\n\t"
"v_fma_f32 v100, v100, v96, 1.0\n\t"
"v_mfma_f32_16x16x32_bf16 a[196:199], a[76:79], v[180:183], a[196:199]\n\t"
"v_fma_f32 v101, v101, v97, 1.0\n\t"
"v_rcp_f32 v98, v98\n\t"
"v_mfma_f32_16x16x32_bf16 a[200:203], a[76:79], v[184:187], a[200:203]\n\t"
"v_rcp_f32 v99, v99\n\t"
"v_mfma_f32_16x16x32_bf16 a[204:207], a[76:79], v[188:191], a[204:207]\n\t"
"s_waitcnt lgkmcnt(4)\n\t"
"v_rcp_f32 v100, v100\n\t"
"v_rcp_f32 v101, v101\n\t"
"v_mfma_f32_16x16x32_bf16 a[208:211], a[64:67], v[176:179], a[208:211]\n\t"
"ds_read_b64_tr_b16 a[76:77], v23 offset:40960\n\t"
"ds_read_b64_tr_b16 a[78:79], v23 offset:46080\n\t"
"v_fma_f32 v94, -v224, v98, v224\n\t"
"v_mfma_f32_16x16x32_bf16 a[212:215], a[64:67], v[180:183], a[212:215]\n\t"
"v_fma_f32 v95, -v225, v99, v225\n\t"
"v_fma_f32 v96, -v226, v100, v226\n\t"
"v_fma_f32 v97, -v227, v101, v227\n\t"
"v_cvt_pk_bf16_f32 v210, v94, v95\n\t"
"v_mfma_f32_16x16x32_bf16 a[216:219], a[64:67], v[184:187], a[216:219]\n\t"
"v_cvt_pk_bf16_f32 v211, v96, v97\n\t"
"v_mfma_f32_16x16x32_bf16 a[220:223], a[64:67], v[188:191], a[220:223]\n\t"
"s_waitcnt lgkmcnt(4)\n\t"
"buffer_store_dwordx4 v[208:211], v29, s[44:47], s83 offen offset:0 sc1\n\t"
"v_mul_f32 v86, s88, v212\n\t"
"v_mul_f32 v87, s88, v213\n\t"
"v_mfma_f32_16x16x32_bf16 a[224:227], a[68:71], v[176:179], a[224:227]\n\t"
"ds_read_b64_tr_b16 a[64:65], v24 offset:40960\n\t"
"ds_read_b64_tr_b16 a[66:67], v24 offset:46080\n\t"
"s_mov_b32 s92, 0x700\n\t"
"s_mov_b32 s93, 0\n\t"
"s_waitcnt vmcnt(1)\n\t"
"v_sub_u32 v110, v110, v48\n\t"
"v_subrev_u32 v110, 2, v110\n\t"
"v_cmp_lt_u32 vcc, 2, v110\n\t"
"s_nop 2\n\t"
"s_and_b64 vcc, vcc, s[92:93]\n\t"
"s_cbranch_scc0 LP3_ok%=\n\t"
"s_mov_b32 s94, 0\n\t"
"LP3_spin%=:\n\t"
"buffer_load_dword v37, v46, s[44:47], s89 offen sc1\n\t"
"s_waitcnt vmcnt(0)\n\t"
"v_cndmask_b32 v111, 1, 2, s[10:11]\n\t"
"v_add_u32 v111, v111, v37\n\t"
"buffer_store_dword v111, v47, s[44:47], 0 offen sc1\n\t"
"buffer_load_dword v110, v46, s[44:47], 0 offen sc1\n\t"
"s_waitcnt vmcnt(0)\n\t"
"s_add_u32 s94, s94, 1\n\t"
"s_cmp_gt_u32 s94, 0x4000\n\t"
"s_cbranch_scc1 LP3_out%=\n\t"
"v_sub_u32 v110, v110, v48\n\t"
"v_subrev_u32 v110, 2, v110\n\t"
"v_cmp_lt_u32 vcc, 2, v110\n\t"
"s_nop 2\n\t"
"s_and_b64 vcc, vcc, s[92:93]\n\t"
"s_cbranch_scc1 LP3_spin%=\n\t"
"LP3_out%=:\n\t"
"LP3_ok%=:\n\t"
"v_mfma_f32_16x16x32_bf16 a[228:231], a[68:71], v[180:183], a[228:231]\n\t"
"v_mfma_f32_16x16x32_bf16 a[232:235], a[68:71], v[184:187], a[232:235]\n\t"
"v_mul_f32 v88, s88, v214\n\t"
"v_mul_f32 v89, s88, v215\n\t"
"v_mfma_f32_16x16x32_bf16 a[236:239], a[68:71], v[188:191], a[236:239]\n\t"
"s_waitcnt lgkmcnt(4)\n\t"
"v_exp_f32 v86, v86\n\t"
"v_exp_f32 v87, v87\n\t"
"v_mfma_f32_16x16x32_bf16 a[240:243], a[72:75], v[176:179], a[240:243]\n\t"
"ds_read_b64_tr_b16 a[68:69], v25 offset:40960\n\t"
"ds_read_b64_tr_b16 a[70:71], v25 offset:46080\n\t"
"v_mfma_f32_16x16x32_bf16 a[244:247], a[72:75], v[180:183], a[244:247]\n\t"
"v_exp_f32 v88, v88\n\t"
"v_exp_f32 v89, v89\n\t"
"v_mfma_f32_16x16x32_bf16 a[248:251], a[72:75], v[184:187], a[248:251]\n\t"
"v_fma_f32 v90, v86, 0.5, 1.0\n\t"
"v_fma_f32 v91, v87, 0.5, 1.0\n\t"
"v_mfma_f32_16x16x32_bf16 a[252:255], a[72:75], v[188:191], a[252:255]\n\t"
"s_waitcnt lgkmcnt(4)\n\t"
"buffer_load_dwordx4 v[192:195], v28, s[44:47], s76 offen offset:0 sc1\n\t"
"v_fma_f32 v92, v88, 0.5, 1.0\n\t"
"v_fma_f32 v93, v89, 0.5, 1.0\n\t"
"v_fma_f32 v90, v90, v86, 1.0\n\t"
"v_mfma_f32_16x16x32_bf16 a[112:115], a[76:79], v[128:131], a[112:115]\n\t"
"ds_read_b64_tr_b16 a[72:73], v26 offset:40960\n\t"
"ds_read_b64_tr_b16 a[74:75], v26 offset:46080\n\t"
"v_fma_f32 v91, v91, v87, 1.0\n\t"
"v_mfma_f32_16x16x32_bf16 a[116:119], a[76:79], v[132:135], a[116:119]\n\t"
"v_fma_f32 v92, v92, v88, 1.0\n\t"
"v_fma_f32 v93, v93, v89, 1.0\n\t"
"v_rcp_f32 v90, v90\n\t"
"v_mfma_f32_16x16x32_bf16 a[120:123], a[76:79], v[136:139], a[120:123]\n\t"
"buffer_load_dwordx4 v[196:199], v28, s[44:47], s76 offen offset:1024 sc1\n\t"
"v_rcp_f32 v91, v91\n\t"
"v_mfma_f32_16x16x32_bf16 a[124:127], a[76:79], v[140:143], a[124:127]\n\t"
"s_waitcnt lgkmcnt(4)\n\t"
"v_rcp_f32 v92, v92\n\t"
"v_mfma_f32_16x16x32_bf16 a[128:131], a[64:67], v[128:131], a[128:131]\n\t"
"ds_read_b64_tr_b16 a[76:77], v23 offset:43008\n\t"
"ds_read_b64_tr_b16 a[78:79], v23 offset:48128\n\t"
"v_rcp_f32 v93, v93\n\t"
"v_mfma_f32_16x16x32_bf16 a[132:135], a[64:67], v[132:135], a[132:135]\n\t"
"v_fma_f32 v86, -v212, v90, v212\n\t"
"v_fma_f32 v87, -v213, v91, v213\n\t"
"v_fma_f32 v88, -v214, v92, v214\n\t"
"v_mfma_f32_16x16x32_bf16 a[136:139], a[64:67], v[136:139], a[136:139]\n\t"
"v_fma_f32 v89, -v215, v93, v215\n\t"
"v_cvt_pk_bf16_f32 v212, v86, v87\n\t"
"v_mfma_f32_16x16x32_bf16 a[140:143], a[64:67], v[140:143], a[140:143]\n\t"
"s_waitcnt lgkmcnt(4)\n\t"
"buffer_load_dwordx4 v[200:203], v28, s[44:47], s76 offen offset:2048 sc1\n\t"
"v_cvt_pk_bf16_f32 v213, v88, v89\n\t"
"v_mul_f32 v94, s88, v228\n\t"
"v_mul_f32 v95, s88, v229\n\t"
"v_mfma_f32_16x16x32_bf16 a[144:147], a[68:71], v[128:131], a[144:147]\n\t"
"ds_read_b64_tr_b16 a[64:65], v24 offset:43008\n\t"
"ds_read_b64_tr_b16 a[66:67], v24 offset:48128\n\t"
"v_mul_f32 v96, s88, v230\n\t"
"v_mfma_f32_16x16x32_bf16 a[148:151], a[68:71], v[132:135], a[148:151]\n\t"
"v_mul_f32 v97, s88, v231\n\t"
"v_exp_f32 v94, v94\n\t"
"v_mfma_f32_16x16x32_bf16 a[152:155], a[68:71], v[136:139], a[152:155]\n\t"
"v_exp_f32 v95, v95\n\t"
"v_mfma_f32_16x16x32_bf16 a[156:159], a[68:71], v[140:143], a[156:159]\n\t"
"s_waitcnt lgkmcnt(4)\n\t"
"buffer_load_dwordx4 v[204:207], v28, s[44:47], s76 offen offset:3072 sc1\n\t"
"v_exp_f32 v96, v96\n\t"
"v_mfma_f32_16x16x32_bf16 a[160:163], a[72:75], v[128:131], a[160:163]\n\t"
"ds_read_b64_tr_b16 a[68:69], v25 offset:43008\n\t"
"ds_read_b64_tr_b16 a[70:71], v25 offset:48128\n\t"
"v_exp_f32 v97, v97\n\t"
"v_mfma_f32_16x16x32_bf16 a[164:167], a[72:75], v[132:135], a[164:167]\n\t"
"v_fma_f32 v98, v94, 0.5, 1.0\n\t"
"v_fma_f32 v99, v95, 0.5, 1.0\n\t"
"v_fma_f32 v100, v96, 0.5, 1.0\n\t"
"v_mfma_f32_16x16x32_bf16 a[168:171], a[72:75], v[136:139], a[168:171]\n\t"
"v_fma_f32 v101, v97, 0.5, 1.0\n\t"
"v_fma_f32 v98, v98, v94, 1.0\n\t"
"v_mfma_f32_16x16x32_bf16 a[172:175], a[72:75], v[140:143], a[172:175]\n\t"
"s_waitcnt lgkmcnt(4)\n\t"
"buffer_load_dwordx4 v[240:243], v29, s[44:47], s76 offen offset:0 sc1\n\t"
"v_fma_f32 v99, v99, v95, 1.0\n\t"
"v_fma_f32 v100, v100, v96, 1.0\n\t"
"v_fma_f32 v101, v101, v97, 1.0\n\t"
"v_mfma_f32_16x16x32_bf16 a[176:179], a[76:79], v[128:131], a[176:179]\n\t"
"ds_read_b64_tr_b16 a[72:73], v26 offset:43008\n\t"
"ds_read_b64_tr_b16 a[74:75], v26 offset:48128\n\t"
"v_mfma_f32_16x16x32_bf16 a[180:183], a[76:79], v[132:135], a[180:183]\n\t"
"v_rcp_f32 v98, v98\n\t"
"v_rcp_f32 v99, v99\n\t"
"v_mfma_f32_16x16x32_bf16 a[184:187], a[76:79], v[136:139], a[184:187]\n\t"
"v_cndmask_b32 v111, 1, 3, s[10:11]\n\t"
"v_add_u32 v111, v111, v37\n\t"
"buffer_store_dword v111, v47, s[44:47], 0 offen sc1\n\t"
"v_mfma_f32_16x16x32_bf16 a[188:191], a[76:79], v[140:143], a[188:191]\n\t"
"s_waitcnt lgkmcnt(4)\n\t"
"v_rcp_f32 v100, v100\n\t"
"v_mfma_f32_16x16x32_bf16 a[192:195], a[64:67], v[128:131], a[192:195]\n\t"
"ds_read_b64_tr_b16 a[76:77], v27 offset:40960\n\t"
"ds_read_b64_tr_b16 a[78:79], v27 offset:46080\n\t"
"v_mfma_f32_16x16x32_bf16 a[196:199], a[64:67], v[132:135], a[196:199]\n\t"
"v_rcp_f32 v101, v101\n\t"
"v_fma_f32 v94, -v228, v98, v228\n\t"
"v_mfma_f32_16x16x32_bf16 a[200:203], a[64:67], v[136:139], a[200:203]\n\t"
"buffer_load_dwordx4 v[244:247], v29, s[44:47], s76 offen offset:1024 sc1\n\t"
"v_fma_f32 v95, -v229, v99, v229\n\t"
"v_fma_f32 v96, -v230, v100, v230\n\t"
"v_fma_f32 v97, -v231, v101, v231\n\t"
"v_mfma_f32_16x16x32_bf16 a[204:207], a[64:67], v[140:143], a[204:207]\n\t"
"s_waitcnt lgkmcnt(4)\n\t"
"v_cvt_pk_bf16_f32 v214, v94, v95\n\t"
"v_cvt_pk_bf16_f32 v215, v96, v97\n\t"
"v_mfma_f32_16x16x32_bf16 a[208:211], a[68:71], v[128:131], a[208:211]\n\t"
"ds_read_b64_tr_b16 a[64:65], v23 offset:51200\n\t"
"ds_read_b64_tr_b16 a[66:67], v23 offset:56320\n\t"
"buffer_store_dwordx4 v[212:215], v29, s[44:47], s83 offen offset:1024 sc1\n\t"
"v_mul_f32 v86, s88, v216\n\t"
"v_mfma_f32_16x16x32_bf16 a[212:215], a[68:71], v[132:135], a[212:215]\n\t"
"buffer_load_dwordx4 v[248:251], v29, s[44:47], s76 offen offset:2048 sc1\n\t"
"v_mul_f32 v87, s88, v217\n\t"
"v_mul_f32 v88, s88, v218\n\t"
"v_mfma_f32_16x16x32_bf16 a[216:219], a[68:71], v[136:139], a[216:219]\n\t"
"v_mul_f32 v89, s88, v219\n\t"
"v_exp_f32 v86, v86\n\t"
"v_mfma_f32_16x16x32_bf16 a[220:223], a[68:71], v[140:143], a[220:223]\n\t"
"s_waitcnt lgkmcnt(4)\n\t"
"v_exp_f32 v87, v87\n\t"
"v_exp_f32 v88, v88\n\t"
"v_mfma_f32_16x16x32_bf16 a[224:227], a[72:75], v[128:131], a[224:227]\n\t"
"ds_read_b64_tr_b16 a[68:69], v24 offset:51200\n\t"
"ds_read_b64_tr_b16 a[70:71], v24 offset:56320\n\t"
"v_mfma_f32_16x16x32_bf16 a[228:231], a[72:75], v[132:135], a[228:231]\n\t"
"v_exp_f32 v89, v89\n\t"
"v_fma_f32 v90, v86, 0.5, 1.0\n\t"
"v_mfma_f32_16x16x32_bf16 a[232:235], a[72:75], v[136:139], a[232:235]\n\t"
"buffer_load_dwordx4 v[252:255], v29, s[44:47], s76 offen offset:3072 sc1\n\t"
"v_fma_f32 v91, v87, 0.5, 1.0\n\t"
"v_fma_f32 v92, v88, 0.5, 1.0\n\t"
"v_fma_f32 v93, v89, 0.5, 1.0\n\t"
"v_mfma_f32_16x16x32_bf16 a[236:239], a[72:75], v[140:143], a[236:239]\n\t"
"s_waitcnt lgkmcnt(4)\n\t"
"v_fma_f32 v90, v90, v86, 1.0\n\t"
"v_fma_f32 v91, v91, v87, 1.0\n\t"
"v_fma_f32 v92, v92, v88, 1.0\n\t"
"v_mfma_f32_16x16x32_bf16 a[240:243], a[76:79], v[128:131], a[240:243]\n\t"
"ds_read_b64_tr_b16 a[72:73], v25 offset:51200\n\t"
"ds_read_b64_tr_b16 a[74:75], v25 offset:56320\n\t"
"v_fma_f32 v93, v93, v89, 1.0\n\t"
"v_mfma_f32_16x16x32_bf16 a[244:247], a[76:79], v[132:135], a[244:247]\n\t"
"buffer_load_dwordx4 v[112:115], v28, s[44:47], s77 offen offset:0 sc1\n\t"
"v_rcp_f32 v90, v90\n\t"
"v_mfma_f32_16x16x32_bf16 a[248:251], a[76:79], v[136:139], a[248:251]\n\t"
"v_rcp_f32 v91, v91\n\t"
"v_rcp_f32 v92, v92\n\t"
"v_mfma_f32_16x16x32_bf16 a[252:255], a[76:79], v[140:143], a[252:255]\n\t"
"s_waitcnt lgkmcnt(4)\n\t"
"v_rcp_f32 v93, v93\n\t"
"v_fma_f32 v86, -v216, v90, v216\n\t"
"v_mfma_f32_16x16x32_bf16 a[112:115], a[64:67], v[160:163], a[112:115]\n\t"
"ds_read_b64_tr_b16 a[76:77], v26 offset:51200\n\t"
"ds_read_b64_tr_b16 a[78:79], v26 offset:56320\n\t"
"v_fma_f32 v87, -v217, v91, v217\n\t"
"v_fma_f32 v88, -v218, v92, v218\n\t"
"v_mfma_f32_16x16x32_bf16 a[116:119], a[64:67], v[164:167], a[116:119]\n\t"
"buffer_load_dwordx4 v[116:119], v28, s[44:47], s77 offen offset:1024 sc1\n\t"
"v_fma_f32 v89, -v219, v93, v219\n\t"
"v_cvt_pk_bf16_f32 v216, v86, v87\n\t"
"v_mfma_f32_16x16x32_bf16 a[120:123], a[64:67], v[168:171], a[120:123]\n\t"
"v_cvt_pk_bf16_f32 v217, v88, v89\n\t"
"v_mul_f32 v94, s88, v232\n\t"
"v_mul_f32 v95, s88, v233\n\t"
"v_mfma_f32_16x16x32_bf16 a[124:127], a[64:67], v[172:175], a[124:127]\n\t"
"s_waitcnt lgkmcnt(4)\n\t"
"v_mul_f32 v96, s88, v234\n\t"
"v_mul_f32 v97, s88, v235\n\t"
"v_exp_f32 v94, v94\n\t"
"v_mfma_f32_16x16x32_bf16 a[128:131], a[68:71], v[160:163], a[128:131]\n\t"
"ds_read_b64_tr_b16 a[64:65], v23 offset:53248\n\t"
"ds_read_b64_tr_b16 a[66:67], v23 offset:58368\n\t"
"buffer_load_dwordx4 v[120:123], v28, s[44:47], s77 offen offset:2048 sc1\n\t"
"v_mfma_f32_16x16x32_bf16 a[132:135], a[68:71], v[164:167], a[132:135]\n\t"
"v_exp_f32 v95, v95\n\t"
"v_mfma_f32_16x16x32_bf16 a[136:139], a[68:71], v[168:171], a[136:139]\n\t"
"v_exp_f32 v96, v96\n\t"
"v_exp_f32 v97, v97\n\t"
"v_mfma_f32_16x16x32_bf16 a[140:143], a[68:71], v[172:175], a[140:143]\n\t"
"s_waitcnt lgkmcnt(4)\n\t"
"v_fma_f32 v98, v94, 0.5, 1.0\n\t"
"v_fma_f32 v99, v95, 0.5, 1.0\n\t"
"v_fma_f32 v100, v96, 0.5, 1.0\n\t"
"v_mfma_f32_16x16x32_bf16 a[144:147], a[72:75], v[160:163], a[144:147]\n\t"
"ds_read_b64_tr_b16 a[68:69], v24 offset:53248\n\t"
"ds_read_b64_tr_b16 a[70:71], v24 offset:58368\n\t"
"v_fma_f32 v101, v97, 0.5, 1.0\n\t"
"v_mfma_f32_16x16x32_bf16 a[148:151], a[72:75], v[164:167], a[148:151]\n\t"
"buffer_load_dwordx4 v[124:127], v28, s[44:47], s77 offen offset:3072 sc1\n\t"
"v_fma_f32 v98, v98, v94, 1.0\n\t"
"v_fma_f32 v99, v99, v95, 1.0\n\t"
"v_fma_f32 v100, v100, v96, 1.0\n\t"
"v_mfma_f32_16x16x32_bf16 a[152:155], a[72:75], v[168:171], a[152:155]\n\t"
"v_fma_f32 v101, v101, v97, 1.0\n\t"
"v_rcp_f32 v98, v98\n\t"
"v_mfma_f32_16x16x32_bf16 a[156:159], a[72:75], v[172:175], a[156:159]\n\t"
"s_waitcnt lgkmcnt(4)\n\t"
"v_rcp_f32 v99, v99\n\t"
"v_rcp_f32 v100, v100\n\t"
"v_mfma_f32_16x16x32_bf16 a[160:163], a[76:79], v[160:163], a[160:163]\n\t"
"ds_read_b64_tr_b16 a[72:73], v25 offset:53248\n\t"
"ds_read_b64_tr_b16 a[74:75], v25 offset:58368\n\t"
"v_mfma_f32_16x16x32_bf16 a[164:167], a[76:79], v[164:167], a[164:167]\n\t"
"v_rcp_f32 v101, v101\n\t"
"v_mfma_f32_16x16x32_bf16 a[168:171], a[76:79], v[168:171], a[168:171]\n\t"
"buffer_load_dwordx4 v[144:147], v29, s[44:47], s77 offen offset:0 sc1\n\t"
"v_fma_f32 v94, -v232, v98, v232\n\t"
"v_fma_f32 v95, -v233, v99, v233\n\t"
"v_fma_f32 v96, -v234, v100, v234\n\t"
"v_mfma_f32_16x16x32_bf16 a[172:175], a[76:79], v[172:175], a[172:175]\n\t"
"s_waitcnt lgkmcnt(4)\n\t"
"v_fma_f32 v97, -v235, v101, v235\n\t"
"v_cvt_pk_bf16_f32 v218, v94, v95\n\t"
"v_cvt_pk_bf16_f32 v219, v96, v97\n\t"
"v_mfma_f32_16x16x32_bf16 a[176:179], a[64:67], v[160:163], a[176:179]\n\t"
"ds_read_b64_tr_b16 a[76:77], v26 offset:53248\n\t"
"ds_read_b64_tr_b16 a[78:79], v26 offset:58368\n\t"
"buffer_store_dwordx4 v[216:219], v29, s[44:47], s83 offen offset:2048 sc1\n\t"
"buffer_load_dwordx4 v[148:151], v29, s[44:47], s77 offen offset:1024 sc1\n\t"
"v_mfma_f32_16x16x32_bf16 a[180:183], a[64:67], v[164:167], a[180:183]\n\t"
"v_mul_f32 v86, s88, v220\n\t"
"v_mul_f32 v87, s88, v221\n\t"
"v_mul_f32 v88, s88, v222\n\t"
"v_mfma_f32_16x16x32_bf16 a[184:187], a[64:67], v[168:171], a[184:187]\n\t"
"v_mul_f32 v89, s88, v223\n\t"
"v_exp_f32 v86, v86\n\t"
"v_mfma_f32_16x16x32_bf16 a[188:191], a[64:67], v[172:175], a[188:191]\n\t"
"s_waitcnt lgkmcnt(4)\n\t"
"v_exp_f32 v87, v87\n\t"
"v_exp_f32 v88, v88\n\t"
"v_mfma_f32_16x16x32_bf16 a[192:195], a[68:71], v[160:163], a[192:195]\n\t"
"ds_read_b64_tr_b16 a[64:65], v27 offset:51200\n\t"
"ds_read_b64_tr_b16 a[66:67], v27 offset:56320\n\t"
"v_mfma_f32_16x16x32_bf16 a[196:199], a[68:71], v[164:167], a[196:199]\n\t"
"v_exp_f32 v89, v89\n\t"
"v_mfma_f32_16x16x32_bf16 a[200:203], a[68:71], v[168:171], a[200:203]\n\t"
"buffer_load_dwordx4 v[152:155], v29, s[44:47], s77 offen offset:2048 sc1\n\t"
"v_fma_f32 v90, v86, 0.5, 1.0\n\t"
"v_fma_f32 v91, v87, 0.5, 1.0\n\t"
"v_fma_f32 v92, v88, 0.5, 1.0\n\t"
"v_mfma_f32_16x16x32_bf16 a[204:207], a[68:71], v[172:175], a[204:207]\n\t"
"s_waitcnt lgkmcnt(4)\n\t"
"v_fma_f32 v93, v89, 0.5, 1.0\n\t"
"v_fma_f32 v90, v90, v86, 1.0\n\t"
"v_fma_f32 v91, v91, v87, 1.0\n\t"
"v_mfma_f32_16x16x32_bf16 a[208:211], a[72:75], v[160:163], a[208:211]\n\t"
"s_waitcnt lgkmcnt(0)\n\t"
"s_barrier\n\t"
"ds_read_b64_tr_b16 a[68:69], v23 offset:30720\n\t"
"ds_read_b64_tr_b16 a[70:71], v23 offset:35840\n\t"
"v_fma_f32 v92, v92, v88, 1.0\n\t"
"v_mfma_f32_16x16x32_bf16 a[212:215], a[72:75], v[164:167], a[212:215]\n\t"
"buffer_load_dwordx4 v[156:159], v29, s[44:47], s77 offen offset:3072 sc1\n\t"
"v_fma_f32 v93, v93, v89, 1.0\n\t"
"v_rcp_f32 v90, v90\n\t"
"v_mfma_f32_16x16x32_bf16 a[216:219], a[72:75], v[168:171], a[216:219]\n\t"
"v_rcp_f32 v91, v91\n\t"
"v_mfma_f32_16x16x32_bf16 a[220:223], a[72:75], v[172:175], a[220:223]\n\t"
"v_rcp_f32 v92, v92\n\t"
"v_rcp_f32 v93, v93\n\t"
"v_mfma_f32_16x16x32_bf16 a[224:227], a[76:79], v[160:163], a[224:227]\n\t"
"ds_read_b64_tr_b16 a[72:73], v24 offset:30720\n\t"
"ds_read_b64_tr_b16 a[74:75], v24 offset:35840\n\t"
"v_fma_f32 v86, -v220, v90, v220\n\t"
"v_mfma_f32_16x16x32_bf16 a[228:231], a[76:79], v[164:167], a[228:231]\n\t"
"v_fma_f32 v87, -v221, v91, v221\n\t"
"v_mfma_f32_16x16x32_bf16 a[232:235], a[76:79], v[168:171], a[232:235]\n\t"
"buffer_load_dwordx4 v[176:179], v28, s[44:47], s78 offen offset:0 sc1\n\t"
"v_fma_f32 v88, -v222, v92, v222\n\t"
"v_fma_f32 v89, -v223, v93, v223\n\t"
"v_cvt_pk_bf16_f32 v220, v86, v87\n\t"
"v_mfma_f32_16x16x32_bf16 a[236:239], a[76:79], v[172:175], a[236:239]\n\t"
"v_cvt_pk_bf16_f32 v221, v88, v89\n\t"
"v_mul_f32 v94, s88, v236\n\t"
"v_mul_f32 v95, s88, v237\n\t"
"v_mfma_f32_16x16x32_bf16 a[240:243], a[64:67], v[160:163], a[240:243]\n\t"
"ds_read_b64_tr_b16 a[76:77], v25 offset:30720\n\t"
"ds_read_b64_tr_b16 a[78:79], v25 offset:35840\n\t"
"v_mul_f32 v96, s88, v238\n\t"
"v_mul_f32 v97, s88, v239\n\t"
"v_mfma_f32_16x16x32_bf16 a[244:247], a[64:67], v[164:167], a[244:247]\n\t"
"buffer_load_dwordx4 v[180:183], v28, s[44:47], s78 offen offset:1024 sc1\n\t"
"v_exp_f32 v94, v94\n\t"
"v_mfma_f32_16x16x32_bf16 a[248:251], a[64:67], v[168:171], a[248:251]\n\t"
"v_exp_f32 v95, v95\n\t"
"v_mfma_f32_16x16x32_bf16 a[252:255], a[64:67], v[172:175], a[252:255]\n\t"
"v_exp_f32 v96, v96\n\t"
"v_exp_f32 v97, v97\n\t"
"v_fma_f32 v98, v94, 0.5, 1.0\n\t"
"v_fma_f32 v99, v95, 0.5, 1.0\n\t"
"v_fma_f32 v100, v96, 0.5, 1.0\n\t"
"buffer_load_dwordx4 v[184:187], v28, s[44:47], s78 offen offset:2048 sc1\n\t"
"v_fma_f32 v101, v97, 0.5, 1.0\n\t"
"v_fma_f32 v98, v98, v94, 1.0\n\t"
"v_fma_f32 v99, v99, v95, 1.0\n\t"
"v_fma_f32 v100, v100, v96, 1.0\n\t"
"v_fma_f32 v101, v101, v97, 1.0\n\t"
"v_rcp_f32 v98, v98\n\t"
"v_rcp_f32 v99, v99\n\t"
"v_rcp_f32 v100, v100\n\t"
"buffer_load_dwordx4 v[188:191], v28, s[44:47], s78 offen offset:3072 sc1\n\t"
"v_rcp_f32 v101, v101\n\t"
"v_fma_f32 v94, -v236, v98, v236\n\t"
"v_fma_f32 v95, -v237, v99, v237\n\t"
"v_fma_f32 v96, -v238, v100, v238\n\t"
"v_fma_f32 v97, -v239, v101, v239\n\t"
"v_cvt_pk_bf16_f32 v222, v94, v95\n\t"
"v_cvt_pk_bf16_f32 v223, v96, v97\n\t"
"buffer_store_dwordx4 v[220:223], v29, s[44:47], s83 offen offset:3072 sc1\n\t"
"s_waitcnt lgkmcnt(4)\n\t"
"v_mfma_f32_16x16x32_bf16 a[112:115], a[68:71], v[208:211], a[112:115]\n\t"
"ds_read_b64_tr_b16 a[64:65], v26 offset:30720\n\t"
"ds_read_b64_tr_b16 a[66:67], v26 offset:35840\n\t"
"v_mfma_f32_16x16x32_bf16 a[116:119], a[68:71], v[212:215], a[116:119]\n\t"
"buffer_load_dword v110, v46, s[44:47], 0 offen sc1\n\t"
"v_mfma_f32_16x16x32_bf16 a[120:123], a[68:71], v[216:219], a[120:123]\n\t"
"buffer_load_dwordx4 v[224:227], v29, s[44:47], s78 offen offset:0 sc1\n\t"
"v_mfma_f32_16x16x32_bf16 a[124:127], a[68:71], v[220:223], a[124:127]\n\t"
"s_waitcnt lgkmcnt(4)\n\t"
"v_mfma_f32_16x16x32_bf16 a[128:131], a[72:75], v[208:211], a[128:131]\n\t"
"ds_read_b64_tr_b16 a[68:69], v23 offset:32768\n\t"
"ds_read_b64_tr_b16 a[70:71], v23 offset:37888\n\t"
"v_mfma_f32_16x16x32_bf16 a[132:135], a[72:75], v[212:215], a[132:135]\n\t"
"v_mfma_f32_16x16x32_bf16 a[136:139], a[72:75], v[216:219], a[136:139]\n\t"
"buffer_load_dwordx4 v[228:231], v29, s[44:47], s78 offen offset:1024 sc1\n\t"
"v_mfma_f32_16x16x32_bf16 a[140:143], a[72:75], v[220:223], a[140:143]\n\t"
"s_waitcnt lgkmcnt(4)\n\t"
"v_mfma_f32_16x16x32_bf16 a[144:147], a[76:79], v[208:211], a[144:147]\n\t"
"ds_read_b64_tr_b16 a[72:73], v24 offset:32768\n\t"
"ds_read_b64_tr_b16 a[74:75], v24 offset:37888\n\t"
"v_mfma_f32_16x16x32_bf16 a[148:151], a[76:79], v[212:215], a[148:151]\n\t"
"v_mfma_f32_16x16x32_bf16 a[152:155], a[76:79], v[216:219], a[152:155]\n\t"
"buffer_load_dwordx4 v[232:235], v29, s[44:47], s78 offen offset:2048 sc1\n\t"
"v_mfma_f32_16x16x32_bf16 a[156:159], a[76:79], v[220:223], a[156:159]\n\t"
"s_waitcnt lgkmcnt(4)\n\t"
"v_mfma_f32_16x16x32_bf16 a[160:163], a[64:67], v[208:211], a[160:163]\n\t"
"ds_read_b64_tr_b16 a[76:77], v25 offset:32768\n\t"
"ds_read_b64_tr_b16 a[78:79], v25 offset:37888\n\t"
"v_mfma_f32_16x16x32_bf16 a[164:167], a[64:67], v[212:215], a[164:167]\n\t"
"v_mfma_f32_16x16x32_bf16 a[168:171], a[64:67], v[216:219], a[168:171]\n\t"
"buffer_load_dwordx4 v[236:239], v29, s[44:47], s78 offen offset:3072 sc1\n\t"
"v_mfma_f32_16x16x32_bf16 a[172:175], a[64:67], v[220:223], a[172:175]\n\t"
"s_waitcnt lgkmcnt(4)\n\t"
"v_mfma_f32_16x16x32_bf16 a[176:179], a[68:71], v[208:211], a[176:179]\n\t"
"ds_read_b64_tr_b16 a[64:65], v26 offset:32768\n\t"
"ds_read_b64_tr_b16 a[66:67], v26 offset:37888\n\t"
"v_mfma_f32_16x16x32_bf16 a[180:183], a[68:71], v[212:215], a[180:183]\n\t"
"v_mfma_f32_16x16x32_bf16 a[184:187], a[68:71], v[216:219], a[184:187]\n\t"
"s_add_u32 m0, s87, 0\n\t"
"s_nop 0\n\t"
"buffer_load_dwordx4 v20, s[40:43], s71 offen offset:0 lds\n\t"
"v_mfma_f32_16x16x32_bf16 a[188:191], a[68:71], v[220:223], a[188:191]\n\t"
"s_waitcnt lgkmcnt(4)\n\t"
"v_mfma_f32_16x16x32_bf16 a[192:195], a[72:75], v[208:211], a[192:195]\n\t"
"ds_read_b64_tr_b16 a[68:69], v27 offset:30720\n\t"
"ds_read_b64_tr_b16 a[70:71], v27 offset:35840\n\t"
"v_mfma_f32_16x16x32_bf16 a[196:199], a[72:75], v[212:215], a[196:199]\n\t"
"v_mfma_f32_16x16x32_bf16 a[200:203], a[72:75], v[216:219], a[200:203]\n\t"
"s_add_u32 m0, s87, 1024\n\t"
"s_nop 0\n\t"
"buffer_load_dwordx4 v21, s[40:43], s71 offen offset:0 lds\n\t"
"v_mfma_f32_16x16x32_bf16 a[204:207], a[72:75], v[220:223], a[204:207]\n\t"
"s_waitcnt lgkmcnt(4)\n\t"
"v_mfma_f32_16x16x32_bf16 a[208:211], a[76:79], v[208:211], a[208:211]\n\t"
"s_waitcnt lgkmcnt(0)\n\t"
"s_barrier\n\t"
"v_add_u32 v23, 61440, v23\n\t"
"v_add_u32 v24, 61440, v24\n\t"
"v_add_u32 v25, 61440, v25\n\t"
"v_add_u32 v26, 61440, v26\n\t"
"v_add_u32 v27, 61440, v27\n\t"
"ds_read_b64_tr_b16 a[72:73], v23 offset:0\n\t"
"ds_read_b64_tr_b16 a[74:75], v23 offset:5120\n\t"
"v_mfma_f32_16x16x32_bf16 a[212:215], a[76:79], v[212:215], a[212:215]\n\t"
"v_mfma_f32_16x16x32_bf16 a[216:219], a[76:79], v[216:219], a[216:219]\n\t"
"s_add_u32 m0, s87, 1920\n\t"
"s_nop 0\n\t"
"buffer_load_dwordx4 v20, s[40:43], s71 offen offset:128 lds\n\t"
"v_mfma_f32_16x16x32_bf16 a[220:223], a[76:79], v[220:223], a[220:223]\n\t"
"v_mfma_f32_16x16x32_bf16 a[224:227], a[64:67], v[208:211], a[224:227]\n\t"
"ds_read_b64_tr_b16 a[76:77], v24 offset:0\n\t"
"ds_read_b64_tr_b16 a[78:79], v24 offset:5120\n\t"
"v_mfma_f32_16x16x32_bf16 a[228:231], a[64:67], v[212:215], a[228:231]\n\t"
"v_mfma_f32_16x16x32_bf16 a[232:235], a[64:67], v[216:219], a[232:235]\n\t"
"s_add_u32 m0, s87, 2944\n\t"
"s_nop 0\n\t"
"buffer_load_dwordx4 v21, s[40:43], s71 offen offset:128 lds\n\t"
"v_mfma_f32_16x16x32_bf16 a[236:239], a[64:67], v[220:223], a[236:239]\n\t"
"v_mfma_f32_16x16x32_bf16 a[240:243], a[68:71], v[208:211], a[240:243]\n\t"
"ds_read_b64_tr_b16 a[64:65], v25 offset:0\n\t"
"ds_read_b64_tr_b16 a[66:67], v25 offset:5120\n\t"
"v_mfma_f32_16x16x32_bf16 a[244:247], a[68:71], v[212:215], a[244:247]\n\t"
"v_mfma_f32_16x16x32_bf16 a[248:251], a[68:71], v[216:219], a[248:251]\n\t"
"s_add_u32 m0, s87, 4096\n\t"
"s_nop 0\n\t"
"buffer_load_dwordx4 v22, s[40:43], s71 offen lds\n\t"
"v_mfma_f32_16x16x32_bf16 a[252:255], a[68:71], v[220:223], a[252:255]\n\t"
"s_waitcnt lgkmcnt(4)\n\t"
"s_waitcnt vmcnt(30)\n\t"
"v_mfma_f32_16x16x32_bf16 a[112:115], a[72:75], v[192:195], a[112:115]\n\t"
"ds_read_b64_tr_b16 a[68:69], v26 offset:0\n\t"
"ds_read_b64_tr_b16 a[70:71], v26 offset:5120\n\t"
"v_mfma_f32_16x16x32_bf16 a[116:119], a[72:75], v[196:199], a[116:119]\n\t"
"v_mfma_f32_16x16x32_bf16 a[120:123], a[72:75], v[200:203], a[120:123]\n\t"
"v_mfma_f32_16x16x32_bf16 a[124:127], a[72:75], v[204:207], a[124:127]\n\t"
"s_waitcnt lgkmcnt(4)\n\t"
"s_waitcnt vmcnt(10)\n\t"
"v_cndmask_b32 v111, 1, 4, s[10:11]\n\t"
"v_add_u32 v111, v111, v37\n\t"
"buffer_store_dword v111, v47, s[44:47], 0 offen sc1\n\t"
"v_mfma_f32_16x16x32_bf16 a[128:131], a[76:79], v[192:195], a[128:131]\n\t"
"ds_read_b64_tr_b16 a[72:73], v23 offset:2048\n\t"
"ds_read_b64_tr_b16 a[74:75], v23 offset:7168\n\t"
"v_mfma_f32_16x16x32_bf16 a[132:135], a[76:79], v[196:199], a[132:135]\n\t"
"v_mfma_f32_16x16x32_bf16 a[136:139], a[76:79], v[200:203], a[136:139]\n\t"
"v_mfma_f32_16x16x32_bf16 a[140:143], a[76:79], v[204:207], a[140:143]\n\t"
"s_waitcnt lgkmcnt(4)\n\t"
"s_mov_b32 s92, 0x700\n\t"
"s_mov_b32 s93, 0\n\t"
"s_waitcnt vmcnt(10)\n\t"
"v_sub_u32 v110, v110, v48\n\t"
"v_subrev_u32 v110, 3, v110\n\t"
"v_cmp_lt_u32 vcc, 1, v110\n\t"
"s_nop 2\n\t"
"s_and_b64 vcc, vcc, s[92:93]\n\t"
"s_cbranch_scc0 LP4_ok%=\n\t"
"s_mov_b32 s94, 0\n\t"
"LP4_spin%=:\n\t"
"buffer_load_dword v37, v46, s[44:47], s89 offen sc1\n\t"
"s_waitcnt vmcnt(0)\n\t"
"v_cndmask_b32 v111, 1, 4, s[10:11]\n\t"
"v_add_u32 v111, v111, v37\n\t"
"buffer_store_dword v111, v47, s[44:47], 0 offen sc1\n\t"
"buffer_load_dword v110, v46, s[44:47], 0 offen sc1\n\t"
"s_waitcnt vmcnt(0)\n\t"
"s_add_u32 s94, s94, 1\n\t"
"s_cmp_gt_u32 s94, 0x4000\n\t"
"s_cbranch_scc1 LP4_out%=\n\t"
"v_sub_u32 v110, v110, v48\n\t"
"v_subrev_u32 v110, 3, v110\n\t"
"v_cmp_lt_u32 vcc, 1, v110\n\t"
"s_nop 2\n\t"
"s_and_b64 vcc, vcc, s[92:93]\n\t"
"s_cbranch_scc1 LP4_spin%=\n\t"
"LP4_out%=:\n\t"
"LP4_ok%=:\n\t"
"v_mfma_f32_16x16x32_bf16 a[144:147], a[64:67], v[192:195], a[144:147]\n\t"
"ds_read_b64_tr_b16 a[76:77], v24 offset:2048\n\t"
"ds_read_b64_tr_b16 a[78:79], v24 offset:7168\n\t"
"v_mfma_f32_16x16x32_bf16 a[148:151], a[64:67], v[196:199], a[148:151]\n\t"
"v_mfma_f32_16x16x32_bf16 a[152:155], a[64:67], v[200:203], a[152:155]\n\t"
"buffer_load_dwordx4 v[128:131], v28, s[44:47], s79 offen offset:0 sc1\n\t"
"v_mfma_f32_16x16x32_bf16 a[156:159], a[64:67], v[204:207], a[156:159]\n\t"
"s_waitcnt lgkmcnt(4)\n\t"
"v_mfma_f32_16x16x32_bf16 a[160:163], a[68:71], v[192:195], a[160:163]\n\t"
"ds_read_b64_tr_b16 a[64:65], v25 offset:2048\n\t"
"ds_read_b64_tr_b16 a[66:67], v25 offset:7168\n\t"
"v_mfma_f32_16x16x32_bf16 a[164:167], a[68:71], v[196:199], a[164:167]\n\t"
"v_mfma_f32_16x16x32_bf16 a[168:171], a[68:71], v[200:203], a[168:171]\n\t"
"buffer_load_dwordx4 v[132:135], v28, s[44:47], s79 offen offset:1024 sc1\n\t"
"v_mfma_f32_16x16x32_bf16 a[172:175], a[68:71], v[204:207], a[172:175]\n\t"
"s_waitcnt lgkmcnt(4)\n\t"
"v_mfma_f32_16x16x32_bf16 a[176:179], a[72:75], v[192:195], a[176:179]\n\t"
"ds_read_b64_tr_b16 a[68:69], v26 offset:2048\n\t"
"ds_read_b64_tr_b16 a[70:71], v26 offset:7168\n\t"
"v_mfma_f32_16x16x32_bf16 a[180:183], a[72:75], v[196:199], a[180:183]\n\t"
"v_mfma_f32_16x16x32_bf16 a[184:187], a[72:75], v[200:203], a[184:187]\n\t"
"buffer_load_dwordx4 v[136:139], v28, s[44:47], s79 offen offset:2048 sc1\n\t"
"v_mfma_f32_16x16x32_bf16 a[188:191], a[72:75], v[204:207], a[188:191]\n\t"
"s_waitcnt lgkmcnt(4)\n\t"
"v_mfma_f32_16x16x32_bf16 a[192:195], a[76:79], v[192:195], a[192:195]\n\t"
"ds_read_b64_tr_b16 a[72:73], v27 offset:0\n\t"
"ds_read_b64_tr_b16 a[74:75], v27 offset:5120\n\t"
"v_mfma_f32_16x16x32_bf16 a[196:199], a[76:79], v[196:199], a[196:199]\n\t"
"v_mfma_f32_16x16x32_bf16 a[200:203], a[76:79], v[200:203], a[200:203]\n\t"
"buffer_load_dwordx4 v[140:143], v28, s[44:47], s79 offen offset:3072 sc1\n\t"
"v_mfma_f32_16x16x32_bf16 a[204:207], a[76:79], v[204:207], a[204:207]\n\t"
"s_waitcnt lgkmcnt(4)\n\t"
"v_mfma_f32_16x16x32_bf16 a[208:211], a[64:67], v[192:195], a[208:211]\n\t"
"ds_read_b64_tr_b16 a[76:77], v23 offset:10240\n\t"
"ds_read_b64_tr_b16 a[78:79], v23 offset:15360\n\t"
"v_mfma_f32_16x16x32_bf16 a[212:215], a[64:67], v[196:199], a[212:215]\n\t"
"v_mfma_f32_16x16x32_bf16 a[216:219], a[64:67], v[200:203], a[216:219]\n\t"
"buffer_load_dwordx4 v[160:163], v28, s[44:47], s80 offen offset:0 sc1\n\t"
"v_mfma_f32_16x16x32_bf16 a[220:223], a[64:67], v[204:207], a[220:223]\n\t"
"s_waitcnt lgkmcnt(4)\n\t"
"v_mfma_f32_16x16x32_bf16 a[224:227], a[68:71], v[192:195], a[224:227]\n\t"
"ds_read_b64_tr_b16 a[64:65], v24 offset:10240\n\t"
"ds_read_b64_tr_b16 a[66:67], v24 offset:15360\n\t"
"v_mfma_f32_16x16x32_bf16 a[228:231], a[68:71], v[196:199], a[228:231]\n\t"
"v_mfma_f32_16x16x32_bf16 a[232:235], a[68:71], v[200:203], a[232:235]\n\t"
"buffer_load_dwordx4 v[164:167], v28, s[44:47], s80 offen offset:1024 sc1\n\t"
"v_mfma_f32_16x16x32_bf16 a[236:239], a[68:71], v[204:207], a[236:239]\n\t"
"s_waitcnt lgkmcnt(4)\n\t"
"v_mfma_f32_16x16x32_bf16 a[240:243], a[72:75], v[192:195], a[240:243]\n\t"
"ds_read_b64_tr_b16 a[68:69], v25 offset:10240\n\t"
"ds_read_b64_tr_b16 a[70:71], v25 offset:15360\n\t"
"v_mfma_f32_16x16x32_bf16 a[244:247], a[72:75], v[196:199], a[244:247]\n\t"
"v_mfma_f32_16x16x32_bf16 a[248:251], a[72:75], v[200:203], a[248:251]\n\t"
"buffer_load_dwordx4 v[168:171], v28, s[44:47], s80 offen offset:2048 sc1\n\t"
"v_mfma_f32_16x16x32_bf16 a[252:255], a[72:75], v[204:207], a[252:255]\n\t"
"s_waitcnt lgkmcnt(4)\n\t"
"v_mfma_f32_16x16x32_bf16 a[112:115], a[76:79], v[240:243], a[112:115]\n\t"
"ds_read_b64_tr_b16 a[72:73], v26 offset:10240\n\t"
"ds_read_b64_tr_b16 a[74:75], v26 offset:15360\n\t"
"v_mfma_f32_16x16x32_bf16 a[116:119], a[76:79], v[244:247], a[116:119]\n\t"
"v_mfma_f32_16x16x32_bf16 a[120:123], a[76:79], v[248:251], a[120:123]\n\t"
"buffer_load_dwordx4 v[172:175], v28, s[44:47], s80 offen offset:3072 sc1\n\t"
"v_mfma_f32_16x16x32_bf16 a[124:127], a[76:79], v[252:255], a[124:127]\n\t"
"s_waitcnt lgkmcnt(4)\n\t"
"v_mfma_f32_16x16x32_bf16 a[128:131], a[64:67], v[240:243], a[128:131]\n\t"
"ds_read_b64_tr_b16 a[76:77], v23 offset:12288\n\t"
"ds_read_b64_tr_b16 a[78:79], v23 offset:17408\n\t"
"v_mfma_f32_16x16x32_bf16 a[132:135], a[64:67], v[244:247], a[132:135]\n\t"
"v_mfma_f32_16x16x32_bf16 a[136:139], a[64:67], v[248:251], a[136:139]\n\t"
"buffer_load_dwordx4 v[208:211], v28, s[44:47], s81 offen offset:0 sc1\n\t"
"v_mfma_f32_16x16x32_bf16 a[140:143], a[64:67], v[252:255], a[140:143]\n\t"
"s_waitcnt lgkmcnt(4)\n\t"
"v_mfma_f32_16x16x32_bf16 a[144:147], a[68:71], v[240:243], a[144:147]\n\t"
"ds_read_b64_tr_b16 a[64:65], v24 offset:12288\n\t"
"ds_read_b64_tr_b16 a[66:67], v24 offset:17408\n\t"
"v_mfma_f32_16x16x32_bf16 a[148:151], a[68:71], v[244:247], a[148:151]\n\t"
"v_mfma_f32_16x16x32_bf16 a[152:155], a[68:71], v[248:251], a[152:155]\n\t"
"buffer_load_dwordx4 v[212:215], v28, s[44:47], s81 offen offset:1024 sc1\n\t"
"v_mfma_f32_16x16x32_bf16 a[156:159], a[68:71], v[252:255], a[156:159]\n\t"
"s_waitcnt lgkmcnt(4)\n\t"
"v_mfma_f32_16x16x32_bf16 a[160:163], a[72:75], v[240:243], a[160:163]\n\t"
"ds_read_b64_tr_b16 a[68:69], v25 offset:12288\n\t"
"ds_read_b64_tr_b16 a[70:71], v25 offset:17408\n\t"
"v_mfma_f32_16x16x32_bf16 a[164:167], a[72:75], v[244:247], a[164:167]\n\t"
"v_mfma_f32_16x16x32_bf16 a[168:171], a[72:75], v[248:251], a[168:171]\n\t"
"buffer_load_dwordx4 v[216:219], v28, s[44:47], s81 offen offset:2048 sc1\n\t"
"v_mfma_f32_16x16x32_bf16 a[172:175], a[72:75], v[252:255], a[172:175]\n\t"
"s_waitcnt lgkmcnt(4)\n\t"
"v_mfma_f32_16x16x32_bf16 a[176:179], a[76:79], v[240:243], a[176:179]\n\t"
"ds_read_b64_tr_b16 a[72:73], v26 offset:12288\n\t"
"ds_read_b64_tr_b16 a[74:75], v26 offset:17408\n\t"
"v_mfma_f32_16x16x32_bf16 a[180:183], a[76:79], v[244:247], a[180:183]\n\t"
"v_mfma_f32_16x16x32_bf16 a[184:187], a[76:79], v[248:251], a[184:187]\n\t"
"buffer_load_dwordx4 v[220:223], v28, s[44:47], s81 offen offset:3072 sc1\n\t"
"v_mfma_f32_16x16x32_bf16 a[188:191], a[76:79], v[252:255], a[188:191]\n\t"
"s_waitcnt lgkmcnt(4)\n\t"
"v_mfma_f32_16x16x32_bf16 a[192:195], a[64:67], v[240:243], a[192:195]\n\t"
"ds_read_b64_tr_b16 a[76:77], v27 offset:10240\n\t"
"ds_read_b64_tr_b16 a[78:79], v27 offset:15360\n\t"
"v_mfma_f32_16x16x32_bf16 a[196:199], a[64:67], v[244:247], a[196:199]\n\t"
"v_mfma_f32_16x16x32_bf16 a[200:203], a[64:67], v[248:251], a[200:203]\n\t"
"v_mfma_f32_16x16x32_bf16 a[204:207], a[64:67], v[252:255], a[204:207]\n\t"
"s_waitcnt lgkmcnt(4)\n\t"
"s_add_u32 m0, s87, 40960\n\t"
"s_nop 0\n\t"
"buffer_load_dwordx4 v20, s[40:43], s72 offen offset:0 lds\n\t"
"v_mfma_f32_16x16x32_bf16 a[208:211], a[68:71], v[240:243], a[208:211]\n\t"
"s_waitcnt vmcnt(14) lgkmcnt(0)\n\t"
"s_barrier\n\t"
"ds_read_b64_tr_b16 a[64:65], v23 offset:20480\n\t"
"ds_read_b64_tr_b16 a[66:67], v23 offset:25600\n\t"
"v_mfma_f32_16x16x32_bf16 a[212:215], a[68:71], v[244:247], a[212:215]\n\t"
"v_mfma_f32_16x16x32_bf16 a[216:219], a[68:71], v[248:251], a[216:219]\n\t"
"v_mfma_f32_16x16x32_bf16 a[220:223], a[68:71], v[252:255], a[220:223]\n\t"
"s_add_u32 m0, s87, 41984\n\t"
"s_nop 0\n\t"
"buffer_load_dwordx4 v21, s[40:43], s72 offen offset:0 lds\n\t"
"v_mfma_f32_16x16x32_bf16 a[224:227], a[72:75], v[240:243], a[224:227]\n\t"
"ds_read_b64_tr_b16 a[68:69], v24 offset:20480\n\t"
"ds_read_b64_tr_b16 a[70:71], v24 offset:25600\n\t"
"v_mfma_f32_16x16x32_bf16 a[228:231], a[72:75], v[244:247], a[228:231]\n\t"
"v_mfma_f32_16x16x32_bf16 a[232:235], a[72:75], v[248:251], a[232:235]\n\t"
"v_mfma_f32_16x16x32_bf16 a[236:239], a[72:75], v[252:255], a[236:239]\n\t"
"s_add_u32 m0, s87, 42880\n\t"
"s_nop 0\n\t"
"buffer_load_dwordx4 v20, s[40:43], s72 offen offset:128 lds\n\t"
"v_mfma_f32_16x16x32_bf16 a[240:243], a[76:79], v[240:243], a[240:243]\n\t"
"ds_read_b64_tr_b16 a[72:73], v25 offset:20480\n\t"
"ds_read_b64_tr_b16 a[74:75], v25 offset:25600\n\t"
"v_mfma_f32_16x16x32_bf16 a[244:247], a[76:79], v[244:247], a[244:247]\n\t"
"v_mfma_f32_16x16x32_bf16 a[248:251], a[76:79], v[248:251], a[248:251]\n\t"
"v_mfma_f32_16x16x32_bf16 a[252:255], a[76:79], v[252:255], a[252:255]\n\t"
"s_waitcnt lgkmcnt(4)\n\t"
"s_add_u32 m0, s87, 43904\n\t"
"s_nop 0\n\t"
"buffer_load_dwordx4 v21, s[40:43], s72 offen offset:128 lds\n\t"
"v_mfma_f32_16x16x32_bf16 a[112:115], a[64:67], v[112:115], a[112:115]\n\t"
"ds_read_b64_tr_b16 a[76:77], v26 offset:20480\n\t"
"ds_read_b64_tr_b16 a[78:79], v26 offset:25600\n\t"
"v_mfma_f32_16x16x32_bf16 a[116:119], a[64:67], v[116:119], a[116:119]\n\t"
"v_mfma_f32_16x16x32_bf16 a[120:123], a[64:67], v[120:123], a[120:123]\n\t"
"v_mfma_f32_16x16x32_bf16 a[124:127], a[64:67], v[124:127], a[124:127]\n\t"
"s_waitcnt lgkmcnt(4)\n\t"
"s_add_u32 m0, s87, 45056\n\t"
"s_nop 0\n\t"
"buffer_load_dwordx4 v22, s[40:43], s72 offen lds\n\t"
"v_mfma_f32_16x16x32_bf16 a[128:131], a[68:71], v[112:115], a[128:131]\n\t"
"ds_read_b64_tr_b16 a[64:65], v23 offset:22528\n\t"
"ds_read_b64_tr_b16 a[66:67], v23 offset:27648\n\t"
"v_mfma_f32_16x16x32_bf16 a[132:135], a[68:71], v[116:119], a[132:135]\n\t"
"v_mfma_f32_16x16x32_bf16 a[136:139], a[68:71], v[120:123], a[136:139]\n\t"
"v_mfma_f32_16x16x32_bf16 a[140:143], a[68:71], v[124:127], a[140:143]\n\t"
"s_waitcnt lgkmcnt(4)\n\t"
"s_add_u32 m0, s87, 20480\n\t"
"s_nop 0\n\t"
"buffer_load_dwordx4 v20, s[40:43], s73 offen offset:0 lds\n\t"
"v_mfma_f32_16x16x32_bf16 a[144:147], a[72:75], v[112:115], a[144:147]\n\t"
"ds_read_b64_tr_b16 a[68:69], v24 offset:22528\n\t"
"ds_read_b64_tr_b16 a[70:71], v24 offset:27648\n\t"
"v_mfma_f32_16x16x32_bf16 a[148:151], a[72:75], v[116:119], a[148:151]\n\t"
"v_mfma_f32_16x16x32_bf16 a[152:155], a[72:75], v[120:123], a[152:155]\n\t"
"v_mfma_f32_16x16x32_bf16 a[156:159], a[72:75], v[124:127], a[156:159]\n\t"
"s_waitcnt lgkmcnt(4)\n\t"
"s_add_u32 m0, s87, 21504\n\t"
"s_nop 0\n\t"
"buffer_load_dwordx4 v21, s[40:43], s73 offen offset:0 lds\n\t"
"v_mfma_f32_16x16x32_bf16 a[160:163], a[76:79], v[112:115], a[160:163]\n\t"
"ds_read_b64_tr_b16 a[72:73], v25 offset:22528\n\t"
"ds_read_b64_tr_b16 a[74:75], v25 offset:27648\n\t"
"v_mfma_f32_16x16x32_bf16 a[164:167], a[76:79], v[116:119], a[164:167]\n\t"
"v_mfma_f32_16x16x32_bf16 a[168:171], a[76:79], v[120:123], a[168:171]\n\t"
"v_mfma_f32_16x16x32_bf16 a[172:175], a[76:79], v[124:127], a[172:175]\n\t"
"s_waitcnt lgkmcnt(4)\n\t"
"s_add_u32 m0, s87, 22400\n\t"
"s_nop 0\n\t"
"buffer_load_dwordx4 v20, s[40:43], s73 offen offset:128 lds\n\t"
"v_mfma_f32_16x16x32_bf16 a[176:179], a[64:67], v[112:115], a[176:179]\n\t"
"ds_read_b64_tr_b16 a[76:77], v26 offset:22528\n\t"
"ds_read_b64_tr_b16 a[78:79], v26 offset:27648\n\t"
"v_mfma_f32_16x16x32_bf16 a[180:183], a[64:67], v[116:119], a[180:183]\n\t"
"v_mfma_f32_16x16x32_bf16 a[184:187], a[64:67], v[120:123], a[184:187]\n\t"
"v_mfma_f32_16x16x32_bf16 a[188:191], a[64:67], v[124:127], a[188:191]\n\t"
"s_waitcnt lgkmcnt(4)\n\t"
"s_add_u32 m0, s87, 23424\n\t"
"s_nop 0\n\t"
"buffer_load_dwordx4 v21, s[40:43], s73 offen offset:128 lds\n\t"
"v_mfma_f32_16x16x32_bf16 a[192:195], a[68:71], v[112:115], a[192:195]\n\t"
"ds_read_b64_tr_b16 a[64:65], v27 offset:20480\n\t"
"ds_read_b64_tr_b16 a[66:67], v27 offset:25600\n\t"
"v_mfma_f32_16x16x32_bf16 a[196:199], a[68:71], v[116:119], a[196:199]\n\t"
"v_mfma_f32_16x16x32_bf16 a[200:203], a[68:71], v[120:123], a[200:203]\n\t"
"v_mfma_f32_16x16x32_bf16 a[204:207], a[68:71], v[124:127], a[204:207]\n\t"
"s_waitcnt lgkmcnt(4)\n\t"
"s_add_u32 m0, s87, 24576\n\t"
"s_nop 0\n\t"
"buffer_load_dwordx4 v22, s[40:43], s73 offen lds\n\t"
"v_mfma_f32_16x16x32_bf16 a[208:211], a[72:75], v[112:115], a[208:211]\n\t"
"ds_read_b64_tr_b16 a[68:69], v23 offset:30720\n\t"
"ds_read_b64_tr_b16 a[70:71], v23 offset:35840\n\t"
"v_mfma_f32_16x16x32_bf16 a[212:215], a[72:75], v[116:119], a[212:215]\n\t"
"v_mfma_f32_16x16x32_bf16 a[216:219], a[72:75], v[120:123], a[216:219]\n\t"
"v_mfma_f32_16x16x32_bf16 a[220:223], a[72:75], v[124:127], a[220:223]\n\t"
"s_waitcnt lgkmcnt(4)\n\t"
"buffer_load_dword v110, v46, s[44:47], 0 offen sc1\n\t"
"v_mfma_f32_16x16x32_bf16 a[224:227], a[76:79], v[112:115], a[224:227]\n\t"
"ds_read_b64_tr_b16 a[72:73], v24 offset:30720\n\t"
"ds_read_b64_tr_b16 a[74:75], v24 offset:35840\n\t"
"v_mfma_f32_16x16x32_bf16 a[228:231], a[76:79], v[116:119], a[228:231]\n\t"
"v_mfma_f32_16x16x32_bf16 a[232:235], a[76:79], v[120:123], a[232:235]\n\t"
"s_add_u32 m0, s87, 61440\n\t"
"s_nop 0\n\t"
"buffer_load_dwordx4 v20, s[40:43], s74 offen offset:0 lds\n\t"
"v_mfma_f32_16x16x32_bf16 a[236:239], a[76:79], v[124:127], a[236:239]\n\t"
"s_waitcnt lgkmcnt(4)\n\t"
"v_mfma_f32_16x16x32_bf16 a[240:243], a[64:67], v[112:115], a[240:243]\n\t"
"ds_read_b64_tr_b16 a[76:77], v25 offset:30720\n\t"
"ds_read_b64_tr_b16 a[78:79], v25 offset:35840\n\t"
"v_mfma_f32_16x16x32_bf16 a[244:247], a[64:67], v[116:119], a[244:247]\n\t"
"v_mfma_f32_16x16x32_bf16 a[248:251], a[64:67], v[120:123], a[248:251]\n\t"
"s_add_u32 m0, s87, 62464\n\t"
"s_nop 0\n\t"
"buffer_load_dwordx4 v21, s[40:43], s74 offen offset:0 lds\n\t"
"v_mfma_f32_16x16x32_bf16 a[252:255], a[64:67], v[124:127], a[252:255]\n\t"
"s_waitcnt lgkmcnt(4)\n\t"
"v_mfma_f32_16x16x32_bf16 a[112:115], a[68:71], v[144:147], a[112:115]\n\t"
"ds_read_b64_tr_b16 a[64:65], v26 offset:30720\n\t"
"ds_read_b64_tr_b16 a[66:67], v26 offset:35840\n\t"
"v_mfma_f32_16x16x32_bf16 a[116:119], a[68:71], v[148:151], a[116:119]\n\t"
"v_mfma_f32_16x16x32_bf16 a[120:123], a[68:71], v[152:155], a[120:123]\n\t"
"s_add_u32 m0, s87, 63360\n\t"
"s_nop 0\n\t"
"buffer_load_dwordx4 v20, s[40:43], s74 offen offset:128 lds\n\t"
"v_mfma_f32_16x16x32_bf16 a[124:127], a[68:71], v[156:159], a[124:127]\n\t"
"s_waitcnt lgkmcnt(4)\n\t"
"v_mfma_f32_16x16x32_bf16 a[128:131], a[72:75], v[144:147], a[128:131]\n\t"
"ds_read_b64_tr_b16 a[68:69], v23 offset:32768\n\t"
"ds_read_b64_tr_b16 a[70:71], v23 offset:37888\n\t"
"v_mfma_f32_16x16x32_bf16 a[132:135], a[72:75], v[148:151], a[132:135]\n\t"
"v_mfma_f32_16x16x32_bf16 a[136:139], a[72:75], v[152:155], a[136:139]\n\t"
"s_add_u32 m0, s87, 64384\n\t"
"s_nop 0\n\t"
"buffer_load_dwordx4 v21, s[40:43], s74 offen offset:128 lds\n\t"
"v_mfma_f32_16x16x32_bf16 a[140:143], a[72:75], v[156:159], a[140:143]\n\t"
"s_waitcnt lgkmcnt(4)\n\t"
"v_mfma_f32_16x16x32_bf16 a[144:147], a[76:79], v[144:147], a[144:147]\n\t"
"ds_read_b64_tr_b16 a[72:73], v24 offset:32768\n\t"
"ds_read_b64_tr_b16 a[74:75], v24 offset:37888\n\t"
"v_mfma_f32_16x16x32_bf16 a[148:151], a[76:79], v[148:151], a[148:151]\n\t"
"v_mfma_f32_16x16x32_bf16 a[152:155], a[76:79], v[152:155], a[152:155]\n\t"
"s_add_u32 m0, s87, 65536\n\t"
"s_nop 0\n\t"
"buffer_load_dwordx4 v22, s[40:43], s74 offen lds\n\t"
"v_mfma_f32_16x16x32_bf16 a[156:159], a[76:79], v[156:159], a[156:159]\n\t"
"s_waitcnt lgkmcnt(4)\n\t"
"v_mfma_f32_16x16x32_bf16 a[160:163], a[64:67], v[144:147], a[160:163]\n\t"
"ds_read_b64_tr_b16 a[76:77], v25 offset:32768\n\t"
"ds_read_b64_tr_b16 a[78:79], v25 offset:37888\n\t"
"v_mfma_f32_16x16x32_bf16 a[164:167], a[64:67], v[148:151], a[164:167]\n\t"
"v_mfma_f32_16x16x32_bf16 a[168:171], a[64:67], v[152:155], a[168:171]\n\t"
"v_mfma_f32_16x16x32_bf16 a[172:175], a[64:67], v[156:159], a[172:175]\n\t"
"s_waitcnt lgkmcnt(4)\n\t"
"v_mfma_f32_16x16x32_bf16 a[176:179], a[68:71], v[144:147], a[176:179]\n\t"
"ds_read_b64_tr_b16 a[64:65], v26 offset:32768\n\t"
"ds_read_b64_tr_b16 a[66:67], v26 offset:37888\n\t"
"v_mfma_f32_16x16x32_bf16 a[180:183], a[68:71], v[148:151], a[180:183]\n\t"
"v_mfma_f32_16x16x32_bf16 a[184:187], a[68:71], v[152:155], a[184:187]\n\t"
"v_mfma_f32_16x16x32_bf16 a[188:191], a[68:71], v[156:159], a[188:191]\n\t"
"s_waitcnt lgkmcnt(4)\n\t"
"v_mfma_f32_16x16x32_bf16 a[192:195], a[72:75], v[144:147], a[192:195]\n\t"
"ds_read_b64_tr_b16 a[68:69], v27 offset:30720\n\t"
"ds_read_b64_tr_b16 a[70:71], v27 offset:35840\n\t"
"v_mfma_f32_16x16x32_bf16 a[196:199], a[72:75], v[148:151], a[196:199]\n\t"
"v_mfma_f32_16x16x32_bf16 a[200:203], a[72:75], v[152:155], a[200:203]\n\t"
"v_mfma_f32_16x16x32_bf16 a[204:207], a[72:75], v[156:159], a[204:207]\n\t"
"s_waitcnt lgkmcnt(4)\n\t"
"v_mfma_f32_16x16x32_bf16 a[208:211], a[76:79], v[144:147], a[208:211]\n\t"
"v_subrev_u32 v23, 61440, v23\n\t"
"v_subrev_u32 v24, 61440, v24\n\t"
"v_subrev_u32 v25, 61440, v25\n\t"
"v_subrev_u32 v26, 61440, v26\n\t"
"v_subrev_u32 v27, 61440, v27\n\t"
"ds_read_b64_tr_b16 a[72:73], v23 offset:0\n\t"
"ds_read_b64_tr_b16 a[74:75], v23 offset:5120\n\t"
"v_mfma_f32_16x16x32_bf16 a[212:215], a[76:79], v[148:151], a[212:215]\n\t"
"v_mfma_f32_16x16x32_bf16 a[216:219], a[76:79], v[152:155], a[216:219]\n\t"
"v_mfma_f32_16x16x32_bf16 a[220:223], a[76:79], v[156:159], a[220:223]\n\t"
"s_waitcnt lgkmcnt(4)\n\t"
"s_mov_b32 s92, 0x700\n\t"
"s_mov_b32 s93, 0\n\t"
"s_waitcnt vmcnt(5)\n\t"
"v_sub_u32 v110, v110, v48\n\t"
"v_subrev_u32 v110, 4, v110\n\t"
"v_cmp_lt_u32 vcc, 0, v110\n\t"
"s_nop 2\n\t"
"s_and_b64 vcc, vcc, s[92:93]\n\t"
"s_cbranch_scc0 LP5_ok%=\n\t"
"s_mov_b32 s94, 0\n\t"
"LP5_spin%=:\n\t"
"buffer_load_dword v37, v46, s[44:47], s89 offen sc1\n\t"
"s_waitcnt vmcnt(0)\n\t"
"v_cndmask_b32 v111, 1, 4, s[10:11]\n\t"
"v_add_u32 v111, v111, v37\n\t"
"buffer_store_dword v111, v47, s[44:47], 0 offen sc1\n\t"
"buffer_load_dword v110, v46, s[44:47], 0 offen sc1\n\t"
"s_waitcnt vmcnt(0)\n\t"
"s_add_u32 s94, s94, 1\n\t"
"s_cmp_gt_u32 s94, 0x4000\n\t"
"s_cbranch_scc1 LP5_out%=\n\t"
"v_sub_u32 v110, v110, v48\n\t"
"v_subrev_u32 v110, 4, v110\n\t"
"v_cmp_lt_u32 vcc, 0, v110\n\t"
"s_nop 2\n\t"
"s_and_b64 vcc, vcc, s[92:93]\n\t"
"s_cbranch_scc1 LP5_spin%=\n\t"
"LP5_out%=:\n\t"
"LP5_ok%=:\n\t"
"v_mfma_f32_16x16x32_bf16 a[224:227], a[64:67], v[144:147], a[224:227]\n\t"
"ds_read_b64_tr_b16 a[76:77], v24 offset:0\n\t"
"ds_read_b64_tr_b16 a[78:79], v24 offset:5120\n\t"
"v_mfma_f32_16x16x32_bf16 a[228:231], a[64:67], v[148:151], a[228:231]\n\t"
"v_mfma_f32_16x16x32_bf16 a[232:235], a[64:67], v[152:155], a[232:235]\n\t"
"buffer_load_dwordx4 a[0:3], v29, s[44:47], s79 offen offset:0 sc1\n\t"
"v_mfma_f32_16x16x32_bf16 a[236:239], a[64:67], v[156:159], a[236:239]\n\t"
"s_waitcnt lgkmcnt(4)\n\t"
"v_mfma_f32_16x16x32_bf16 a[240:243], a[68:71], v[144:147], a[240:243]\n\t"
"ds_read_b64_tr_b16 a[64:65], v25 offset:0\n\t"
"ds_read_b64_tr_b16 a[66:67], v25 offset:5120\n\t"
"v_mfma_f32_16x16x32_bf16 a[244:247], a[68:71], v[148:151], a[244:247]\n\t"
"v_mfma_f32_16x16x32_bf16 a[248:251], a[68:71], v[152:155], a[248:251]\n\t"
"buffer_load_dwordx4 a[4:7], v29, s[44:47], s79 offen offset:1024 sc1\n\t"
"v_mfma_f32_16x16x32_bf16 a[252:255], a[68:71], v[156:159], a[252:255]\n\t"
"s_waitcnt lgkmcnt(4)\n\t"
"v_mfma_f32_16x16x32_bf16 a[112:115], a[72:75], v[176:179], a[112:115]\n\t"
"ds_read_b64_tr_b16 a[68:69], v26 offset:0\n\t"
"ds_read_b64_tr_b16 a[70:71], v26 offset:5120\n\t"
"v_mfma_f32_16x16x32_bf16 a[116:119], a[72:75], v[180:183], a[116:119]\n\t"
"v_mfma_f32_16x16x32_bf16 a[120:123], a[72:75], v[184:187], a[120:123]\n\t"
"buffer_load_dwordx4 a[8:11], v29, s[44:47], s79 offen offset:2048 sc1\n\t"
"v_mfma_f32_16x16x32_bf16 a[124:127], a[72:75], v[188:191], a[124:127]\n\t"
"s_waitcnt lgkmcnt(4)\n\t"
"v_mfma_f32_16x16x32_bf16 a[128:131], a[76:79], v[176:179], a[128:131]\n\t"
"ds_read_b64_tr_b16 a[72:73], v23 offset:2048\n\t"
"ds_read_b64_tr_b16 a[74:75], v23 offset:7168\n\t"
"v_mfma_f32_16x16x32_bf16 a[132:135], a[76:79], v[180:183], a[132:135]\n\t"
"v_mfma_f32_16x16x32_bf16 a[136:139], a[76:79], v[184:187], a[136:139]\n\t"
"buffer_load_dwordx4 a[12:15], v29, s[44:47], s79 offen offset:3072 sc1\n\t"
"v_mfma_f32_16x16x32_bf16 a[140:143], a[76:79], v[188:191], a[140:143]\n\t"
"s_waitcnt lgkmcnt(4)\n\t"
"v_mfma_f32_16x16x32_bf16 a[144:147], a[64:67], v[176:179], a[144:147]\n\t"
"ds_read_b64_tr_b16 a[76:77], v24 offset:2048\n\t"
"ds_read_b64_tr_b16 a[78:79], v24 offset:7168\n\t"
"v_mfma_f32_16x16x32_bf16 a[148:151], a[64:67], v[180:183], a[148:151]\n\t"
"v_mfma_f32_16x16x32_bf16 a[152:155], a[64:67], v[184:187], a[152:155]\n\t"
"buffer_load_dwordx4 a[16:19], v29, s[44:47], s80 offen offset:0 sc1\n\t"
"v_mfma_f32_16x16x32_bf16 a[156:159], a[64:67], v[188:191], a[156:159]\n\t"
"s_waitcnt lgkmcnt(4)\n\t"
"v_mfma_f32_16x16x32_bf16 a[160:163], a[68:71], v[176:179], a[160:163]\n\t"
"ds_read_b64_tr_b16 a[64:65], v25 offset:2048\n\t"
"ds_read_b64_tr_b16 a[66:67], v25 offset:7168\n\t"
"v_mfma_f32_16x16x32_bf16 a[164:167], a[68:71], v[180:183], a[164:167]\n\t"
"v_mfma_f32_16x16x32_bf16 a[168:171], a[68:71], v[184:187], a[168:171]\n\t"
"buffer_load_dwordx4 a[20:23], v29, s[44:47], s80 offen offset:1024 sc1\n\t"
"v_mfma_f32_16x16x32_bf16 a[172:175], a[68:71], v[188:191], a[172:175]\n\t"
"s_waitcnt lgkmcnt(4)\n\t"
"v_mfma_f32_16x16x32_bf16 a[176:179], a[72:75], v[176:179], a[176:179]\n\t"
"ds_read_b64_tr_b16 a[68:69], v26 offset:2048\n\t"
"ds_read_b64_tr_b16 a[70:71], v26 offset:7168\n\t"
"v_mfma_f32_16x16x32_bf16 a[180:183], a[72:75], v[180:183], a[180:183]\n\t"
"v_mfma_f32_16x16x32_bf16 a[184:187], a[72:75], v[184:187], a[184:187]\n\t"
"buffer_load_dwordx4 a[24:27], v29, s[44:47], s80 offen offset:2048 sc1\n\t"
"v_mfma_f32_16x16x32_bf16 a[188:191], a[72:75], v[188:191], a[188:191]\n\t"
"s_waitcnt lgkmcnt(4)\n\t"
"v_mfma_f32_16x16x32_bf16 a[192:195], a[76:79], v[176:179], a[192:195]\n\t"
"ds_read_b64_tr_b16 a[72:73], v27 offset:0\n\t"
"ds_read_b64_tr_b16 a[74:75], v27 offset:5120\n\t"
"v_mfma_f32_16x16x32_bf16 a[196:199], a[76:79], v[180:183], a[196:199]\n\t"
"v_mfma_f32_16x16x32_bf16 a[200:203], a[76:79], v[184:187], a[200:203]\n\t"
"buffer_load_dwordx4 a[28:31], v29, s[44:47], s80 offen offset:3072 sc1\n\t"
"v_mfma_f32_16x16x32_bf16 a[204:207], a[76:79], v[188:191], a[204:207]\n\t"
"s_waitcnt lgkmcnt(4)\n\t"
"v_mfma_f32_16x16x32_bf16 a[208:211], a[64:67], v[176:179], a[208:211]\n\t"
"ds_read_b64_tr_b16 a[76:77], v23 offset:10240\n\t"
"ds_read_b64_tr_b16 a[78:79], v23 offset:15360\n\t"
"v_mfma_f32_16x16x32_bf16 a[212:215], a[64:67], v[180:183], a[212:215]\n\t"
"v_mfma_f32_16x16x32_bf16 a[216:219], a[64:67], v[184:187], a[216:219]\n\t"
"buffer_load_dwordx4 a[32:35], v29, s[44:47], s81 offen offset:0 sc1\n\t"
"v_mfma_f32_16x16x32_bf16 a[220:223], a[64:67], v[188:191], a[220:223]\n\t"
"s_waitcnt lgkmcnt(4)\n\t"
"v_mfma_f32_16x16x32_bf16 a[224:227], a[68:71], v[176:179], a[224:227]\n\t"
"ds_read_b64_tr_b16 a[64:65], v24 offset:10240\n\t"
"ds_read_b64_tr_b16 a[66:67], v24 offset:15360\n\t"
"v_mfma_f32_16x16x32_bf16 a[228:231], a[68:71], v[180:183], a[228:231]\n\t"
"v_mfma_f32_16x16x32_bf16 a[232:235], a[68:71], v[184:187], a[232:235]\n\t"
"buffer_load_dwordx4 a[36:39], v29, s[44:47], s81 offen offset:1024 sc1\n\t"
"v_mfma_f32_16x16x32_bf16 a[236:239], a[68:71], v[188:191], a[236:239]\n\t"
"s_waitcnt lgkmcnt(4)\n\t"
"v_mfma_f32_16x16x32_bf16 a[240:243], a[72:75], v[176:179], a[240:243]\n\t"
"ds_read_b64_tr_b16 a[68:69], v25 offset:10240\n\t"
"ds_read_b64_tr_b16 a[70:71], v25 offset:15360\n\t"
"v_mfma_f32_16x16x32_bf16 a[244:247], a[72:75], v[180:183], a[244:247]\n\t"
"v_mfma_f32_16x16x32_bf16 a[248:251], a[72:75], v[184:187], a[248:251]\n\t"
"buffer_load_dwordx4 a[40:43], v29, s[44:47], s81 offen offset:2048 sc1\n\t"
"v_mfma_f32_16x16x32_bf16 a[252:255], a[72:75], v[188:191], a[252:255]\n\t"
"s_waitcnt lgkmcnt(4)\n\t"
"v_mfma_f32_16x16x32_bf16 a[112:115], a[76:79], v[224:227], a[112:115]\n\t"
"ds_read_b64_tr_b16 a[72:73], v26 offset:10240\n\t"
"ds_read_b64_tr_b16 a[74:75], v26 offset:15360\n\t"
"v_mfma_f32_16x16x32_bf16 a[116:119], a[76:79], v[228:231], a[116:119]\n\t"
"v_mfma_f32_16x16x32_bf16 a[120:123], a[76:79], v[232:235], a[120:123]\n\t"
"buffer_load_dwordx4 a[44:47], v29, s[44:47], s81 offen offset:3072 sc1\n\t"
"v_mfma_f32_16x16x32_bf16 a[124:127], a[76:79], v[236:239], a[124:127]\n\t"
"s_waitcnt lgkmcnt(4)\n\t"
"v_mfma_f32_16x16x32_bf16 a[128:131], a[64:67], v[224:227], a[128:131]\n\t"
"ds_read_b64_tr_b16 a[76:77], v23 offset:12288\n\t"
"ds_read_b64_tr_b16 a[78:79], v23 offset:17408\n\t"
"v_mfma_f32_16x16x32_bf16 a[132:135], a[64:67], v[228:231], a[132:135]\n\t"
"v_mfma_f32_16x16x32_bf16 a[136:139], a[64:67], v[232:235], a[136:139]\n\t"
"v_mfma_f32_16x16x32_bf16 a[140:143], a[64:67], v[236:239], a[140:143]\n\t"
"s_waitcnt lgkmcnt(4)\n\t"
"v_mfma_f32_16x16x32_bf16 a[144:147], a[68:71], v[224:227], a[144:147]\n\t"
"ds_read_b64_tr_b16 a[64:65], v24 offset:12288\n\t"
"ds_read_b64_tr_b16 a[66:67], v24 offset:17408\n\t"
"v_mfma_f32_16x16x32_bf16 a[148:151], a[68:71], v[228:231], a[148:151]\n\t"
"v_mfma_f32_16x16x32_bf16 a[152:155], a[68:71], v[232:235], a[152:155]\n\t"
"v_mfma_f32_16x16x32_bf16 a[156:159], a[68:71], v[236:239], a[156:159]\n\t"
"s_waitcnt lgkmcnt(4)\n\t"
"v_mfma_f32_16x16x32_bf16 a[160:163], a[72:75], v[224:227], a[160:163]\n\t"
"ds_read_b64_tr_b16 a[68:69], v25 offset:12288\n\t"
"ds_read_b64_tr_b16 a[70:71], v25 offset:17408\n\t"
"v_mfma_f32_16x16x32_bf16 a[164:167], a[72:75], v[228:231], a[164:167]\n\t"
"v_mfma_f32_16x16x32_bf16 a[168:171], a[72:75], v[232:235], a[168:171]\n\t"
"v_mfma_f32_16x16x32_bf16 a[172:175], a[72:75], v[236:239], a[172:175]\n\t"
"s_waitcnt lgkmcnt(4)\n\t"
"v_mfma_f32_16x16x32_bf16 a[176:179], a[76:79], v[224:227], a[176:179]\n\t"
"ds_read_b64_tr_b16 a[72:73], v26 offset:12288\n\t"
"ds_read_b64_tr_b16 a[74:75], v26 offset:17408\n\t"
"v_mfma_f32_16x16x32_bf16 a[180:183], a[76:79], v[228:231], a[180:183]\n\t"
"v_mfma_f32_16x16x32_bf16 a[184:187], a[76:79], v[232:235], a[184:187]\n\t"
"v_mfma_f32_16x16x32_bf16 a[188:191], a[76:79], v[236:239], a[188:191]\n\t"
"s_waitcnt lgkmcnt(4)\n\t"
"v_mfma_f32_16x16x32_bf16 a[192:195], a[64:67], v[224:227], a[192:195]\n\t"
"ds_read_b64_tr_b16 a[76:77], v27 offset:10240\n\t"
"ds_read_b64_tr_b16 a[78:79], v27 offset:15360\n\t"
"v_mfma_f32_16x16x32_bf16 a[196:199], a[64:67], v[228:231], a[196:199]\n\t"
"v_mfma_f32_16x16x32_bf16 a[200:203], a[64:67], v[232:235], a[200:203]\n\t"
"v_mfma_f32_16x16x32_bf16 a[204:207], a[64:67], v[236:239], a[204:207]\n\t"
"s_waitcnt lgkmcnt(4)\n\t"
"v_mfma_f32_16x16x32_bf16 a[208:211], a[68:71], v[224:227], a[208:211]\n\t"
"s_waitcnt vmcnt(12) lgkmcnt(0)\n\t"
"s_barrier\n\t"
"v_add_u32 v23, 20480, v23\n\t"
"v_add_u32 v24, 20480, v24\n\t"
"v_add_u32 v25, 20480, v25\n\t"
"v_add_u32 v26, 20480, v26\n\t"
"v_add_u32 v27, 20480, v27\n\t"
"ds_read_b64_tr_b16 a[64:65], v23 offset:20480\n\t"
"ds_read_b64_tr_b16 a[66:67], v23 offset:25600\n\t"
"v_mfma_f32_16x16x32_bf16 a[212:215], a[68:71], v[228:231], a[212:215]\n\t"
"s_add_u32 s82, s82, 20971520\n\t"
"v_mfma_f32_16x16x32_bf16 a[216:219], a[68:71], v[232:235], a[216:219]\n\t"
"s_add_u32 s83, s83, 20971520\n\t"
"v_mfma_f32_16x16x32_bf16 a[220:223], a[68:71], v[236:239], a[220:223]\n\t"
"s_add_u32 s84, s84, 20971520\n\t"
"v_mfma_f32_16x16x32_bf16 a[224:227], a[72:75], v[224:227], a[224:227]\n\t"
"ds_read_b64_tr_b16 a[68:69], v23 offset:0\n\t"
"ds_read_b64_tr_b16 a[70:71], v23 offset:5120\n\t"
"v_mfma_f32_16x16x32_bf16 a[228:231], a[72:75], v[228:231], a[228:231]\n\t"
"v_mfma_f32_16x16x32_bf16 a[232:235], a[72:75], v[232:235], a[232:235]\n\t"
"v_mfma_f32_16x16x32_bf16 a[236:239], a[72:75], v[236:239], a[236:239]\n\t"
"v_mfma_f32_16x16x32_bf16 a[240:243], a[76:79], v[224:227], a[240:243]\n\t"
"ds_read_b64_tr_b16 a[72:73], v23 offset:40960\n\t"
"ds_read_b64_tr_b16 a[74:75], v23 offset:46080\n\t"
"v_mfma_f32_16x16x32_bf16 a[244:247], a[76:79], v[228:231], a[244:247]\n\t"
"v_mfma_f32_16x16x32_bf16 a[248:251], a[76:79], v[232:235], a[248:251]\n\t"
"v_mfma_f32_16x16x32_bf16 a[252:255], a[76:79], v[236:239], a[252:255]\n\t"
"s_waitcnt lgkmcnt(4)\n\t"
"v_mfma_f32_16x16x32_bf16 a[112:115], a[64:67], v[128:131], a[112:115]\n\t"
"ds_read_b64_tr_b16 a[76:77], v23 offset:30720\n\t"
"ds_read_b64_tr_b16 a[78:79], v23 offset:35840\n\t"
"v_mfma_f32_16x16x32_bf16 a[116:119], a[64:67], v[132:135], a[116:119]\n\t"
"v_mfma_f32_16x16x32_bf16 a[120:123], a[64:67], v[136:139], a[120:123]\n\t"
"v_mfma_f32_16x16x32_bf16 a[124:127], a[64:67], v[140:143], a[124:127]\n\t"
"s_waitcnt lgkmcnt(4)\n\t"
"v_mfma_f32_16x16x32_bf16 a[112:115], a[68:71], v[160:163], a[112:115]\n\t"
"ds_read_b64_tr_b16 a[64:65], v23 offset:10240\n\t"
"ds_read_b64_tr_b16 a[66:67], v23 offset:15360\n\t"
"v_mfma_f32_16x16x32_bf16 a[116:119], a[68:71], v[164:167], a[116:119]\n\t"
"v_mfma_f32_16x16x32_bf16 a[120:123], a[68:71], v[168:171], a[120:123]\n\t"
"v_mfma_f32_16x16x32_bf16 a[124:127], a[68:71], v[172:175], a[124:127]\n\t"
"s_waitcnt lgkmcnt(4)\n\t"
"v_mfma_f32_16x16x32_bf16 a[112:115], a[72:75], v[208:211], a[112:115]\n\t"
"ds_read_b64_tr_b16 a[68:69], v23 offset:51200\n\t"
"ds_read_b64_tr_b16 a[70:71], v23 offset:56320\n\t"
"v_mfma_f32_16x16x32_bf16 a[116:119], a[72:75], v[212:215], a[116:119]\n\t"
"v_mfma_f32_16x16x32_bf16 a[120:123], a[72:75], v[216:219], a[120:123]\n\t"
"v_mfma_f32_16x16x32_bf16 a[124:127], a[72:75], v[220:223], a[124:127]\n\t"
"s_waitcnt lgkmcnt(4)\n\t"
"s_waitcnt vmcnt(8)\n\t"
"v_mfma_f32_16x16x32_bf16 a[112:115], a[76:79], a[0:3], a[112:115]\n\t"
"ds_read_b64_tr_b16 a[72:73], v24 offset:20480\n\t"
"ds_read_b64_tr_b16 a[74:75], v24 offset:25600\n\t"
"v_mfma_f32_16x16x32_bf16 a[116:119], a[76:79], a[4:7], a[116:119]\n\t"
"v_mfma_f32_16x16x32_bf16 a[120:123], a[76:79], a[8:11], a[120:123]\n\t"
"v_mfma_f32_16x16x32_bf16 a[124:127], a[76:79], a[12:15], a[124:127]\n\t"
"s_waitcnt lgkmcnt(4)\n\t"
"s_waitcnt vmcnt(4)\n\t"
"v_mfma_f32_16x16x32_bf16 a[112:115], a[64:67], a[16:19], a[112:115]\n\t"
"ds_read_b64_tr_b16 a[76:77], v24 offset:0\n\t"
"ds_read_b64_tr_b16 a[78:79], v24 offset:5120\n\t"
"v_mfma_f32_16x16x32_bf16 a[116:119], a[64:67], a[20:23], a[116:119]\n\t"
"v_mfma_f32_16x16x32_bf16 a[120:123], a[64:67], a[24:27], a[120:123]\n\t"
"v_mfma_f32_16x16x32_bf16 a[124:127], a[64:67], a[28:31], a[124:127]\n\t"
"s_waitcnt lgkmcnt(4)\n\t"
"s_waitcnt vmcnt(0)\n\t"
"v_mfma_f32_16x16x32_bf16 a[112:115], a[68:71], a[32:35], a[112:115]\n\t"
"ds_read_b64_tr_b16 a[64:65], v24 offset:40960\n\t"
"ds_read_b64_tr_b16 a[66:67], v24 offset:46080\n\t"
"v_mfma_f32_16x16x32_bf16 a[116:119], a[68:71], a[36:39], a[116:119]\n\t"
"v_mfma_f32_16x16x32_bf16 a[120:123], a[68:71], a[40:43], a[120:123]\n\t"
"v_mfma_f32_16x16x32_bf16 a[124:127], a[68:71], a[44:47], a[124:127]\n\t"
"s_waitcnt lgkmcnt(4)\n\t"
"v_mfma_f32_16x16x32_bf16 a[128:131], a[72:75], v[128:131], a[128:131]\n\t"
"ds_read_b64_tr_b16 a[68:69], v24 offset:30720\n\t"
"ds_read_b64_tr_b16 a[70:71], v24 offset:35840\n\t"
"v_mfma_f32_16x16x32_bf16 a[132:135], a[72:75], v[132:135], a[132:135]\n\t"
"v_mfma_f32_16x16x32_bf16 a[136:139], a[72:75], v[136:139], a[136:139]\n\t"
"v_mfma_f32_16x16x32_bf16 a[140:143], a[72:75], v[140:143], a[140:143]\n\t"
"s_waitcnt lgkmcnt(4)\n\t"
"v_mfma_f32_16x16x32_bf16 a[128:131], a[76:79], v[160:163], a[128:131]\n\t"
"ds_read_b64_tr_b16 a[72:73], v24 offset:10240\n\t"
"ds_read_b64_tr_b16 a[74:75], v24 offset:15360\n\t"
"v_mfma_f32_16x16x32_bf16 a[132:135], a[76:79], v[164:167], a[132:135]\n\t"
"v_mfma_f32_16x16x32_bf16 a[136:139], a[76:79], v[168:171], a[136:139]\n\t"
"v_mfma_f32_16x16x32_bf16 a[140:143], a[76:79], v[172:175], a[140:143]\n\t"
"s_waitcnt lgkmcnt(4)\n\t"
"v_mfma_f32_16x16x32_bf16 a[128:131], a[64:67], v[208:211], a[128:131]\n\t"
"ds_read_b64_tr_b16 a[76:77], v24 offset:51200\n\t"
"ds_read_b64_tr_b16 a[78:79], v24 offset:56320\n\t"
"v_mfma_f32_16x16x32_bf16 a[132:135], a[64:67], v[212:215], a[132:135]\n\t"
"v_mfma_f32_16x16x32_bf16 a[136:139], a[64:67], v[216:219], a[136:139]\n\t"
"v_mfma_f32_16x16x32_bf16 a[140:143], a[64:67], v[220:223], a[140:143]\n\t"
"s_waitcnt lgkmcnt(4)\n\t"
"v_mfma_f32_16x16x32_bf16 a[128:131], a[68:71], a[0:3], a[128:131]\n\t"
"ds_read_b64_tr_b16 a[64:65], v25 offset:20480\n\t"
"ds_read_b64_tr_b16 a[66:67], v25 offset:25600\n\t"
"v_mfma_f32_16x16x32_bf16 a[132:135], a[68:71], a[4:7], a[132:135]\n\t"
"v_mfma_f32_16x16x32_bf16 a[136:139], a[68:71], a[8:11], a[136:139]\n\t"
"v_mfma_f32_16x16x32_bf16 a[140:143], a[68:71], a[12:15], a[140:143]\n\t"
"s_waitcnt lgkmcnt(4)\n\t"
"v_mfma_f32_16x16x32_bf16 a[128:131], a[72:75], a[16:19], a[128:131]\n\t"
"ds_read_b64_tr_b16 a[68:69], v25 offset:0\n\t"
"ds_read_b64_tr_b16 a[70:71], v25 offset:5120\n\t"
"v_mfma_f32_16x16x32_bf16 a[132:135], a[72:75], a[20:23], a[132:135]\n\t"
"v_mfma_f32_16x16x32_bf16 a[136:139], a[72:75], a[24:27], a[136:139]\n\t"
"v_mfma_f32_16x16x32_bf16 a[140:143], a[72:75], a[28:31], a[140:143]\n\t"
"s_waitcnt lgkmcnt(4)\n\t"
"v_mfma_f32_16x16x32_bf16 a[128:131], a[76:79], a[32:35], a[128:131]\n\t"
"ds_read_b64_tr_b16 a[72:73], v25 offset:40960\n\t"
"ds_read_b64_tr_b16 a[74:75], v25 offset:46080\n\t"
"v_mfma_f32_16x16x32_bf16 a[132:135], a[76:79], a[36:39], a[132:135]\n\t"
"v_mfma_f32_16x16x32_bf16 a[136:139], a[76:79], a[40:43], a[136:139]\n\t"
"v_mfma_f32_16x16x32_bf16 a[140:143], a[76:79], a[44:47], a[140:143]\n\t"
"s_waitcnt lgkmcnt(4)\n\t"
"v_mfma_f32_16x16x32_bf16 a[144:147], a[64:67], v[128:131], a[144:147]\n\t"
"ds_read_b64_tr_b16 a[76:77], v25 offset:30720\n\t"
"ds_read_b64_tr_b16 a[78:79], v25 offset:35840\n\t"
"v_mfma_f32_16x16x32_bf16 a[148:151], a[64:67], v[132:135], a[148:151]\n\t"
"v_mfma_f32_16x16x32_bf16 a[152:155], a[64:67], v[136:139], a[152:155]\n\t"
"v_mfma_f32_16x16x32_bf16 a[156:159], a[64:67], v[140:143], a[156:159]\n\t"
"s_waitcnt lgkmcnt(4)\n\t"
"v_mfma_f32_16x16x32_bf16 a[144:147], a[68:71], v[160:163], a[144:147]\n\t"
"ds_read_b64_tr_b16 a[64:65], v25 offset:10240\n\t"
"ds_read_b64_tr_b16 a[66:67], v25 offset:15360\n\t"
"v_mfma_f32_16x16x32_bf16 a[148:151], a[68:71], v[164:167], a[148:151]\n\t"
"v_accvgpr_read_b32 v86, a112\n\t"
"v_accvgpr_read_b32 v87, a113\n\t"
"v_mfma_f32_16x16x32_bf16 a[152:155], a[68:71], v[168:171], a[152:155]\n\t"
"v_accvgpr_read_b32 v88, a114\n\t"
"v_accvgpr_read_b32 v89, a115\n\t"
"v_mfma_f32_16x16x32_bf16 a[156:159], a[68:71], v[172:175], a[156:159]\n\t"
"s_waitcnt lgkmcnt(4)\n\t"
"v_cvt_pk_bf16_f32 v90, v86, v87\n\t"
"v_cvt_pk_bf16_f32 v91, v88, v89\n\t"
"v_mfma_f32_16x16x32_bf16 a[144:147], a[72:75], v[208:211], a[144:147]\n\t"
"ds_read_b64_tr_b16 a[68:69], v25 offset:51200\n\t"
"ds_read_b64_tr_b16 a[70:71], v25 offset:56320\n\t"
"v_mfma_f32_16x16x32_bf16 a[148:151], a[72:75], v[212:215], a[148:151]\n\t"
"v_accvgpr_read_b32 v86, a128\n\t"
"v_accvgpr_read_b32 v87, a129\n\t"
"v_mfma_f32_16x16x32_bf16 a[152:155], a[72:75], v[216:219], a[152:155]\n\t"
"v_accvgpr_read_b32 v88, a130\n\t"
"v_accvgpr_read_b32 v89, a131\n\t"
"v_mfma_f32_16x16x32_bf16 a[156:159], a[72:75], v[220:223], a[156:159]\n\t"
"s_waitcnt lgkmcnt(4)\n\t"
"v_cvt_pk_bf16_f32 v92, v86, v87\n\t"
"v_mfma_f32_16x16x32_bf16 a[144:147], a[76:79], a[0:3], a[144:147]\n\t"
"ds_read_b64_tr_b16 a[72:73], v26 offset:20480\n\t"
"ds_read_b64_tr_b16 a[74:75], v26 offset:25600\n\t"
"v_cvt_pk_bf16_f32 v93, v88, v89\n\t"
"v_mfma_f32_16x16x32_bf16 a[148:151], a[76:79], a[4:7], a[148:151]\n\t"
"buffer_store_dwordx4 v[90:93], v28, s[44:47], s82 offen offset:0 sc1\n\t"
"v_mfma_f32_16x16x32_bf16 a[152:155], a[76:79], a[8:11], a[152:155]\n\t"
"v_accvgpr_read_b32 v86, a116\n\t"
"v_accvgpr_read_b32 v87, a117\n\t"
"v_mfma_f32_16x16x32_bf16 a[156:159], a[76:79], a[12:15], a[156:159]\n\t"
"s_waitcnt lgkmcnt(4)\n\t"
"v_accvgpr_read_b32 v88, a118\n\t"
"v_mfma_f32_16x16x32_bf16 a[144:147], a[64:67], a[16:19], a[144:147]\n\t"
"ds_read_b64_tr_b16 a[76:77], v26 offset:0\n\t"
"ds_read_b64_tr_b16 a[78:79], v26 offset:5120\n\t"
"v_accvgpr_read_b32 v89, a119\n\t"
"v_mfma_f32_16x16x32_bf16 a[148:151], a[64:67], a[20:23], a[148:151]\n\t"
"v_cvt_pk_bf16_f32 v94, v86, v87\n\t"
"v_mfma_f32_16x16x32_bf16 a[152:155], a[64:67], a[24:27], a[152:155]\n\t"
"v_cvt_pk_bf16_f32 v95, v88, v89\n\t"
"v_mfma_f32_16x16x32_bf16 a[156:159], a[64:67], a[28:31], a[156:159]\n\t"
"s_waitcnt lgkmcnt(4)\n\t"
"v_accvgpr_read_b32 v86, a132\n\t"
"v_accvgpr_read_b32 v87, a133\n\t"
"v_mfma_f32_16x16x32_bf16 a[144:147], a[68:71], a[32:35], a[144:147]\n\t"
"ds_read_b64_tr_b16 a[64:65], v26 offset:40960\n\t"
"ds_read_b64_tr_b16 a[66:67], v26 offset:46080\n\t"
"v_accvgpr_read_b32 v88, a134\n\t"
"v_mfma_f32_16x16x32_bf16 a[148:151], a[68:71], a[36:39], a[148:151]\n\t"
"v_accvgpr_read_b32 v89, a135\n\t"
"v_mfma_f32_16x16x32_bf16 a[152:155], a[68:71], a[40:43], a[152:155]\n\t"
"v_cvt_pk_bf16_f32 v96, v86, v87\n\t"
"v_mfma_f32_16x16x32_bf16 a[156:159], a[68:71], a[44:47], a[156:159]\n\t"
"s_waitcnt lgkmcnt(4)\n\t"
"v_cvt_pk_bf16_f32 v97, v88, v89\n\t"
"v_mfma_f32_16x16x32_bf16 a[160:163], a[72:75], v[128:131], a[160:163]\n\t"
"ds_read_b64_tr_b16 a[68:69], v26 offset:30720\n\t"
"ds_read_b64_tr_b16 a[70:71], v26 offset:35840\n\t"
"buffer_store_dwordx4 v[94:97], v28, s[44:47], s82 offen offset:1024 sc1\n\t"
"v_mfma_f32_16x16x32_bf16 a[164:167], a[72:75], v[132:135], a[164:167]\n\t"
"v_accvgpr_read_b32 v86, a120\n\t"
"v_accvgpr_read_b32 v87, a121\n\t"
"v_mfma_f32_16x16x32_bf16 a[168:171], a[72:75], v[136:139], a[168:171]\n\t"
"v_accvgpr_read_b32 v88, a122\n\t"
"v_mfma_f32_16x16x32_bf16 a[172:175], a[72:75], v[140:143], a[172:175]\n\t"
"s_waitcnt lgkmcnt(4)\n\t"
"v_accvgpr_read_b32 v89, a123\n\t"
"v_mfma_f32_16x16x32_bf16 a[160:163], a[76:79], v[160:163], a[160:163]\n\t"
"ds_read_b64_tr_b16 a[72:73], v26 offset:10240\n\t"
"ds_read_b64_tr_b16 a[74:75], v26 offset:15360\n\t"
"v_cvt_pk_bf16_f32 v90, v86, v87\n\t"
"v_mfma_f32_16x16x32_bf16 a[164:167], a[76:79], v[164:167], a[164:167]\n\t"
"v_cvt_pk_bf16_f32 v91, v88, v89\n\t"
"v_accvgpr_read_b32 v86, a136\n\t"
"v_mfma_f32_16x16x32_bf16 a[168:171], a[76:79], v[168:171], a[168:171]\n\t"
"v_accvgpr_read_b32 v87, a137\n\t"
"v_mfma_f32_16x16x32_bf16 a[172:175], a[76:79], v[172:175], a[172:175]\n\t"
"s_waitcnt lgkmcnt(4)\n\t"
"v_accvgpr_read_b32 v88, a138\n\t"
"v_mfma_f32_16x16x32_bf16 a[160:163], a[64:67], v[208:211], a[160:163]\n\t"
"ds_read_b64_tr_b16 a[76:77], v26 offset:51200\n\t"
"ds_read_b64_tr_b16 a[78:79], v26 offset:56320\n\t"
"v_accvgpr_read_b32 v89, a139\n\t"
"v_mfma_f32_16x16x32_bf16 a[164:167], a[64:67], v[212:215], a[164:167]\n\t"
"v_cvt_pk_bf16_f32 v92, v86, v87\n\t"
"v_cvt_pk_bf16_f32 v93, v88, v89\n\t"
"v_mfma_f32_16x16x32_bf16 a[168:171], a[64:67], v[216:219], a[168:171]\n\t"
"buffer_store_dwordx4 v[90:93], v28, s[44:47], s82 offen offset:2048 sc1\n\t"
"v_mfma_f32_16x16x32_bf16 a[172:175], a[64:67], v[220:223], a[172:175]\n\t"
"s_waitcnt lgkmcnt(4)\n\t"
"v_accvgpr_read_b32 v86, a124\n\t"
"v_mfma_f32_16x16x32_bf16 a[160:163], a[68:71], a[0:3], a[160:163]\n\t"
"ds_read_b64_tr_b16 a[64:65], v23 offset:22528\n\t"
"ds_read_b64_tr_b16 a[66:67], v23 offset:27648\n\t"
"v_accvgpr_read_b32 v87, a125\n\t"
"v_mfma_f32_16x16x32_bf16 a[164:167], a[68:71], a[4:7], a[164:167]\n\t"
"v_accvgpr_read_b32 v88, a126\n\t"
"v_mfma_f32_16x16x32_bf16 a[168:171], a[68:71], a[8:11], a[168:171]\n\t"
"v_accvgpr_read_b32 v89, a127\n\t"
"v_cvt_pk_bf16_f32 v94, v86, v87\n\t"
"v_mfma_f32_16x16x32_bf16 a[172:175], a[68:71], a[12:15], a[172:175]\n\t"
"s_waitcnt lgkmcnt(4)\n\t"
"v_cvt_pk_bf16_f32 v95, v88, v89\n\t"
"v_mfma_f32_16x16x32_bf16 a[160:163], a[72:75], a[16:19], a[160:163]\n\t"
"ds_read_b64_tr_b16 a[68:69], v23 offset:2048\n\t"
"ds_read_b64_tr_b16 a[70:71], v23 offset:7168\n\t"
"v_accvgpr_read_b32 v86, a140\n\t"
"v_mfma_f32_16x16x32_bf16 a[164:167], a[72:75], a[20:23], a[164:167]\n\t"
"v_accvgpr_read_b32 v87, a141\n\t"
"v_mfma_f32_16x16x32_bf16 a[168:171], a[72:75], a[24:27], a[168:171]\n\t"
"v_accvgpr_read_b32 v88, a142\n\t"
"v_mfma_f32_16x16x32_bf16 a[172:175], a[72:75], a[28:31], a[172:175]\n\t"
"s_waitcnt lgkmcnt(4)\n\t"
"v_accvgpr_read_b32 v89, a143\n\t"
"v_cvt_pk_bf16_f32 v96, v86, v87\n\t"
"v_mfma_f32_16x16x32_bf16 a[160:163], a[76:79], a[32:35], a[160:163]\n\t"
"ds_read_b64_tr_b16 a[72:73], v23 offset:43008\n\t"
"ds_read_b64_tr_b16 a[74:75], v23 offset:48128\n\t"
"v_cvt_pk_bf16_f32 v97, v88, v89\n\t"
"v_mfma_f32_16x16x32_bf16 a[164:167], a[76:79], a[36:39], a[164:167]\n\t"
"buffer_store_dwordx4 v[94:97], v28, s[44:47], s82 offen offset:3072 sc1\n\t"
"v_mfma_f32_16x16x32_bf16 a[168:171], a[76:79], a[40:43], a[168:171]\n\t"
"v_mfma_f32_16x16x32_bf16 a[172:175], a[76:79], a[44:47], a[172:175]\n\t"
"s_waitcnt lgkmcnt(4)\n\t"
"v_mfma_f32_16x16x32_bf16 a[176:179], a[64:67], v[128:131], a[176:179]\n\t"
"ds_read_b64_tr_b16 a[76:77], v23 offset:32768\n\t"
"ds_read_b64_tr_b16 a[78:79], v23 offset:37888\n\t"
"v_mfma_f32_16x16x32_bf16 a[180:183], a[64:67], v[132:135], a[180:183]\n\t"
"v_mfma_f32_16x16x32_bf16 a[184:187], a[64:67], v[136:139], a[184:187]\n\t"
"v_mfma_f32_16x16x32_bf16 a[188:191], a[64:67], v[140:143], a[188:191]\n\t"
"s_waitcnt lgkmcnt(4)\n\t"
"v_mfma_f32_16x16x32_bf16 a[176:179], a[68:71], v[160:163], a[176:179]\n\t"
"ds_read_b64_tr_b16 a[64:65], v23 offset:12288\n\t"
"ds_read_b64_tr_b16 a[66:67], v23 offset:17408\n\t"
"v_mfma_f32_16x16x32_bf16 a[180:183], a[68:71], v[164:167], a[180:183]\n\t"
"v_accvgpr_read_b32 v86, a144\n\t"
"v_accvgpr_read_b32 v87, a145\n\t"
"v_mfma_f32_16x16x32_bf16 a[184:187], a[68:71], v[168:171], a[184:187]\n\t"
"v_accvgpr_read_b32 v88, a146\n\t"
"v_accvgpr_read_b32 v89, a147\n\t"
"v_mfma_f32_16x16x32_bf16 a[188:191], a[68:71], v[172:175], a[188:191]\n\t"
"s_waitcnt lgkmcnt(4)\n\t"
"v_cvt_pk_bf16_f32 v90, v86, v87\n\t"
"v_cvt_pk_bf16_f32 v91, v88, v89\n\t"
"v_mfma_f32_16x16x32_bf16 a[176:179], a[72:75], v[208:211], a[176:179]\n\t"
"ds_read_b64_tr_b16 a[68:69], v23 offset:53248\n\t"
"ds_read_b64_tr_b16 a[70:71], v23 offset:58368\n\t"
"v_mfma_f32_16x16x32_bf16 a[180:183], a[72:75], v[212:215], a[180:183]\n\t"
"v_accvgpr_read_b32 v86, a160\n\t"
"v_accvgpr_read_b32 v87, a161\n\t"
"v_mfma_f32_16x16x32_bf16 a[184:187], a[72:75], v[216:219], a[184:187]\n\t"
"v_accvgpr_read_b32 v88, a162\n\t"
"v_accvgpr_read_b32 v89, a163\n\t"
"v_mfma_f32_16x16x32_bf16 a[188:191], a[72:75], v[220:223], a[188:191]\n\t"
"s_waitcnt lgkmcnt(4)\n\t"
"v_cvt_pk_bf16_f32 v92, v86, v87\n\t"
"v_mfma_f32_16x16x32_bf16 a[176:179], a[76:79], a[0:3], a[176:179]\n\t"
"ds_read_b64_tr_b16 a[72:73], v24 offset:22528\n\t"
"ds_read_b64_tr_b16 a[74:75], v24 offset:27648\n\t"
"v_cvt_pk_bf16_f32 v93, v88, v89\n\t"
"v_mfma_f32_16x16x32_bf16 a[180:183], a[76:79], a[4:7], a[180:183]\n\t"
"buffer_store_dwordx4 v[90:93], v29, s[44:47], s82 offen offset:0 sc1\n\t"
"v_mfma_f32_16x16x32_bf16 a[184:187], a[76:79], a[8:11], a[184:187]\n\t"
"v_accvgpr_read_b32 v86, a148\n\t"
"v_accvgpr_read_b32 v87, a149\n\t"
"v_mfma_f32_16x16x32_bf16 a[188:191], a[76:79], a[12:15], a[188:191]\n\t"
"s_waitcnt lgkmcnt(4)\n\t"
"v_accvgpr_read_b32 v88, a150\n\t"
"v_mfma_f32_16x16x32_bf16 a[176:179], a[64:67], a[16:19], a[176:179]\n\t"
"ds_read_b64_tr_b16 a[76:77], v24 offset:2048\n\t"
"ds_read_b64_tr_b16 a[78:79], v24 offset:7168\n\t"
"v_accvgpr_read_b32 v89, a151\n\t"
"v_mfma_f32_16x16x32_bf16 a[180:183], a[64:67], a[20:23], a[180:183]\n\t"
"v_cvt_pk_bf16_f32 v94, v86, v87\n\t"
"v_mfma_f32_16x16x32_bf16 a[184:187], a[64:67], a[24:27], a[184:187]\n\t"
"v_cvt_pk_bf16_f32 v95, v88, v89\n\t"
"v_mfma_f32_16x16x32_bf16 a[188:191], a[64:67], a[28:31], a[188:191]\n\t"
"s_waitcnt lgkmcnt(4)\n\t"
"v_accvgpr_read_b32 v86, a164\n\t"
"v_accvgpr_read_b32 v87, a165\n\t"
"v_mfma_f32_16x16x32_bf16 a[176:179], a[68:71], a[32:35], a[176:179]\n\t"
"ds_read_b64_tr_b16 a[64:65], v24 offset:43008\n\t"
"ds_read_b64_tr_b16 a[66:67], v24 offset:48128\n\t"
"v_accvgpr_read_b32 v88, a166\n\t"
"v_mfma_f32_16x16x32_bf16 a[180:183], a[68:71], a[36:39], a[180:183]\n\t"
"v_accvgpr_read_b32 v89, a167\n\t"
"v_mfma_f32_16x16x32_bf16 a[184:187], a[68:71], a[40:43], a[184:187]\n\t"
"v_cvt_pk_bf16_f32 v96, v86, v87\n\t"
"v_mfma_f32_16x16x32_bf16 a[188:191], a[68:71], a[44:47], a[188:191]\n\t"
"s_waitcnt lgkmcnt(4)\n\t"
"v_cvt_pk_bf16_f32 v97, v88, v89\n\t"
"v_mfma_f32_16x16x32_bf16 a[192:195], a[72:75], v[128:131], a[192:195]\n\t"
"ds_read_b64_tr_b16 a[68:69], v24 offset:32768\n\t"
"ds_read_b64_tr_b16 a[70:71], v24 offset:37888\n\t"
"buffer_store_dwordx4 v[94:97], v29, s[44:47], s82 offen offset:1024 sc1\n\t"
"v_mfma_f32_16x16x32_bf16 a[196:199], a[72:75], v[132:135], a[196:199]\n\t"
"v_accvgpr_read_b32 v86, a152\n\t"
"v_accvgpr_read_b32 v87, a153\n\t"
"v_mfma_f32_16x16x32_bf16 a[200:203], a[72:75], v[136:139], a[200:203]\n\t"
"v_accvgpr_read_b32 v88, a154\n\t"
"v_mfma_f32_16x16x32_bf16 a[204:207], a[72:75], v[140:143], a[204:207]\n\t"
"s_waitcnt lgkmcnt(4)\n\t"
"v_accvgpr_read_b32 v89, a155\n\t"
"v_mfma_f32_16x16x32_bf16 a[192:195], a[76:79], v[160:163], a[192:195]\n\t"
"ds_read_b64_tr_b16 a[72:73], v24 offset:12288\n\t"
"ds_read_b64_tr_b16 a[74:75], v24 offset:17408\n\t"
"v_cvt_pk_bf16_f32 v90, v86, v87\n\t"
"v_mfma_f32_16x16x32_bf16 a[196:199], a[76:79], v[164:167], a[196:199]\n\t"
"v_cvt_pk_bf16_f32 v91, v88, v89\n\t"
"v_accvgpr_read_b32 v86, a168\n\t"
"v_mfma_f32_16x16x32_bf16 a[200:203], a[76:79], v[168:171], a[200:203]\n\t"
"v_accvgpr_read_b32 v87, a169\n\t"
"v_mfma_f32_16x16x32_bf16 a[204:207], a[76:79], v[172:175], a[204:207]\n\t"
"s_waitcnt lgkmcnt(4)\n\t"
"v_accvgpr_read_b32 v88, a170\n\t"
"v_mfma_f32_16x16x32_bf16 a[192:195], a[64:67], v[208:211], a[192:195]\n\t"
"ds_read_b64_tr_b16 a[76:77], v24 offset:53248\n\t"
"ds_read_b64_tr_b16 a[78:79], v24 offset:58368\n\t"
"v_accvgpr_read_b32 v89, a171\n\t"
"v_mfma_f32_16x16x32_bf16 a[196:199], a[64:67], v[212:215], a[196:199]\n\t"
"v_cvt_pk_bf16_f32 v92, v86, v87\n\t"
"v_cvt_pk_bf16_f32 v93, v88, v89\n\t"
"v_mfma_f32_16x16x32_bf16 a[200:203], a[64:67], v[216:219], a[200:203]\n\t"
"buffer_store_dwordx4 v[90:93], v29, s[44:47], s82 offen offset:2048 sc1\n\t"
"v_mfma_f32_16x16x32_bf16 a[204:207], a[64:67], v[220:223], a[204:207]\n\t"
"s_waitcnt lgkmcnt(4)\n\t"
"v_accvgpr_read_b32 v86, a156\n\t"
"v_mfma_f32_16x16x32_bf16 a[192:195], a[68:71], a[0:3], a[192:195]\n\t"
"ds_read_b64_tr_b16 a[64:65], v25 offset:22528\n\t"
"ds_read_b64_tr_b16 a[66:67], v25 offset:27648\n\t"
"v_accvgpr_read_b32 v87, a157\n\t"
"v_mfma_f32_16x16x32_bf16 a[196:199], a[68:71], a[4:7], a[196:199]\n\t"
"v_accvgpr_read_b32 v88, a158\n\t"
"v_mfma_f32_16x16x32_bf16 a[200:203], a[68:71], a[8:11], a[200:203]\n\t"
"v_accvgpr_read_b32 v89, a159\n\t"
"v_cvt_pk_bf16_f32 v94, v86, v87\n\t"
"v_mfma_f32_16x16x32_bf16 a[204:207], a[68:71], a[12:15], a[204:207]\n\t"
"s_waitcnt lgkmcnt(4)\n\t"
"v_cvt_pk_bf16_f32 v95, v88, v89\n\t"
"v_mfma_f32_16x16x32_bf16 a[192:195], a[72:75], a[16:19], a[192:195]\n\t"
"ds_read_b64_tr_b16 a[68:69], v25 offset:2048\n\t"
"ds_read_b64_tr_b16 a[70:71], v25 offset:7168\n\t"
"v_accvgpr_read_b32 v86, a172\n\t"
"v_mfma_f32_16x16x32_bf16 a[196:199], a[72:75], a[20:23], a[196:199]\n\t"
"v_accvgpr_read_b32 v87, a173\n\t"
"v_mfma_f32_16x16x32_bf16 a[200:203], a[72:75], a[24:27], a[200:203]\n\t"
"v_accvgpr_read_b32 v88, a174\n\t"
"v_mfma_f32_16x16x32_bf16 a[204:207], a[72:75], a[28:31], a[204:207]\n\t"
"s_waitcnt lgkmcnt(4)\n\t"
"v_accvgpr_read_b32 v89, a175\n\t"
"v_cvt_pk_bf16_f32 v96, v86, v87\n\t"
"v_mfma_f32_16x16x32_bf16 a[192:195], a[76:79], a[32:35], a[192:195]\n\t"
"ds_read_b64_tr_b16 a[72:73], v25 offset:43008\n\t"
"ds_read_b64_tr_b16 a[74:75], v25 offset:48128\n\t"
"v_cvt_pk_bf16_f32 v97, v88, v89\n\t"
"v_mfma_f32_16x16x32_bf16 a[196:199], a[76:79], a[36:39], a[196:199]\n\t"
"buffer_store_dwordx4 v[94:97], v29, s[44:47], s82 offen offset:3072 sc1\n\t"
"v_mfma_f32_16x16x32_bf16 a[200:203], a[76:79], a[40:43], a[200:203]\n\t"
"v_mfma_f32_16x16x32_bf16 a[204:207], a[76:79], a[44:47], a[204:207]\n\t"
"s_waitcnt lgkmcnt(4)\n\t"
"v_mfma_f32_16x16x32_bf16 a[208:211], a[64:67], v[128:131], a[208:211]\n\t"
"ds_read_b64_tr_b16 a[76:77], v25 offset:32768\n\t"
"ds_read_b64_tr_b16 a[78:79], v25 offset:37888\n\t"
"v_mfma_f32_16x16x32_bf16 a[212:215], a[64:67], v[132:135], a[212:215]\n\t"
"v_mfma_f32_16x16x32_bf16 a[216:219], a[64:67], v[136:139], a[216:219]\n\t"
"v_mfma_f32_16x16x32_bf16 a[220:223], a[64:67], v[140:143], a[220:223]\n\t"
"s_waitcnt lgkmcnt(4)\n\t"
"v_mfma_f32_16x16x32_bf16 a[208:211], a[68:71], v[160:163], a[208:211]\n\t"
"ds_read_b64_tr_b16 a[64:65], v25 offset:12288\n\t"
"ds_read_b64_tr_b16 a[66:67], v25 offset:17408\n\t"
"v_mfma_f32_16x16x32_bf16 a[212:215], a[68:71], v[164:167], a[212:215]\n\t"
"v_accvgpr_read_b32 v86, a176\n\t"
"v_accvgpr_read_b32 v87, a177\n\t"
"v_mfma_f32_16x16x32_bf16 a[216:219], a[68:71], v[168:171], a[216:219]\n\t"
"v_accvgpr_read_b32 v88, a178\n\t"
"v_accvgpr_read_b32 v89, a179\n\t"
"v_mfma_f32_16x16x32_bf16 a[220:223], a[68:71], v[172:175], a[220:223]\n\t"
"s_waitcnt lgkmcnt(4)\n\t"
"v_cvt_pk_bf16_f32 v90, v86, v87\n\t"
"v_cvt_pk_bf16_f32 v91, v88, v89\n\t"
"v_mfma_f32_16x16x32_bf16 a[208:211], a[72:75], v[208:211], a[208:211]\n\t"
"ds_read_b64_tr_b16 a[68:69], v25 offset:53248\n\t"
"ds_read_b64_tr_b16 a[70:71], v25 offset:58368\n\t"
"v_mfma_f32_16x16x32_bf16 a[212:215], a[72:75], v[212:215], a[212:215]\n\t"
"v_accvgpr_read_b32 v86, a192\n\t"
"v_accvgpr_read_b32 v87, a193\n\t"
"v_mfma_f32_16x16x32_bf16 a[216:219], a[72:75], v[216:219], a[216:219]\n\t"
"v_accvgpr_read_b32 v88, a194\n\t"
"v_accvgpr_read_b32 v89, a195\n\t"
"v_mfma_f32_16x16x32_bf16 a[220:223], a[72:75], v[220:223], a[220:223]\n\t"
"s_waitcnt lgkmcnt(4)\n\t"
"v_cvt_pk_bf16_f32 v92, v86, v87\n\t"
"v_mfma_f32_16x16x32_bf16 a[208:211], a[76:79], a[0:3], a[208:211]\n\t"
"ds_read_b64_tr_b16 a[72:73], v26 offset:22528\n\t"
"ds_read_b64_tr_b16 a[74:75], v26 offset:27648\n\t"
"v_cvt_pk_bf16_f32 v93, v88, v89\n\t"
"v_mfma_f32_16x16x32_bf16 a[212:215], a[76:79], a[4:7], a[212:215]\n\t"
"buffer_store_dwordx4 v[90:93], v28, s[44:47], s83 offen offset:0 sc1\n\t"
"v_mfma_f32_16x16x32_bf16 a[216:219], a[76:79], a[8:11], a[216:219]\n\t"
"v_accvgpr_read_b32 v86, a180\n\t"
"v_accvgpr_read_b32 v87, a181\n\t"
"v_mfma_f32_16x16x32_bf16 a[220:223], a[76:79], a[12:15], a[220:223]\n\t"
"s_waitcnt lgkmcnt(4)\n\t"
"v_accvgpr_read_b32 v88, a182\n\t"
"v_mfma_f32_16x16x32_bf16 a[208:211], a[64:67], a[16:19], a[208:211]\n\t"
"ds_read_b64_tr_b16 a[76:77], v26 offset:2048\n\t"
"ds_read_b64_tr_b16 a[78:79], v26 offset:7168\n\t"
"v_accvgpr_read_b32 v89, a183\n\t"
"v_mfma_f32_16x16x32_bf16 a[212:215], a[64:67], a[20:23], a[212:215]\n\t"
"v_cvt_pk_bf16_f32 v94, v86, v87\n\t"
"v_mfma_f32_16x16x32_bf16 a[216:219], a[64:67], a[24:27], a[216:219]\n\t"
"v_cvt_pk_bf16_f32 v95, v88, v89\n\t"
"v_mfma_f32_16x16x32_bf16 a[220:223], a[64:67], a[28:31], a[220:223]\n\t"
"s_waitcnt lgkmcnt(4)\n\t"
"v_accvgpr_read_b32 v86, a196\n\t"
"v_accvgpr_read_b32 v87, a197\n\t"
"v_mfma_f32_16x16x32_bf16 a[208:211], a[68:71], a[32:35], a[208:211]\n\t"
"ds_read_b64_tr_b16 a[64:65], v26 offset:43008\n\t"
"ds_read_b64_tr_b16 a[66:67], v26 offset:48128\n\t"
"v_accvgpr_read_b32 v88, a198\n\t"
"v_mfma_f32_16x16x32_bf16 a[212:215], a[68:71], a[36:39], a[212:215]\n\t"
"v_accvgpr_read_b32 v89, a199\n\t"
"v_mfma_f32_16x16x32_bf16 a[216:219], a[68:71], a[40:43], a[216:219]\n\t"
"v_cvt_pk_bf16_f32 v96, v86, v87\n\t"
"v_mfma_f32_16x16x32_bf16 a[220:223], a[68:71], a[44:47], a[220:223]\n\t"
"s_waitcnt lgkmcnt(4)\n\t"
"v_cvt_pk_bf16_f32 v97, v88, v89\n\t"
"v_mfma_f32_16x16x32_bf16 a[224:227], a[72:75], v[128:131], a[224:227]\n\t"
"ds_read_b64_tr_b16 a[68:69], v26 offset:32768\n\t"
"ds_read_b64_tr_b16 a[70:71], v26 offset:37888\n\t"
"buffer_store_dwordx4 v[94:97], v28, s[44:47], s83 offen offset:1024 sc1\n\t"
"v_mfma_f32_16x16x32_bf16 a[228:231], a[72:75], v[132:135], a[228:231]\n\t"
"v_accvgpr_read_b32 v86, a184\n\t"
"v_accvgpr_read_b32 v87, a185\n\t"
"v_mfma_f32_16x16x32_bf16 a[232:235], a[72:75], v[136:139], a[232:235]\n\t"
"v_accvgpr_read_b32 v88, a186\n\t"
"v_mfma_f32_16x16x32_bf16 a[236:239], a[72:75], v[140:143], a[236:239]\n\t"
"s_waitcnt lgkmcnt(4)\n\t"
"v_accvgpr_read_b32 v89, a187\n\t"
"v_mfma_f32_16x16x32_bf16 a[224:227], a[76:79], v[160:163], a[224:227]\n\t"
"ds_read_b64_tr_b16 a[72:73], v26 offset:12288\n\t"
"ds_read_b64_tr_b16 a[74:75], v26 offset:17408\n\t"
"v_cvt_pk_bf16_f32 v90, v86, v87\n\t"
"v_mfma_f32_16x16x32_bf16 a[228:231], a[76:79], v[164:167], a[228:231]\n\t"
"v_cvt_pk_bf16_f32 v91, v88, v89\n\t"
"v_accvgpr_read_b32 v86, a200\n\t"
"v_mfma_f32_16x16x32_bf16 a[232:235], a[76:79], v[168:171], a[232:235]\n\t"
"v_accvgpr_read_b32 v87, a201\n\t"
"v_mfma_f32_16x16x32_bf16 a[236:239], a[76:79], v[172:175], a[236:239]\n\t"
"s_waitcnt lgkmcnt(4)\n\t"
"v_accvgpr_read_b32 v88, a202\n\t"
"v_mfma_f32_16x16x32_bf16 a[224:227], a[64:67], v[208:211], a[224:227]\n\t"
"ds_read_b64_tr_b16 a[76:77], v26 offset:53248\n\t"
"ds_read_b64_tr_b16 a[78:79], v26 offset:58368\n\t"
"v_accvgpr_read_b32 v89, a203\n\t"
"v_mfma_f32_16x16x32_bf16 a[228:231], a[64:67], v[212:215], a[228:231]\n\t"
"v_cvt_pk_bf16_f32 v92, v86, v87\n\t"
"v_cvt_pk_bf16_f32 v93, v88, v89\n\t"
"v_mfma_f32_16x16x32_bf16 a[232:235], a[64:67], v[216:219], a[232:235]\n\t"
"buffer_store_dwordx4 v[90:93], v28, s[44:47], s83 offen offset:2048 sc1\n\t"
"v_mfma_f32_16x16x32_bf16 a[236:239], a[64:67], v[220:223], a[236:239]\n\t"
"s_waitcnt lgkmcnt(4)\n\t"
"v_accvgpr_read_b32 v86, a188\n\t"
"v_mfma_f32_16x16x32_bf16 a[224:227], a[68:71], a[0:3], a[224:227]\n\t"
"ds_read_b64_tr_b16 a[64:65], v27 offset:20480\n\t"
"ds_read_b64_tr_b16 a[66:67], v27 offset:25600\n\t"
"v_accvgpr_read_b32 v87, a189\n\t"
"v_mfma_f32_16x16x32_bf16 a[228:231], a[68:71], a[4:7], a[228:231]\n\t"
"v_accvgpr_read_b32 v88, a190\n\t"
"v_mfma_f32_16x16x32_bf16 a[232:235], a[68:71], a[8:11], a[232:235]\n\t"
"v_accvgpr_read_b32 v89, a191\n\t"
"v_cvt_pk_bf16_f32 v94, v86, v87\n\t"
"v_mfma_f32_16x16x32_bf16 a[236:239], a[68:71], a[12:15], a[236:239]\n\t"
"s_waitcnt lgkmcnt(4)\n\t"
"v_cvt_pk_bf16_f32 v95, v88, v89\n\t"
"v_mfma_f32_16x16x32_bf16 a[224:227], a[72:75], a[16:19], a[224:227]\n\t"
"ds_read_b64_tr_b16 a[68:69], v27 offset:0\n\t"
"ds_read_b64_tr_b16 a[70:71], v27 offset:5120\n\t"
"v_accvgpr_read_b32 v86, a204\n\t"
"v_mfma_f32_16x16x32_bf16 a[228:231], a[72:75], a[20:23], a[228:231]\n\t"
"v_accvgpr_read_b32 v87, a205\n\t"
"v_mfma_f32_16x16x32_bf16 a[232:235], a[72:75], a[24:27], a[232:235]\n\t"
"v_accvgpr_read_b32 v88, a206\n\t"
"v_mfma_f32_16x16x32_bf16 a[236:239], a[72:75], a[28:31], a[236:239]\n\t"
"s_waitcnt lgkmcnt(4)\n\t"
"v_accvgpr_read_b32 v89, a207\n\t"
"v_cvt_pk_bf16_f32 v96, v86, v87\n\t"
"v_mfma_f32_16x16x32_bf16 a[224:227], a[76:79], a[32:35], a[224:227]\n\t"
"ds_read_b64_tr_b16 a[72:73], v27 offset:40960\n\t"
"ds_read_b64_tr_b16 a[74:75], v27 offset:46080\n\t"
"v_cvt_pk_bf16_f32 v97, v88, v89\n\t"
"v_mfma_f32_16x16x32_bf16 a[228:231], a[76:79], a[36:39], a[228:231]\n\t"
"buffer_store_dwordx4 v[94:97], v28, s[44:47], s83 offen offset:3072 sc1\n\t"
"v_mfma_f32_16x16x32_bf16 a[232:235], a[76:79], a[40:43], a[232:235]\n\t"
"v_mfma_f32_16x16x32_bf16 a[236:239], a[76:79], a[44:47], a[236:239]\n\t"
"s_waitcnt lgkmcnt(4)\n\t"
"v_mfma_f32_16x16x32_bf16 a[240:243], a[64:67], v[128:131], a[240:243]\n\t"
"ds_read_b64_tr_b16 a[76:77], v27 offset:30720\n\t"
"ds_read_b64_tr_b16 a[78:79], v27 offset:35840\n\t"
"v_mfma_f32_16x16x32_bf16 a[244:247], a[64:67], v[132:135], a[244:247]\n\t"
"v_mfma_f32_16x16x32_bf16 a[248:251], a[64:67], v[136:139], a[248:251]\n\t"
"v_mfma_f32_16x16x32_bf16 a[252:255], a[64:67], v[140:143], a[252:255]\n\t"
"s_waitcnt lgkmcnt(4)\n\t"
"v_accvgpr_read_b32 v86, a208\n\t"
"v_accvgpr_read_b32 v87, a209\n\t"
"v_mfma_f32_16x16x32_bf16 a[240:243], a[68:71], v[160:163], a[240:243]\n\t"
"ds_read_b64_tr_b16 a[64:65], v27 offset:10240\n\t"
"ds_read_b64_tr_b16 a[66:67], v27 offset:15360\n\t"
"v_accvgpr_read_b32 v88, a210\n\t"
"v_mfma_f32_16x16x32_bf16 a[244:247], a[68:71], v[164:167], a[244:247]\n\t"
"v_accvgpr_read_b32 v89, a211\n\t"
"v_cvt_pk_bf16_f32 v90, v86, v87\n\t"
"v_cvt_pk_bf16_f32 v91, v88, v89\n\t"
"v_mfma_f32_16x16x32_bf16 a[248:251], a[68:71], v[168:171], a[248:251]\n\t"
"v_accvgpr_read_b32 v86, a224\n\t"
"v_accvgpr_read_b32 v87, a225\n\t"
"v_mfma_f32_16x16x32_bf16 a[252:255], a[68:71], v[172:175], a[252:255]\n\t"
"s_waitcnt lgkmcnt(4)\n\t"
"v_accvgpr_read_b32 v88, a226\n\t"
"v_accvgpr_read_b32 v89, a227\n\t"
"v_cvt_pk_bf16_f32 v92, v86, v87\n\t"
"v_mfma_f32_16x16x32_bf16 a[240:243], a[72:75], v[208:211], a[240:243]\n\t"
"ds_read_b64_tr_b16 a[68:69], v27 offset:51200\n\t"
"ds_read_b64_tr_b16 a[70:71], v27 offset:56320\n\t"
"v_cvt_pk_bf16_f32 v93, v88, v89\n\t"
"v_mfma_f32_16x16x32_bf16 a[244:247], a[72:75], v[212:215], a[244:247]\n\t"
"buffer_store_dwordx4 v[90:93], v29, s[44:47], s83 offen offset:0 sc1\n\t"
"v_accvgpr_read_b32 v86, a212\n\t"
"v_mfma_f32_16x16x32_bf16 a[248:251], a[72:75], v[216:219], a[248:251]\n\t"
"v_accvgpr_read_b32 v87, a213\n\t"
"v_accvgpr_read_b32 v88, a214\n\t"
"v_accvgpr_read_b32 v89, a215\n\t"
"v_mfma_f32_16x16x32_bf16 a[252:255], a[72:75], v[220:223], a[252:255]\n\t"
"s_waitcnt lgkmcnt(4)\n\t"
"v_cvt_pk_bf16_f32 v94, v86, v87\n\t"
"v_cvt_pk_bf16_f32 v95, v88, v89\n\t"
"v_accvgpr_read_b32 v86, a228\n\t"
"v_mfma_f32_16x16x32_bf16 a[240:243], a[76:79], a[0:3], a[240:243]\n\t"
"v_accvgpr_read_b32 v87, a229\n\t"
"v_accvgpr_read_b32 v88, a230\n\t"
"v_mfma_f32_16x16x32_bf16 a[244:247], a[76:79], a[4:7], a[244:247]\n\t"
"v_accvgpr_read_b32 v89, a231\n\t"
"v_cvt_pk_bf16_f32 v96, v86, v87\n\t"
"v_cvt_pk_bf16_f32 v97, v88, v89\n\t"
"v_mfma_f32_16x16x32_bf16 a[248:251], a[76:79], a[8:11], a[248:251]\n\t"
"buffer_store_dwordx4 v[94:97], v29, s[44:47], s83 offen offset:1024 sc1\n\t"
"v_accvgpr_read_b32 v86, a216\n\t"
"v_accvgpr_read_b32 v87, a217\n\t"
"v_mfma_f32_16x16x32_bf16 a[252:255], a[76:79], a[12:15], a[252:255]\n\t"
"s_waitcnt lgkmcnt(2)\n\t"
"v_accvgpr_read_b32 v88, a218\n\t"
"v_accvgpr_read_b32 v89, a219\n\t"
"v_mfma_f32_16x16x32_bf16 a[240:243], a[64:67], a[16:19], a[240:243]\n\t"
"v_cvt_pk_bf16_f32 v90, v86, v87\n\t"
"v_cvt_pk_bf16_f32 v91, v88, v89\n\t"
"v_accvgpr_read_b32 v86, a232\n\t"
"v_mfma_f32_16x16x32_bf16 a[244:247], a[64:67], a[20:23], a[244:247]\n\t"
"v_accvgpr_read_b32 v87, a233\n\t"
"v_accvgpr_read_b32 v88, a234\n\t"
"v_accvgpr_read_b32 v89, a235\n\t"
"v_mfma_f32_16x16x32_bf16 a[248:251], a[64:67], a[24:27], a[248:251]\n\t"
"v_cvt_pk_bf16_f32 v92, v86, v87\n\t"
"v_cvt_pk_bf16_f32 v93, v88, v89\n\t"
"v_mfma_f32_16x16x32_bf16 a[252:255], a[64:67], a[28:31], a[252:255]\n\t"
"s_waitcnt lgkmcnt(0)\n\t"
"buffer_store_dwordx4 v[90:93], v29, s[44:47], s83 offen offset:2048 sc1\n\t"
"v_accvgpr_read_b32 v86, a220\n\t"
"v_accvgpr_read_b32 v87, a221\n\t"
"v_mfma_f32_16x16x32_bf16 a[240:243], a[68:71], a[32:35], a[240:243]\n\t"
"v_accvgpr_read_b32 v88, a222\n\t"
"v_accvgpr_read_b32 v89, a223\n\t"
"v_cvt_pk_bf16_f32 v94, v86, v87\n\t"
"v_mfma_f32_16x16x32_bf16 a[244:247], a[68:71], a[36:39], a[244:247]\n\t"
"v_cvt_pk_bf16_f32 v95, v88, v89\n\t"
"v_accvgpr_read_b32 v86, a236\n\t"
"v_mfma_f32_16x16x32_bf16 a[248:251], a[68:71], a[40:43], a[248:251]\n\t"
"v_accvgpr_read_b32 v87, a237\n\t"
"v_accvgpr_read_b32 v88, a238\n\t"
"v_accvgpr_read_b32 v89, a239\n\t"
"v_mfma_f32_16x16x32_bf16 a[252:255], a[68:71], a[44:47], a[252:255]\n\t"
"v_cvt_pk_bf16_f32 v96, v86, v87\n\t"
"v_cvt_pk_bf16_f32 v97, v88, v89\n\t"
"buffer_store_dwordx4 v[94:97], v29, s[44:47], s83 offen offset:3072 sc1\n\t"
"v_accvgpr_read_b32 v86, a240\n\t"
"v_accvgpr_read_b32 v87, a241\n\t"
"v_accvgpr_read_b32 v88, a242\n\t"
"v_accvgpr_read_b32 v89, a243\n\t"
"v_cvt_pk_bf16_f32 v90, v86, v87\n\t"
"v_cvt_pk_bf16_f32 v91, v88, v89\n\t"
"buffer_store_dwordx2 v[90:91], v30, s[44:47], s84 offen offset:0 sc1\n\t"
"v_accvgpr_read_b32 v86, a244\n\t"
"v_accvgpr_read_b32 v87, a245\n\t"
"v_accvgpr_read_b32 v88, a246\n\t"
"v_accvgpr_read_b32 v89, a247\n\t"
"v_cvt_pk_bf16_f32 v92, v86, v87\n\t"
"v_cvt_pk_bf16_f32 v93, v88, v89\n\t"
"buffer_store_dwordx2 v[92:93], v30, s[44:47], s84 offen offset:1024 sc1\n\t"
"v_accvgpr_read_b32 v86, a248\n\t"
"v_accvgpr_read_b32 v87, a249\n\t"
"v_accvgpr_read_b32 v88, a250\n\t"
"v_accvgpr_read_b32 v89, a251\n\t"
"v_cvt_pk_bf16_f32 v94, v86, v87\n\t"
"v_cvt_pk_bf16_f32 v95, v88, v89\n\t"
"buffer_store_dwordx2 v[94:95], v30, s[44:47], s84 offen offset:2048 sc1\n\t"
"v_accvgpr_read_b32 v86, a252\n\t"
"v_accvgpr_read_b32 v87, a253\n\t"
"v_accvgpr_read_b32 v88, a254\n\t"
"v_accvgpr_read_b32 v89, a255\n\t"
"v_cvt_pk_bf16_f32 v96, v86, v87\n\t"
"v_cvt_pk_bf16_f32 v97, v88, v89\n\t"
"buffer_store_dwordx2 v[96:97], v30, s[44:47], s84 offen offset:3072 sc1\n\t"
"LEND%=:\n\t"
      : "+{v0}"(tid)
      : "s"(blockIdx.x), "s"(__builtin_amdgcn_kernarg_segment_ptr()), "s"((u32)(size_t)lds)
      : "memory", "scc", "vcc", "s4","s5","s6","s7","s8","s9","s10","s11","s12","s13","s14","s15","s16","s17","s18","s19","s20","s21","s22","s23","s24","s25","s26","s27","s28","s29","s30","s31","s33","s34","s35","s36","s37","s38","s39","s40","s41","s42","s43","s44","s45","s46","s47","s48","s49","s50","s51","s52","s53","s54","s55","s56","s57","s58","s59","s60","s61","s62","s63","s64","s65","s66","s67","s68","s69","s70","s71","s72","s73","s74","s75","s76","s77","s78","s79","s80","s81","s82","s83","s84","s85","s86","s87","s88","s89","s90","s91","s92","s93","s94","s95","s96","s97","s98","s99", "v1","v2","v3","v4","v5","v6","v7","v8","v9","v10","v11","v12","v13","v14","v15","v16","v17","v18","v19","v20","v21","v22","v23","v24","v25","v26","v27","v28","v29","v30","v31","v32","v33","v34","v35","v36","v37","v38","v39","v40","v41","v42","v43","v44","v45","v46","v47","v48","v49","v50","v51","v52","v53","v54","v55","v56","v57","v58","v59","v60","v61","v62","v63","v64","v65","v66","v67","v68","v69","v70","v71","v72","v73","v74","v75","v76","v77","v78","v79","v80","v81","v82","v83","v84","v85","v86","v87","v88","v89","v90","v91","v92","v93","v94","v95","v96","v97","v98","v99","v100","v101","v102","v103","v104","v105","v106","v107","v108","v109","v110","v111","v112","v113","v114","v115","v116","v117","v118","v119","v120","v121","v122","v123","v124","v125","v126","v127","v128","v129","v130","v131","v132","v133","v134","v135","v136","v137","v138","v139","v140","v141","v142","v143","v144","v145","v146","v147","v148","v149","v150","v151","v152","v153","v154","v155","v156","v157","v158","v159","v160","v161","v162","v163","v164","v165","v166","v167","v168","v169","v170","v171","v172","v173","v174","v175","v176","v177","v178","v179","v180","v181","v182","v183","v184","v185","v186","v187","v188","v189","v190","v191","v192","v193","v194","v195","v196","v197","v198","v199","v200","v201","v202","v203","v204","v205","v206","v207","v208","v209","v210","v211","v212","v213","v214","v215","v216","v217","v218","v219","v220","v221","v222","v223","v224","v225","v226","v227","v228","v229","v230","v231","v232","v233","v234","v235","v236","v237","v238","v239","v240","v241","v242","v243","v244","v245","v246","v247","v248","v249","v250","v251","v252","v253","v254","v255", "a0","a1","a2","a3","a4","a5","a6","a7","a8","a9","a10","a11","a12","a13","a14","a15","a16","a17","a18","a19","a20","a21","a22","a23","a24","a25","a26","a27","a28","a29","a30","a31","a32","a33","a34","a35","a36","a37","a38","a39","a40","a41","a42","a43","a44","a45","a46","a47","a48","a49","a50","a51","a52","a53","a54","a55","a56","a57","a58","a59","a60","a61","a62","a63","a64","a65","a66","a67","a68","a69","a70","a71","a72","a73","a74","a75","a76","a77","a78","a79","a80","a81","a82","a83","a84","a85","a86","a87","a88","a89","a90","a91","a92","a93","a94","a95","a96","a97","a98","a99","a100","a101","a102","a103","a104","a105","a106","a107","a108","a109","a110","a111","a112","a113","a114","a115","a116","a117","a118","a119","a120","a121","a122","a123","a124","a125","a126","a127","a128","a129","a130","a131","a132","a133","a134","a135","a136","a137","a138","a139","a140","a141","a142","a143","a144","a145","a146","a147","a148","a149","a150","a151","a152","a153","a154","a155","a156","a157","a158","a159","a160","a161","a162","a163","a164","a165","a166","a167","a168","a169","a170","a171","a172","a173","a174","a175","a176","a177","a178","a179","a180","a181","a182","a183","a184","a185","a186","a187","a188","a189","a190","a191","a192","a193","a194","a195","a196","a197","a198","a199","a200","a201","a202","a203","a204","a205","a206","a207","a208","a209","a210","a211","a212","a213","a214","a215","a216","a217","a218","a219","a220","a221","a222","a223","a224","a225","a226","a227","a228","a229","a230","a231","a232","a233","a234","a235","a236","a237","a238","a239","a240","a241","a242","a243","a244","a245","a246","a247","a248","a249","a250","a251","a252","a253","a254","a255");
}

__global__ __launch_bounds__(576) void k2(const u16* __restrict__ x, const u16* __restrict__ gw, const char* __restrict__ ws, u16* __restrict__ out) {
  __shared__ float red[72];
  const u32 i = threadIdx.x, l = i & 63, lam = i >> 6, t0 = ((blockIdx.x & 7) * 32 + (blockIdx.x >> 3)) * 8;
  const u32 tk = l & 7, gm = (l >> 3) & 3, sl = l >> 5;
  const u32 rt = t0 >> 8, w = (t0 >> 6) & 3, tt = (t0 >> 4) & 3, ln = gm * 16 + (t0 & 8) + tk;
  const char* pb = ws + WS_PART + (rt * 32 + w) * 73728u + (2 * lam + sl) * 4096u + tt * 1024u;
  u32x4 p[8];
#pragma unroll
  for (int s = 0; s < 8; ++s) {
    if (lam < 8) p[s] = *(const u32x4*)(pb + s * 294912u + ln * 16u);
    else {
      const u32x2 a = *(const u32x2*)(pb + s * 294912u + ln * 8u), c = *(const u32x2*)(pb + s * 294912u + 512u + ln * 8u);
      p[s] = u32x4{a[0], a[1], c[0], c[1]};
    }
  }
  const u32x4 xv = *(const u32x4*)(x + (size_t)t0 * 576 + i * 8), gv = *(const u32x4*)(gw + (i % 72) * 8);
  const u32 d0 = 64 * lam + 32 * sl + 4 * gm, t = t0 + tk;
  const u16* at = (const u16*)(ws + WS_AT);
  float a[8];
#pragma unroll
  for (int e = 0; e < 8; ++e) a[e] = 1 ? bflo(at[(d0 + (e & 3) + 16 * (e >> 2)) * (ROW / 2) + t]) : 0.f;
  float z = 0.f;
#pragma unroll
  for (int e = 0; e < 4; ++e) z += bflo(xv[e]) * bflo(gv[e]) + bfhi(xv[e]) * bfhi(gv[e]);
  z += __shfl_xor(z, 1); z += __shfl_xor(z, 2); z += __shfl_xor(z, 4);
  if ((i & 7) == 0) red[i >> 3] = z;
  __syncthreads();
  z = 0.f;
#pragma unroll
  for (int e = 0; e < 9; ++e) z += red[tk * 9 + e];
  const float gt = __builtin_amdgcn_rcpf(1.f + __builtin_amdgcn_exp2f(-1.44269504f * z));
  float o[8];
#pragma unroll
  for (int e = 0; e < 8; ++e) {
    float sum = 0.f;
#pragma unroll
    for (int s = 0; s < 8; ++s) sum += (e & 1) ? bfhi(p[s][e >> 1]) : bflo(p[s][e >> 1]);
    o[e] = gt * a[e] + (1.f - gt) * sum;
  }
  u16* op = out + (size_t)t * 576 + d0;
  *(u32x2*)op = u32x2{pk(o[0], o[1]), pk(o[2], o[3])};
  *(u32x2*)(op + 16) = u32x2{pk(o[4], o[5]), pk(o[6], o[7])};
}

extern "C" void kernel_launch(void* const* d_in, const int* in_sizes, int n_in, void* d_out, int out_size, void* d_ws, size_t ws_size,
                              hipStream_t stream) {
  k1<<<256, 256, 0, stream>>>((const u16*)d_in[0], (const u16*)d_in[1], (const u16*)d_in[2], (const u16*)d_in[3], (const u16*)d_in[4],
                              (const u16*)d_in[5], (const u16*)d_in[6], (const u16*)d_in[8], (const u16*)d_in[9], (const u16*)d_in[10], (char*)d_ws);
  k2<<<256, 576, 0, stream>>>((const u16*)d_in[0], (const u16*)d_in[11], (const char*)d_ws, (u16*)d_out);
}
```
